```python
import torch

def _softmax(x, axis=-1):
    return torch.softmax(x, dim=axis)
def _gelu(x):
    return torch.nn.functional.gelu(x, approximate="tanh")
def _silu(x):
    return torch.nn.functional.silu(x)
def _sigmoid(x):
    return torch.sigmoid(x)
def _layernorm(x, axis=-1, eps=1e-5):
    m = x.mean(dim=axis, keepdim=True)
    v = x.var(dim=axis, keepdim=True, unbiased=False)
    return (x - m) / torch.sqrt(v + eps)
def _scatter_add(tgt, idx, src, axis=0):
    return tgt.clone().index_add_(axis, idx, src)
def _segment_sum(src, seg, n):
    out = torch.zeros((n,) + src.shape[1:], dtype=src.dtype)
    return out.index_add_(0, seg, src)
def _pad(x, pw):
    flat = tuple(v for pair in reversed(pw) for v in pair)
    return torch.nn.functional.pad(x, flat)

kernel_name = 'concat_heads__mha_block_cached__gqa_block_cached__near_ridge'

def setup_inputs():

    _w = (lambda a: (lambda t: t.to(torch.bfloat16) if t.is_floating_point() else t)(torch.as_tensor(a)))
    def _A_setup():
        B, Sq, D = 16, 96, 512
        H, Sk, Dh = 32, 512, 128
        return {
            "x": _w((lambda *s: torch.randn(*s, dtype=torch.bfloat16) * 0.8)(B, Sq, D)),
            "k_cache": _w((lambda *s: torch.randn(*s, dtype=torch.bfloat16) * 0.8)(B, H, Sk, Dh)),
            "v_cache": _w((lambda *s: torch.randn(*s, dtype=torch.bfloat16) * 0.05)(B, H, Sk, Dh)),
            "wq": _w((lambda *s: torch.randn(*s, dtype=torch.bfloat16) * 0.8)(D, H * Dh) * (D ** -0.5)),
            "wo": _w((lambda *s: torch.randn(*s, dtype=torch.bfloat16) * 0.05)(H * Dh, D) * ((H * Dh) ** -0.5)),
        }
    _A_in = _A_setup()

    def _B_setup():
        B, Sq, D = 16, 96, 512
        Hq, Hkv, Sk, Dh = 32, 32, 512, 128
        assert Hq % Hkv == 0, (Hq, Hkv)
        return {
            "x": _w((lambda *s: torch.randn(*s, dtype=torch.bfloat16) * 0.8)(B, Sq, D)),
            "k_cache": _w((lambda *s: torch.randn(*s, dtype=torch.bfloat16) * 0.8)(B, Hkv, Sk, Dh)),
            "v_cache": _w((lambda *s: torch.randn(*s, dtype=torch.bfloat16) * 0.05)(B, Hkv, Sk, Dh)),
            "wq": _w((lambda *s: torch.randn(*s, dtype=torch.bfloat16) * 0.8)(D, Hq * Dh) * (D ** -0.5)),
            "wo": _w((lambda *s: torch.randn(*s, dtype=torch.bfloat16) * 0.05)(Hq * Dh, D) * ((Hq * Dh) ** -0.5)),
        }
    _B_in = _B_setup()

    return {
        "A_x": _A_in["x"],
        "A_k_cache": _A_in["k_cache"],
        "A_v_cache": _A_in["v_cache"],
        "A_wq": _A_in["wq"],
        "A_wo": _A_in["wo"],
        "B_x": _B_in["x"],
        "B_k_cache": _B_in["k_cache"],
        "B_v_cache": _B_in["v_cache"],
        "B_wq": _B_in["wq"],
        "B_wo": _B_in["wo"],
    }

def reference(A_x, A_k_cache, A_v_cache, A_wq, A_wo, B_x, B_k_cache, B_v_cache, B_wq, B_wo):
    def _A_ref(x, k_cache, v_cache, wq, wo):
        B, Sq, D = x.shape
        H, Dh = 32, 128
        q = (lambda x, *d: x.permute(*d))((x @ wq).reshape(B, Sq, H, Dh), 0, 2, 1, 3)
        scores = torch.einsum("bhqd,bhkd->bhqk", q, k_cache) * (Dh ** -0.5)
        p = _softmax(scores, axis=-1)
        o = torch.einsum("bhqk,bhkd->bhqd", p, v_cache)
        return (lambda x, *d: x.permute(*d))(o, 0, 2, 1, 3).reshape(B, Sq, H * Dh) @ wo

    def _B_ref(x, k_cache, v_cache, wq, wo):
        B, Sq, D = x.shape
        Hq, Dh = 32, 128
        Hkv = k_cache.shape[1]
        rep = Hq // Hkv
        q = (lambda x, *d: x.permute(*d))((x @ wq).reshape(B, Sq, Hq, Dh), 0, 2, 1, 3)
        k = (lambda x, n, axis: torch.repeat_interleave(x, n, dim=axis))(k_cache, rep, axis=1)
        v = (lambda x, n, axis: torch.repeat_interleave(x, n, dim=axis))(v_cache, rep, axis=1)
        scores = torch.einsum("bhqd,bhkd->bhqk", q, k) * (Dh ** -0.5)
        p = _softmax(scores, axis=-1)
        o = torch.einsum("bhqk,bhkd->bhqd", p, v)
        return (lambda x, *d: x.permute(*d))(o, 0, 2, 1, 3).reshape(B, Sq, Hq * Dh) @ wo

    a = _A_ref(A_x, A_k_cache, A_v_cache, A_wq, A_wo)
    b = _B_ref(B_x, B_k_cache, B_v_cache, B_wq, B_wo)
    return torch.concatenate([a, b], axis=-1)
```

```cpp
#include <hip/hip_runtime.h>
#include <hip/hip_bf16.h>
typedef __hip_bfloat16 bf16;
constexpr int B = 16, SQ = 96, D = 512, H = 32, SK = 512, DH = 128;

__device__ inline float b2f(bf16 v) { return __bfloat162float(v); }

__global__ void naive_q(const bf16* x, const bf16* wq, float* q) {
  int n = blockIdx.x * 256 + threadIdx.x;
  int row = blockIdx.y;
  float acc = 0.f;
  for (int c = 0; c < D; ++c) acc += b2f(x[row * D + c]) * b2f(wq[(size_t)c * (H * DH) + n]);
  q[(size_t)row * (H * DH) + n] = b2f(__float2bfloat16(acc));
}
__global__ void naive_attn(const float* q, const bf16* k, const bf16* v, float* o) {
  int s = blockIdx.x, h = blockIdx.y, b = blockIdx.z, t = threadIdx.x;
  __shared__ float qs[DH], p[SK], red[128];
  qs[t] = q[(size_t)(b * SQ + s) * (H * DH) + h * DH + t];
  __syncthreads();
  const bf16* kb = k + (size_t)(b * H + h) * SK * DH;
  const bf16* vb = v + (size_t)(b * H + h) * SK * DH;
  float mx = -1e30f;
  for (int j = t; j < SK; j += 128) {
    float a = 0.f;
    for (int d = 0; d < DH; ++d) a += qs[d] * b2f(kb[j * DH + d]);
    a *= 0.08838834764831845f;
    p[j] = a; mx = fmaxf(mx, a);
  }
  red[t] = mx; __syncthreads();
  for (int o2 = 64; o2 > 0; o2 >>= 1) { if (t < o2) red[t] = fmaxf(red[t], red[t + o2]); __syncthreads(); }
  mx = red[0]; __syncthreads();
  float sum = 0.f;
  for (int j = t; j < SK; j += 128) { float e = __expf(p[j] - mx); p[j] = e; sum += e; }
  red[t] = sum; __syncthreads();
  for (int o2 = 64; o2 > 0; o2 >>= 1) { if (t < o2) red[t] += red[t + o2]; __syncthreads(); }
  sum = red[0];
  float a = 0.f;
  for (int j = 0; j < SK; ++j) a += p[j] * b2f(vb[j * DH + t]);
  o[(size_t)(b * SQ + s) * (H * DH) + h * DH + t] = b2f(__float2bfloat16(a / sum));
}
__global__ void naive_out(const float* o, const bf16* wo, bf16* out, int coloff) {
  int n = blockIdx.x * 256 + threadIdx.x;
  int row = blockIdx.y;
  float acc = 0.f;
  for (int c = 0; c < H * DH; ++c) acc += o[(size_t)row * (H * DH) + c] * b2f(wo[(size_t)c * D + n]);
  out[(size_t)row * (2 * D) + coloff + n] = __float2bfloat16(acc);
}

extern "C" void kernel_launch(void* const* d_in, const int* in_sizes, int n_in, void* d_out, int out_size, void* d_ws,
                              size_t ws_size, hipStream_t stream) {
  float* q = (float*)d_ws;
  float* o = q + (size_t)B * SQ * H * DH;
  for (int br = 0; br < 2; ++br) {
    const bf16* x = (const bf16*)d_in[br * 5 + 0];
    const bf16* k = (const bf16*)d_in[br * 5 + 1];
    const bf16* v = (const bf16*)d_in[br * 5 + 2];
    const bf16* wq = (const bf16*)d_in[br * 5 + 3];
    const bf16* wo = (const bf16*)d_in[br * 5 + 4];
    naive_q<<<dim3(H * DH / 256, B * SQ), 256, 0, stream>>>(x, wq, q);
    naive_attn<<<dim3(SQ, H, B), 128, 0, stream>>>(q, k, v, o);
    naive_out<<<dim3(D / 256, B * SQ), 256, 0, stream>>>(o, wo, (bf16*)d_out, br * D);
  }
}
```

```cpp
#include <hip/hip_runtime.h>
#include <hip/hip_bf16.h>

namespace {

constexpr int kLdsBytes = 163840;
constexpr size_t kPartBytes = 98304;

__global__ __launch_bounds__(256, 1) void fused_attn_kernel(const char* xA, const char* kA, const char* vA, const char* wqA,
                                                            const char* woA, const char* xB, const char* kB, const char* vB,
                                                            const char* wqB, const char* woB, char* part) {
  const unsigned bid = blockIdx.x;
  const unsigned xcd = bid & 7u, idx = bid >> 3;
  const unsigned br = xcd >> 2, b = idx >> 1, g = (xcd & 3u) * 2u + (idx & 1u);
  const char* kp = (br ? kB : kA) + (size_t)b * (32u * 131072u);
  const char* vp = (br ? vB : vA) + (size_t)b * (32u * 131072u);
  const char* wq = br ? wqB : wqA;
  const char* wo = br ? woB : woA;
  const char* xp = (br ? xB : xA) + (size_t)b * 98304u;
  char* pp = part + (size_t)((br * 16u + b) * 8u + g) * kPartBytes;
  const unsigned wid = __builtin_amdgcn_readfirstlane(threadIdx.x >> 6);
  const unsigned h0 = g * 4u;
  asm volatile(
    "s_mov_b64 s[36:37], %0\n"
    "s_and_b32 s37, s37, 0xffff\n"
    "s_mov_b32 s38, 4194304\n"
    "s_mov_b32 s39, 0x20000\n"
    "s_mov_b64 s[40:41], %1\n"
    "s_and_b32 s41, s41, 0xffff\n"
    "s_mov_b32 s42, 4194304\n"
    "s_mov_b32 s43, 0x20000\n"
    "s_mov_b64 s[44:45], %2\n"
    "s_and_b32 s45, s45, 0xffff\n"
    "s_mov_b32 s46, 4194304\n"
    "s_mov_b32 s47, 0x20000\n"
    "s_mov_b64 s[48:49], %3\n"
    "s_and_b32 s49, s49, 0xffff\n"
    "s_mov_b32 s50, 4194304\n"
    "s_mov_b32 s51, 0x20000\n"
    "s_mov_b64 s[52:53], %4\n"
    "s_and_b32 s53, s53, 0xffff\n"
    "s_mov_b32 s54, 98304\n"
    "s_mov_b32 s55, 0x20000\n"
    "s_mov_b64 s[56:57], %5\n"
    "s_and_b32 s57, s57, 0xffff\n"
    "s_mov_b32 s58, 98304\n"
    "s_mov_b32 s59, 0x20000\n"
    "s_mov_b32 s60, %6\n"
    "s_mov_b32 s78, %7\n"
    "s_lshl_b32 s61, s60, 12\n"
    "s_lshl_b32 s66, s78, 17\n"
    "s_add_u32 s62, s66, s61\n"
    "s_add_u32 s63, s62, 131072\n"
    "s_lshr_b32 s67, s60, 1\n"
    "s_lshl_b32 s67, s67, 13\n"
    "s_and_b32 s74, s60, 1\n"
    "s_lshl_b32 s74, s74, 9\n"
    "s_add_u32 s74, s74, s67\n"
    "s_mov_b32 s65, s66\n"
    "s_mov_b32 s64, 268435456\n"
    "s_mov_b32 s68, 4\n"
    "s_mov_b32 s69, 0\n"
    "s_mov_b32 s72, 1040356334\n"
    "s_mov_b32 s73, 1040356334\n"
    "s_lshl_b32 s67, s78, 8\n"
    "s_add_u32 s75, s67, 256\n"
    "s_add_u32 s76, s67, 512\n"
    "s_lshl_b32 s79, s60, 17\n"
    "s_add_u32 s79, s79, s67\n"
    "s_lshl_b32 s80, s60, 13\n"
    "s_lshl_b32 s82, s60, 10\n"
    "s_add_u32 s81, s78, 3\n"
    "s_lshl_b32 s81, s81, 17\n"
    "s_lshl_b32 s77, s60, 15\n"
    "s_lshl_b32 s83, %8, 18\n"
    "s_lshl_b32 s84, %8, 13\n"
    "s_mov_b32 s90, 0\n"
    "s_cmp_eq_u32 s60, 3\n"
    "s_cbranch_scc1 L_w3_entry\n"
    "v_mbcnt_lo_u32_b32 v0, -1, 0\n"
    "v_mbcnt_hi_u32_b32 v0, -1, v0\n"
    "v_and_b32 v1, 31, v0\n"
    "v_lshrrev_b32 v2, 5, v0\n"
    "v_bfe_u32 v3, v0, 2, 3\n"
    "v_and_b32 v4, 3, v0\n"
    "v_bfe_u32 v5, v0, 2, 2\n"
    "v_bfe_u32 v6, v0, 4, 1\n"
    "v_bfe_u32 v7, v0, 2, 2\n"
    "v_lshrrev_b32 v8, 3, v1\n"
    "v_and_b32 v9, 7, v1\n"
    "v_xor_b32 v10, v2, v7\n"
    "v_xor_b32 v11, 2, v10\n"
    "v_lshrrev_b32 v12, 2, v3\n"
    "v_xor_b32 v12, v12, v4\n"
    "v_xor_b32 v13, 2, v12\n"
    "v_lshlrev_b32 v14, 6, v2\n"
    "v_and_b32 v15, 1, v4\n"
    "v_lshrrev_b32 v16, 1, v4\n"
    "v_lshlrev_b32 v243, 8, v3\n"
    "v_lshl_add_u32 v243, v14, 0, v243\n"
    "v_lshl_add_u32 v243, v12, 4, v243\n"
    "v_lshlrev_b32 v244, 8, v3\n"
    "v_lshl_add_u32 v244, v14, 0, v244\n"
    "v_lshl_add_u32 v244, v13, 4, v244\n"
    "v_lshlrev_b32 v245, 8, v3\n"
    "v_lshl_add_u32 v245, v14, 0, v245\n"
    "v_lshl_add_u32 v245, v4, 4, v245\n"
    "v_lshlrev_b32 v246, 10, v3\n"
    "v_lshl_add_u32 v246, v14, 0, v246\n"
    "v_lshl_add_u32 v246, v4, 4, v246\n"
    "v_lshlrev_b32 v240, 11, v8\n"
    "v_lshl_add_u32 v240, v9, 6, v240\n"
    "v_lshl_add_u32 v240, v10, 4, v240\n"
    "v_lshlrev_b32 v241, 11, v8\n"
    "v_lshl_add_u32 v241, v9, 6, v241\n"
    "v_lshl_add_u32 v241, v11, 4, v241\n"
    "v_lshlrev_b32 v242, 8, v2\n"
    "v_lshl_add_u32 v242, v5, 6, v242\n"
    "v_lshl_add_u32 v242, v6, 5, v242\n"
    "v_lshl_add_u32 v242, v4, 3, v242\n"
    "v_add_u32 v242, 32768, v242\n"
    "v_lshlrev_b32 v254, 4, v0\n"
    "v_add_u32 v254, 131072, v254\n"
    "v_mov_b32 v17, s60\n"
    "v_lshl_add_u32 v254, v17, 13, v254\n"
    "v_lshlrev_b32 v224, 11, v2\n"
    "v_lshl_add_u32 v224, v5, 6, v224\n"
    "v_lshl_add_u32 v224, v6, 5, v224\n"
    "v_lshl_add_u32 v224, v15, 4, v224\n"
    "v_lshl_add_u32 v224, v16, 3, v224\n"
    "v_add_u32 v229, 65536, v224\n"
    "v_add_u32 v230, 131072, v224\n"
    "v_lshlrev_b32 v225, 13, v3\n"
    "v_lshl_add_u32 v225, v14, 0, v225\n"
    "v_lshl_add_u32 v225, v4, 4, v225\n"
    "s_and_b32 s66, s60, 1\n"
    "s_lshl_b32 s66, s66, 1\n"
    "v_xor_b32 v18, s66, v12\n"
    "v_lshlrev_b32 v226, 10, v3\n"
    "v_lshl_add_u32 v226, v14, 0, v226\n"
    "v_lshl_add_u32 v226, v18, 4, v226\n"
    "v_lshlrev_b32 v227, 10, v8\n"
    "v_lshl_add_u32 v227, v9, 6, v227\n"
    "v_lshl_add_u32 v227, v10, 4, v227\n"
    "v_lshlrev_b32 v228, 10, v8\n"
    "v_lshl_add_u32 v228, v9, 6, v228\n"
    "v_lshl_add_u32 v228, v11, 4, v228\n"
    "s_lshl_b32 s66, s60, 12\n"
    "s_add_u32 s66, s66, 32768\n"
    "v_add_u32 v227, s66, v227\n"
    "v_add_u32 v228, s66, v228\n"
    "v_add_u32 v231, 49152, v227\n"
    "v_add_u32 v232, 49152, v228\n"
    "s_add_u32 m0, s82, 32768\n"
    "s_add_u32 s66, s80, 0\n"
    "buffer_load_dwordx4 v226, s[52:55], s66 offen lds\n"
    "s_add_u32 m0, s82, 36864\n"
    "s_add_u32 s66, s80, 32768\n"
    "buffer_load_dwordx4 v226, s[52:55], s66 offen lds\n"
    "s_add_u32 m0, s82, 40960\n"
    "s_add_u32 s66, s80, 65536\n"
    "buffer_load_dwordx4 v226, s[52:55], s66 offen lds\n"
    "s_add_u32 m0, s61, 131072\n"
    "s_add_u32 s66, s79, 0\n"
    "buffer_load_dwordx4 v225, s[44:47], s66 offen lds\n"
    "s_add_u32 m0, s61, 131968\n"
    "s_nop 0\n"
    "buffer_load_dwordx4 v225, s[44:47], s66 offen offset:128 lds\n"
    "s_add_u32 m0, s61, 133120\n"
    "s_add_u32 s66, s79, 65536\n"
    "buffer_load_dwordx4 v225, s[44:47], s66 offen lds\n"
    "s_add_u32 m0, s61, 134016\n"
    "s_nop 0\n"
    "buffer_load_dwordx4 v225, s[44:47], s66 offen offset:128 lds\n"
    "s_add_u32 m0, s82, 44928\n"
    "s_add_u32 s66, s80, 0\n"
    "buffer_load_dwordx4 v226, s[52:55], s66 offen offset:128 lds\n"
    "s_add_u32 m0, s82, 49024\n"
    "s_add_u32 s66, s80, 32768\n"
    "buffer_load_dwordx4 v226, s[52:55], s66 offen offset:128 lds\n"
    "s_add_u32 m0, s82, 53120\n"
    "s_add_u32 s66, s80, 65536\n"
    "buffer_load_dwordx4 v226, s[52:55], s66 offen offset:128 lds\n"
    "s_add_u32 m0, s61, 147456\n"
    "s_add_u32 s66, s79, 524288\n"
    "buffer_load_dwordx4 v225, s[44:47], s66 offen lds\n"
    "s_add_u32 m0, s61, 148352\n"
    "s_nop 0\n"
    "buffer_load_dwordx4 v225, s[44:47], s66 offen offset:128 lds\n"
    "s_add_u32 m0, s61, 149504\n"
    "s_add_u32 s66, s79, 589824\n"
    "buffer_load_dwordx4 v225, s[44:47], s66 offen lds\n"
    "s_add_u32 m0, s61, 150400\n"
    "s_nop 0\n"
    "buffer_load_dwordx4 v225, s[44:47], s66 offen offset:128 lds\n"
    "s_add_u32 m0, s82, 57088\n"
    "s_add_u32 s66, s80, 0\n"
    "buffer_load_dwordx4 v226, s[52:55], s66 offen offset:256 lds\n"
    "s_add_u32 m0, s82, 61184\n"
    "s_add_u32 s66, s80, 32768\n"
    "buffer_load_dwordx4 v226, s[52:55], s66 offen offset:256 lds\n"
    "s_add_u32 m0, s82, 65280\n"
    "s_add_u32 s66, s80, 65536\n"
    "buffer_load_dwordx4 v226, s[52:55], s66 offen offset:256 lds\n"
    "s_add_u32 m0, s61, 0\n"
    "s_add_u32 s66, s79, 1048576\n"
    "buffer_load_dwordx4 v225, s[44:47], s66 offen lds\n"
    "s_add_u32 m0, s61, 896\n"
    "s_nop 0\n"
    "buffer_load_dwordx4 v225, s[44:47], s66 offen offset:128 lds\n"
    "s_add_u32 m0, s61, 2048\n"
    "s_add_u32 s66, s79, 1114112\n"
    "buffer_load_dwordx4 v225, s[44:47], s66 offen lds\n"
    "s_add_u32 m0, s61, 2944\n"
    "s_nop 0\n"
    "buffer_load_dwordx4 v225, s[44:47], s66 offen offset:128 lds\n"
    "s_add_u32 m0, s82, 69248\n"
    "s_add_u32 s66, s80, 0\n"
    "buffer_load_dwordx4 v226, s[52:55], s66 offen offset:384 lds\n"
    "s_add_u32 m0, s82, 73344\n"
    "s_add_u32 s66, s80, 32768\n"
    "buffer_load_dwordx4 v226, s[52:55], s66 offen offset:384 lds\n"
    "s_add_u32 m0, s82, 77440\n"
    "s_add_u32 s66, s80, 65536\n"
    "buffer_load_dwordx4 v226, s[52:55], s66 offen offset:384 lds\n"
    "s_add_u32 m0, s61, 16384\n"
    "s_add_u32 s66, s79, 1572864\n"
    "buffer_load_dwordx4 v225, s[44:47], s66 offen lds\n"
    "s_add_u32 m0, s61, 17280\n"
    "s_nop 0\n"
    "buffer_load_dwordx4 v225, s[44:47], s66 offen offset:128 lds\n"
    "s_add_u32 m0, s61, 18432\n"
    "s_add_u32 s66, s79, 1638400\n"
    "buffer_load_dwordx4 v225, s[44:47], s66 offen lds\n"
    "s_add_u32 m0, s61, 19328\n"
    "s_nop 0\n"
    "buffer_load_dwordx4 v225, s[44:47], s66 offen offset:128 lds\n"
    "s_add_u32 m0, s82, 81408\n"
    "s_add_u32 s66, s80, 0\n"
    "buffer_load_dwordx4 v226, s[52:55], s66 offen offset:512 lds\n"
    "s_add_u32 m0, s82, 85504\n"
    "s_add_u32 s66, s80, 32768\n"
    "buffer_load_dwordx4 v226, s[52:55], s66 offen offset:512 lds\n"
    "s_add_u32 m0, s82, 89600\n"
    "s_add_u32 s66, s80, 65536\n"
    "buffer_load_dwordx4 v226, s[52:55], s66 offen offset:512 lds\n"
    "s_add_u32 m0, s82, 93568\n"
    "s_add_u32 s66, s80, 0\n"
    "buffer_load_dwordx4 v226, s[52:55], s66 offen offset:640 lds\n"
    "s_add_u32 m0, s82, 97664\n"
    "s_add_u32 s66, s80, 32768\n"
    "buffer_load_dwordx4 v226, s[52:55], s66 offen offset:640 lds\n"
    "s_add_u32 m0, s82, 101760\n"
    "s_add_u32 s66, s80, 65536\n"
    "buffer_load_dwordx4 v226, s[52:55], s66 offen offset:640 lds\n"
    "s_add_u32 m0, s82, 105728\n"
    "s_add_u32 s66, s80, 0\n"
    "buffer_load_dwordx4 v226, s[52:55], s66 offen offset:768 lds\n"
    "s_add_u32 m0, s82, 109824\n"
    "s_add_u32 s66, s80, 32768\n"
    "buffer_load_dwordx4 v226, s[52:55], s66 offen offset:768 lds\n"
    "s_add_u32 m0, s82, 113920\n"
    "s_add_u32 s66, s80, 65536\n"
    "buffer_load_dwordx4 v226, s[52:55], s66 offen offset:768 lds\n"
    "s_add_u32 m0, s82, 117888\n"
    "s_add_u32 s66, s80, 0\n"
    "buffer_load_dwordx4 v226, s[52:55], s66 offen offset:896 lds\n"
    "s_add_u32 m0, s82, 121984\n"
    "s_add_u32 s66, s80, 32768\n"
    "buffer_load_dwordx4 v226, s[52:55], s66 offen offset:896 lds\n"
    "s_add_u32 m0, s82, 126080\n"
    "s_add_u32 s66, s80, 65536\n"
    "buffer_load_dwordx4 v226, s[52:55], s66 offen offset:896 lds\n"
    "s_waitcnt vmcnt(37)\n"
    "s_waitcnt vmcnt(33)\n"
    "s_barrier\n"
    "ds_read_b128 v[64:67], v227 offset:0\n"
    "ds_read_b128 v[68:71], v228 offset:0\n"
    "ds_read_b128 v[72:75], v227 offset:512\n"
    "ds_read_b128 v[76:79], v228 offset:512\n"
    "ds_read_b64_tr_b16 v[192:193], v230 offset:0\n"
    "ds_read_b64_tr_b16 v[194:195], v230 offset:256\n"
    "ds_read_b64_tr_b16 v[196:197], v230 offset:512\n"
    "ds_read_b64_tr_b16 v[198:199], v230 offset:768\n"
    "ds_read_b64_tr_b16 v[200:201], v230 offset:1024\n"
    "ds_read_b64_tr_b16 v[202:203], v230 offset:1280\n"
    "ds_read_b64_tr_b16 v[204:205], v230 offset:1536\n"
    "ds_read_b64_tr_b16 v[206:207], v230 offset:1792\n"
    "ds_read_b64_tr_b16 v[208:209], v230 offset:4096\n"
    "ds_read_b64_tr_b16 v[210:211], v230 offset:4352\n"
    "ds_read_b64_tr_b16 v[212:213], v230 offset:4608\n"
    "s_waitcnt lgkmcnt(14)\n"
    "ds_read_b64_tr_b16 v[214:215], v230 offset:4864\n"
    "s_waitcnt lgkmcnt(8)\n"
    "v_mfma_f32_32x32x16_bf16 v[0:15], v[192:195], v[64:67], 0\n"
    "ds_read_b64_tr_b16 v[216:217], v230 offset:5120\n"
    "ds_read_b64_tr_b16 v[218:219], v230 offset:5376\n"
    "v_mfma_f32_32x32x16_bf16 v[16:31], v[196:199], v[64:67], 0\n"
    "ds_read_b64_tr_b16 v[220:221], v230 offset:5632\n"
    "ds_read_b64_tr_b16 v[222:223], v230 offset:5888\n"
    "s_waitcnt lgkmcnt(8)\n"
    "v_mfma_f32_32x32x16_bf16 v[32:47], v[200:203], v[64:67], 0\n"
    "ds_read_b64_tr_b16 v[192:193], v230 offset:8192\n"
    "ds_read_b64_tr_b16 v[194:195], v230 offset:8448\n"
    "v_mfma_f32_32x32x16_bf16 v[48:63], v[204:207], v[64:67], 0\n"
    "ds_read_b64_tr_b16 v[196:197], v230 offset:8704\n"
    "ds_read_b64_tr_b16 v[198:199], v230 offset:8960\n"
    "s_waitcnt lgkmcnt(8)\n"
    "v_mfma_f32_32x32x16_bf16 v[0:15], v[208:211], v[68:71], v[0:15]\n"
    "ds_read_b64_tr_b16 v[200:201], v230 offset:9216\n"
    "ds_read_b64_tr_b16 v[202:203], v230 offset:9472\n"
    "v_mfma_f32_32x32x16_bf16 v[16:31], v[212:215], v[68:71], v[16:31]\n"
    "ds_read_b64_tr_b16 v[204:205], v230 offset:9728\n"
    "ds_read_b64_tr_b16 v[206:207], v230 offset:9984\n"
    "s_waitcnt lgkmcnt(8)\n"
    "v_mfma_f32_32x32x16_bf16 v[32:47], v[216:219], v[68:71], v[32:47]\n"
    "ds_read_b64_tr_b16 v[208:209], v230 offset:12288\n"
    "ds_read_b64_tr_b16 v[210:211], v230 offset:12544\n"
    "v_mfma_f32_32x32x16_bf16 v[48:63], v[220:223], v[68:71], v[48:63]\n"
    "ds_read_b64_tr_b16 v[212:213], v230 offset:12800\n"
    "ds_read_b64_tr_b16 v[214:215], v230 offset:13056\n"
    "s_waitcnt lgkmcnt(8)\n"
    "v_mfma_f32_32x32x16_bf16 v[0:15], v[192:195], v[72:75], v[0:15]\n"
    "ds_read_b64_tr_b16 v[216:217], v230 offset:13312\n"
    "ds_read_b64_tr_b16 v[218:219], v230 offset:13568\n"
    "v_mfma_f32_32x32x16_bf16 v[16:31], v[196:199], v[72:75], v[16:31]\n"
    "ds_read_b64_tr_b16 v[220:221], v230 offset:13824\n"
    "ds_read_b64_tr_b16 v[222:223], v230 offset:14080\n"
    "s_waitcnt lgkmcnt(8)\n"
    "v_mfma_f32_32x32x16_bf16 v[32:47], v[200:203], v[72:75], v[32:47]\n"
    "s_waitcnt vmcnt(26)\n"
    "s_barrier\n"
    "ds_read_b64_tr_b16 v[192:193], v230 offset:16384\n"
    "ds_read_b64_tr_b16 v[194:195], v230 offset:16640\n"
    "ds_read_b128 v[80:83], v227 offset:12288\n"
    "v_mfma_f32_32x32x16_bf16 v[48:63], v[204:207], v[72:75], v[48:63]\n"
    "ds_read_b64_tr_b16 v[196:197], v230 offset:16896\n"
    "ds_read_b64_tr_b16 v[198:199], v230 offset:17152\n"
    "ds_read_b128 v[84:87], v228 offset:12288\n"
    "s_waitcnt lgkmcnt(10)\n"
    "v_mfma_f32_32x32x16_bf16 v[0:15], v[208:211], v[76:79], v[0:15]\n"
    "ds_read_b64_tr_b16 v[200:201], v230 offset:17408\n"
    "ds_read_b64_tr_b16 v[202:203], v230 offset:17664\n"
    "ds_read_b128 v[88:91], v227 offset:12800\n"
    "v_mfma_f32_32x32x16_bf16 v[16:31], v[212:215], v[76:79], v[16:31]\n"
    "ds_read_b64_tr_b16 v[204:205], v230 offset:17920\n"
    "ds_read_b64_tr_b16 v[206:207], v230 offset:18176\n"
    "s_waitcnt lgkmcnt(14)\n"
    "ds_read_b128 v[92:95], v228 offset:12800\n"
    "s_waitcnt lgkmcnt(12)\n"
    "v_mfma_f32_32x32x16_bf16 v[32:47], v[216:219], v[76:79], v[32:47]\n"
    "ds_read_b64_tr_b16 v[208:209], v230 offset:20480\n"
    "ds_read_b64_tr_b16 v[210:211], v230 offset:20736\n"
    "v_mfma_f32_32x32x16_bf16 v[48:63], v[220:223], v[76:79], v[48:63]\n"
    "ds_read_b64_tr_b16 v[212:213], v230 offset:20992\n"
    "s_waitcnt lgkmcnt(14)\n"
    "ds_read_b64_tr_b16 v[214:215], v230 offset:21248\n"
    "s_waitcnt lgkmcnt(11)\n"
    "v_mfma_f32_32x32x16_bf16 v[0:15], v[192:195], v[80:83], v[0:15]\n"
    "ds_read_b64_tr_b16 v[216:217], v230 offset:21504\n"
    "ds_read_b64_tr_b16 v[218:219], v230 offset:21760\n"
    "v_mfma_f32_32x32x16_bf16 v[16:31], v[196:199], v[80:83], v[16:31]\n"
    "ds_read_b64_tr_b16 v[220:221], v230 offset:22016\n"
    "ds_read_b64_tr_b16 v[222:223], v230 offset:22272\n"
    "s_waitcnt lgkmcnt(9)\n"
    "v_mfma_f32_32x32x16_bf16 v[32:47], v[200:203], v[80:83], v[32:47]\n"
    "ds_read_b64_tr_b16 v[192:193], v230 offset:24576\n"
    "ds_read_b64_tr_b16 v[194:195], v230 offset:24832\n"
    "v_mfma_f32_32x32x16_bf16 v[48:63], v[204:207], v[80:83], v[48:63]\n"
    "ds_read_b64_tr_b16 v[196:197], v230 offset:25088\n"
    "ds_read_b64_tr_b16 v[198:199], v230 offset:25344\n"
    "s_waitcnt lgkmcnt(8)\n"
    "v_mfma_f32_32x32x16_bf16 v[0:15], v[208:211], v[84:87], v[0:15]\n"
    "ds_read_b64_tr_b16 v[200:201], v230 offset:25600\n"
    "ds_read_b64_tr_b16 v[202:203], v230 offset:25856\n"
    "v_mfma_f32_32x32x16_bf16 v[16:31], v[212:215], v[84:87], v[16:31]\n"
    "ds_read_b64_tr_b16 v[204:205], v230 offset:26112\n"
    "ds_read_b64_tr_b16 v[206:207], v230 offset:26368\n"
    "s_waitcnt lgkmcnt(8)\n"
    "v_mfma_f32_32x32x16_bf16 v[32:47], v[216:219], v[84:87], v[32:47]\n"
    "ds_read_b64_tr_b16 v[208:209], v230 offset:28672\n"
    "ds_read_b64_tr_b16 v[210:211], v230 offset:28928\n"
    "v_mfma_f32_32x32x16_bf16 v[48:63], v[220:223], v[84:87], v[48:63]\n"
    "ds_read_b64_tr_b16 v[212:213], v230 offset:29184\n"
    "ds_read_b64_tr_b16 v[214:215], v230 offset:29440\n"
    "s_waitcnt lgkmcnt(8)\n"
    "v_mfma_f32_32x32x16_bf16 v[0:15], v[192:195], v[88:91], v[0:15]\n"
    "ds_read_b64_tr_b16 v[216:217], v230 offset:29696\n"
    "ds_read_b64_tr_b16 v[218:219], v230 offset:29952\n"
    "v_mfma_f32_32x32x16_bf16 v[16:31], v[196:199], v[88:91], v[16:31]\n"
    "ds_read_b64_tr_b16 v[220:221], v230 offset:30208\n"
    "ds_read_b64_tr_b16 v[222:223], v230 offset:30464\n"
    "s_waitcnt lgkmcnt(8)\n"
    "v_mfma_f32_32x32x16_bf16 v[32:47], v[200:203], v[88:91], v[32:47]\n"
    "s_waitcnt vmcnt(19)\n"
    "s_barrier\n"
    "ds_read_b64_tr_b16 v[192:193], v224 offset:0\n"
    "ds_read_b64_tr_b16 v[194:195], v224 offset:256\n"
    "ds_read_b128 v[64:67], v227 offset:24576\n"
    "v_mfma_f32_32x32x16_bf16 v[48:63], v[204:207], v[88:91], v[48:63]\n"
    "ds_read_b64_tr_b16 v[196:197], v224 offset:512\n"
    "ds_read_b64_tr_b16 v[198:199], v224 offset:768\n"
    "ds_read_b128 v[68:71], v228 offset:24576\n"
    "s_add_u32 m0, s61, 131072\n"
    "s_add_u32 s66, s79, 2097152\n"
    "buffer_load_dwordx4 v225, s[44:47], s66 offen lds\n"
    "s_waitcnt lgkmcnt(10)\n"
    "v_mfma_f32_32x32x16_bf16 v[0:15], v[208:211], v[92:95], v[0:15]\n"
    "ds_read_b64_tr_b16 v[200:201], v224 offset:1024\n"
    "ds_read_b64_tr_b16 v[202:203], v224 offset:1280\n"
    "ds_read_b128 v[72:75], v227 offset:25088\n"
    "s_add_u32 m0, s61, 131968\n"
    "s_nop 0\n"
    "buffer_load_dwordx4 v225, s[44:47], s66 offen offset:128 lds\n"
    "s_add_u32 m0, s61, 133120\n"
    "s_add_u32 s66, s79, 2162688\n"
    "buffer_load_dwordx4 v225, s[44:47], s66 offen lds\n"
    "v_mfma_f32_32x32x16_bf16 v[16:31], v[212:215], v[92:95], v[16:31]\n"
    "ds_read_b64_tr_b16 v[204:205], v224 offset:1536\n"
    "ds_read_b64_tr_b16 v[206:207], v224 offset:1792\n"
    "s_waitcnt lgkmcnt(14)\n"
    "ds_read_b128 v[76:79], v228 offset:25088\n"
    "s_add_u32 m0, s61, 134016\n"
    "s_nop 0\n"
    "buffer_load_dwordx4 v225, s[44:47], s66 offen offset:128 lds\n"
    "s_waitcnt lgkmcnt(12)\n"
    "v_mfma_f32_32x32x16_bf16 v[32:47], v[216:219], v[92:95], v[32:47]\n"
    "ds_read_b64_tr_b16 v[208:209], v224 offset:4096\n"
    "ds_read_b64_tr_b16 v[210:211], v224 offset:4352\n"
    "v_mfma_f32_32x32x16_bf16 v[48:63], v[220:223], v[92:95], v[48:63]\n"
    "ds_read_b64_tr_b16 v[212:213], v224 offset:4608\n"
    "s_waitcnt lgkmcnt(14)\n"
    "ds_read_b64_tr_b16 v[214:215], v224 offset:4864\n"
    "s_waitcnt lgkmcnt(11)\n"
    "v_mfma_f32_32x32x16_bf16 v[0:15], v[192:195], v[64:67], v[0:15]\n"
    "ds_read_b64_tr_b16 v[216:217], v224 offset:5120\n"
    "ds_read_b64_tr_b16 v[218:219], v224 offset:5376\n"
    "v_mfma_f32_32x32x16_bf16 v[16:31], v[196:199], v[64:67], v[16:31]\n"
    "ds_read_b64_tr_b16 v[220:221], v224 offset:5632\n"
    "ds_read_b64_tr_b16 v[222:223], v224 offset:5888\n"
    "s_waitcnt lgkmcnt(9)\n"
    "v_mfma_f32_32x32x16_bf16 v[32:47], v[200:203], v[64:67], v[32:47]\n"
    "ds_read_b64_tr_b16 v[192:193], v224 offset:8192\n"
    "ds_read_b64_tr_b16 v[194:195], v224 offset:8448\n"
    "v_mfma_f32_32x32x16_bf16 v[48:63], v[204:207], v[64:67], v[48:63]\n"
    "ds_read_b64_tr_b16 v[196:197], v224 offset:8704\n"
    "ds_read_b64_tr_b16 v[198:199], v224 offset:8960\n"
    "s_waitcnt lgkmcnt(8)\n"
    "v_mfma_f32_32x32x16_bf16 v[0:15], v[208:211], v[68:71], v[0:15]\n"
    "ds_read_b64_tr_b16 v[200:201], v224 offset:9216\n"
    "ds_read_b64_tr_b16 v[202:203], v224 offset:9472\n"
    "v_mfma_f32_32x32x16_bf16 v[16:31], v[212:215], v[68:71], v[16:31]\n"
    "ds_read_b64_tr_b16 v[204:205], v224 offset:9728\n"
    "ds_read_b64_tr_b16 v[206:207], v224 offset:9984\n"
    "s_waitcnt lgkmcnt(8)\n"
    "v_mfma_f32_32x32x16_bf16 v[32:47], v[216:219], v[68:71], v[32:47]\n"
    "ds_read_b64_tr_b16 v[208:209], v224 offset:12288\n"
    "ds_read_b64_tr_b16 v[210:211], v224 offset:12544\n"
    "v_mfma_f32_32x32x16_bf16 v[48:63], v[220:223], v[68:71], v[48:63]\n"
    "ds_read_b64_tr_b16 v[212:213], v224 offset:12800\n"
    "ds_read_b64_tr_b16 v[214:215], v224 offset:13056\n"
    "s_waitcnt lgkmcnt(8)\n"
    "v_mfma_f32_32x32x16_bf16 v[0:15], v[192:195], v[72:75], v[0:15]\n"
    "ds_read_b64_tr_b16 v[216:217], v224 offset:13312\n"
    "ds_read_b64_tr_b16 v[218:219], v224 offset:13568\n"
    "v_mfma_f32_32x32x16_bf16 v[16:31], v[196:199], v[72:75], v[16:31]\n"
    "ds_read_b64_tr_b16 v[220:221], v224 offset:13824\n"
    "ds_read_b64_tr_b16 v[222:223], v224 offset:14080\n"
    "s_waitcnt lgkmcnt(8)\n"
    "v_mfma_f32_32x32x16_bf16 v[32:47], v[200:203], v[72:75], v[32:47]\n"
    "s_waitcnt vmcnt(16)\n"
    "s_barrier\n"
    "ds_read_b64_tr_b16 v[192:193], v224 offset:16384\n"
    "ds_read_b64_tr_b16 v[194:195], v224 offset:16640\n"
    "ds_read_b128 v[80:83], v227 offset:36864\n"
    "v_mfma_f32_32x32x16_bf16 v[48:63], v[204:207], v[72:75], v[48:63]\n"
    "ds_read_b64_tr_b16 v[196:197], v224 offset:16896\n"
    "ds_read_b64_tr_b16 v[198:199], v224 offset:17152\n"
    "ds_read_b128 v[84:87], v228 offset:36864\n"
    "s_add_u32 m0, s61, 147456\n"
    "s_add_u32 s66, s79, 2621440\n"
    "buffer_load_dwordx4 v225, s[44:47], s66 offen lds\n"
    "s_waitcnt lgkmcnt(10)\n"
    "v_mfma_f32_32x32x16_bf16 v[0:15], v[208:211], v[76:79], v[0:15]\n"
    "ds_read_b64_tr_b16 v[200:201], v224 offset:17408\n"
    "ds_read_b64_tr_b16 v[202:203], v224 offset:17664\n"
    "ds_read_b128 v[88:91], v227 offset:37376\n"
    "s_add_u32 m0, s61, 148352\n"
    "s_nop 0\n"
    "buffer_load_dwordx4 v225, s[44:47], s66 offen offset:128 lds\n"
    "s_add_u32 m0, s61, 149504\n"
    "s_add_u32 s66, s79, 2686976\n"
    "buffer_load_dwordx4 v225, s[44:47], s66 offen lds\n"
    "v_mfma_f32_32x32x16_bf16 v[16:31], v[212:215], v[76:79], v[16:31]\n"
    "ds_read_b64_tr_b16 v[204:205], v224 offset:17920\n"
    "ds_read_b64_tr_b16 v[206:207], v224 offset:18176\n"
    "s_waitcnt lgkmcnt(14)\n"
    "ds_read_b128 v[92:95], v228 offset:37376\n"
    "s_add_u32 m0, s61, 150400\n"
    "s_nop 0\n"
    "buffer_load_dwordx4 v225, s[44:47], s66 offen offset:128 lds\n"
    "s_waitcnt lgkmcnt(12)\n"
    "v_mfma_f32_32x32x16_bf16 v[32:47], v[216:219], v[76:79], v[32:47]\n"
    "ds_read_b64_tr_b16 v[208:209], v224 offset:20480\n"
    "ds_read_b64_tr_b16 v[210:211], v224 offset:20736\n"
    "s_add_u32 m0, s61, 32768\n"
    "s_add_u32 s66, s62, 0\n"
    "buffer_load_dwordx4 v245, s[40:43], s66 offen nt lds\n"
    "buffer_load_dwordx4 v245, s[40:43], s66 offen offset:2048 nt lds\n"
    "v_mfma_f32_32x32x16_bf16 v[48:63], v[220:223], v[76:79], v[48:63]\n"
    "ds_read_b64_tr_b16 v[212:213], v224 offset:20992\n"
    "s_waitcnt lgkmcnt(14)\n"
    "ds_read_b64_tr_b16 v[214:215], v224 offset:21248\n"
    "s_add_u32 m0, s61, 33664\n"
    "s_nop 0\n"
    "buffer_load_dwordx4 v245, s[40:43], s66 offen offset:128 nt lds\n"
    "buffer_load_dwordx4 v245, s[40:43], s66 offen offset:2176 nt lds\n"
    "s_waitcnt lgkmcnt(11)\n"
    "v_mfma_f32_32x32x16_bf16 v[0:15], v[192:195], v[80:83], v[0:15]\n"
    "ds_read_b64_tr_b16 v[216:217], v224 offset:21504\n"
    "ds_read_b64_tr_b16 v[218:219], v224 offset:21760\n"
    "v_mfma_f32_32x32x16_bf16 v[16:31], v[196:199], v[80:83], v[16:31]\n"
    "ds_read_b64_tr_b16 v[220:221], v224 offset:22016\n"
    "ds_read_b64_tr_b16 v[222:223], v224 offset:22272\n"
    "s_waitcnt lgkmcnt(9)\n"
    "v_mfma_f32_32x32x16_bf16 v[32:47], v[200:203], v[80:83], v[32:47]\n"
    "ds_read_b64_tr_b16 v[192:193], v224 offset:24576\n"
    "ds_read_b64_tr_b16 v[194:195], v224 offset:24832\n"
    "v_mfma_f32_32x32x16_bf16 v[48:63], v[204:207], v[80:83], v[48:63]\n"
    "ds_read_b64_tr_b16 v[196:197], v224 offset:25088\n"
    "ds_read_b64_tr_b16 v[198:199], v224 offset:25344\n"
    "s_waitcnt lgkmcnt(8)\n"
    "v_mfma_f32_32x32x16_bf16 v[0:15], v[208:211], v[84:87], v[0:15]\n"
    "ds_read_b64_tr_b16 v[200:201], v224 offset:25600\n"
    "ds_read_b64_tr_b16 v[202:203], v224 offset:25856\n"
    "v_mfma_f32_32x32x16_bf16 v[16:31], v[212:215], v[84:87], v[16:31]\n"
    "ds_read_b64_tr_b16 v[204:205], v224 offset:26112\n"
    "ds_read_b64_tr_b16 v[206:207], v224 offset:26368\n"
    "s_waitcnt lgkmcnt(8)\n"
    "v_mfma_f32_32x32x16_bf16 v[32:47], v[216:219], v[84:87], v[32:47]\n"
    "ds_read_b64_tr_b16 v[208:209], v224 offset:28672\n"
    "ds_read_b64_tr_b16 v[210:211], v224 offset:28928\n"
    "v_mfma_f32_32x32x16_bf16 v[48:63], v[220:223], v[84:87], v[48:63]\n"
    "ds_read_b64_tr_b16 v[212:213], v224 offset:29184\n"
    "ds_read_b64_tr_b16 v[214:215], v224 offset:29440\n"
    "s_waitcnt lgkmcnt(8)\n"
    "v_mfma_f32_32x32x16_bf16 v[0:15], v[192:195], v[88:91], v[0:15]\n"
    "ds_read_b64_tr_b16 v[216:217], v224 offset:29696\n"
    "ds_read_b64_tr_b16 v[218:219], v224 offset:29952\n"
    "v_mfma_f32_32x32x16_bf16 v[16:31], v[196:199], v[88:91], v[16:31]\n"
    "ds_read_b64_tr_b16 v[220:221], v224 offset:30208\n"
    "ds_read_b64_tr_b16 v[222:223], v224 offset:30464\n"
    "s_waitcnt lgkmcnt(8)\n"
    "v_mfma_f32_32x32x16_bf16 v[32:47], v[200:203], v[88:91], v[32:47]\n"
    "s_waitcnt vmcnt(8)\n"
    "s_barrier\n"
    "ds_read_b64_tr_b16 v[192:193], v230 offset:0\n"
    "ds_read_b64_tr_b16 v[194:195], v230 offset:256\n"
    "ds_read_b128 v[64:67], v231 offset:0\n"
    "v_mfma_f32_32x32x16_bf16 v[48:63], v[204:207], v[88:91], v[48:63]\n"
    "ds_read_b64_tr_b16 v[196:197], v230 offset:512\n"
    "ds_read_b64_tr_b16 v[198:199], v230 offset:768\n"
    "ds_read_b128 v[68:71], v232 offset:0\n"
    "s_add_u32 m0, s61, 49152\n"
    "s_add_u32 s66, s79, 3145728\n"
    "buffer_load_dwordx4 v225, s[44:47], s66 offen lds\n"
    "s_waitcnt lgkmcnt(10)\n"
    "v_mfma_f32_32x32x16_bf16 v[0:15], v[208:211], v[92:95], v[0:15]\n"
    "ds_read_b64_tr_b16 v[200:201], v230 offset:1024\n"
    "ds_read_b64_tr_b16 v[202:203], v230 offset:1280\n"
    "ds_read_b128 v[72:75], v231 offset:512\n"
    "s_add_u32 m0, s61, 50048\n"
    "s_nop 0\n"
    "buffer_load_dwordx4 v225, s[44:47], s66 offen offset:128 lds\n"
    "s_add_u32 m0, s61, 51200\n"
    "s_add_u32 s66, s79, 3211264\n"
    "buffer_load_dwordx4 v225, s[44:47], s66 offen lds\n"
    "v_mfma_f32_32x32x16_bf16 v[16:31], v[212:215], v[92:95], v[16:31]\n"
    "ds_read_b64_tr_b16 v[204:205], v230 offset:1536\n"
    "ds_read_b64_tr_b16 v[206:207], v230 offset:1792\n"
    "s_waitcnt lgkmcnt(14)\n"
    "ds_read_b128 v[76:79], v232 offset:512\n"
    "s_add_u32 m0, s61, 52096\n"
    "s_nop 0\n"
    "buffer_load_dwordx4 v225, s[44:47], s66 offen offset:128 lds\n"
    "s_waitcnt lgkmcnt(12)\n"
    "v_mfma_f32_32x32x16_bf16 v[32:47], v[216:219], v[92:95], v[32:47]\n"
    "ds_read_b64_tr_b16 v[208:209], v230 offset:4096\n"
    "ds_read_b64_tr_b16 v[210:211], v230 offset:4352\n"
    "s_add_u32 m0, s61, 0\n"
    "s_add_u32 s66, s62, 0\n"
    "buffer_load_dwordx4 v243, s[36:39], s66 offen nt lds\n"
    "buffer_load_dwordx4 v244, s[36:39], s66 offen offset:2048 nt lds\n"
    "v_mfma_f32_32x32x16_bf16 v[48:63], v[220:223], v[92:95], v[48:63]\n"
    "ds_read_b64_tr_b16 v[212:213], v230 offset:4608\n"
    "s_waitcnt lgkmcnt(14)\n"
    "ds_read_b64_tr_b16 v[214:215], v230 offset:4864\n"
    "s_add_u32 m0, s61, 896\n"
    "s_nop 0\n"
    "buffer_load_dwordx4 v243, s[36:39], s66 offen offset:128 nt lds\n"
    "buffer_load_dwordx4 v244, s[36:39], s66 offen offset:2176 nt lds\n"
    "s_waitcnt lgkmcnt(11)\n"
    "v_mfma_f32_32x32x16_bf16 v[0:15], v[192:195], v[64:67], v[0:15]\n"
    "ds_read_b64_tr_b16 v[216:217], v230 offset:5120\n"
    "ds_read_b64_tr_b16 v[218:219], v230 offset:5376\n"
    "v_mfma_f32_32x32x16_bf16 v[16:31], v[196:199], v[64:67], v[16:31]\n"
    "ds_read_b64_tr_b16 v[220:221], v230 offset:5632\n"
    "ds_read_b64_tr_b16 v[222:223], v230 offset:5888\n"
    "s_waitcnt lgkmcnt(9)\n"
    "v_mfma_f32_32x32x16_bf16 v[32:47], v[200:203], v[64:67], v[32:47]\n"
    "ds_read_b64_tr_b16 v[192:193], v230 offset:8192\n"
    "ds_read_b64_tr_b16 v[194:195], v230 offset:8448\n"
    "v_mfma_f32_32x32x16_bf16 v[48:63], v[204:207], v[64:67], v[48:63]\n"
    "ds_read_b64_tr_b16 v[196:197], v230 offset:8704\n"
    "ds_read_b64_tr_b16 v[198:199], v230 offset:8960\n"
    "s_waitcnt lgkmcnt(8)\n"
    "v_mfma_f32_32x32x16_bf16 v[0:15], v[208:211], v[68:71], v[0:15]\n"
    "ds_read_b64_tr_b16 v[200:201], v230 offset:9216\n"
    "ds_read_b64_tr_b16 v[202:203], v230 offset:9472\n"
    "v_mfma_f32_32x32x16_bf16 v[16:31], v[212:215], v[68:71], v[16:31]\n"
    "ds_read_b64_tr_b16 v[204:205], v230 offset:9728\n"
    "ds_read_b64_tr_b16 v[206:207], v230 offset:9984\n"
    "s_waitcnt lgkmcnt(8)\n"
    "v_mfma_f32_32x32x16_bf16 v[32:47], v[216:219], v[68:71], v[32:47]\n"
    "ds_read_b64_tr_b16 v[208:209], v230 offset:12288\n"
    "ds_read_b64_tr_b16 v[210:211], v230 offset:12544\n"
    "v_mfma_f32_32x32x16_bf16 v[48:63], v[220:223], v[68:71], v[48:63]\n"
    "ds_read_b64_tr_b16 v[212:213], v230 offset:12800\n"
    "ds_read_b64_tr_b16 v[214:215], v230 offset:13056\n"
    "s_waitcnt lgkmcnt(8)\n"
    "v_mfma_f32_32x32x16_bf16 v[0:15], v[192:195], v[72:75], v[0:15]\n"
    "ds_read_b64_tr_b16 v[216:217], v230 offset:13312\n"
    "ds_read_b64_tr_b16 v[218:219], v230 offset:13568\n"
    "v_mfma_f32_32x32x16_bf16 v[16:31], v[196:199], v[72:75], v[16:31]\n"
    "ds_read_b64_tr_b16 v[220:221], v230 offset:13824\n"
    "ds_read_b64_tr_b16 v[222:223], v230 offset:14080\n"
    "s_waitcnt lgkmcnt(8)\n"
    "v_mfma_f32_32x32x16_bf16 v[32:47], v[200:203], v[72:75], v[32:47]\n"
    "s_waitcnt vmcnt(12)\n"
    "s_barrier\n"
    "ds_read_b64_tr_b16 v[192:193], v230 offset:16384\n"
    "ds_read_b64_tr_b16 v[194:195], v230 offset:16640\n"
    "ds_read_b128 v[80:83], v231 offset:12288\n"
    "v_mfma_f32_32x32x16_bf16 v[48:63], v[204:207], v[72:75], v[48:63]\n"
    "ds_read_b64_tr_b16 v[196:197], v230 offset:16896\n"
    "ds_read_b64_tr_b16 v[198:199], v230 offset:17152\n"
    "ds_read_b128 v[84:87], v232 offset:12288\n"
    "s_add_u32 m0, s61, 65536\n"
    "s_add_u32 s66, s79, 3670016\n"
    "buffer_load_dwordx4 v225, s[44:47], s66 offen lds\n"
    "s_waitcnt lgkmcnt(10)\n"
    "v_mfma_f32_32x32x16_bf16 v[0:15], v[208:211], v[76:79], v[0:15]\n"
    "ds_read_b64_tr_b16 v[200:201], v230 offset:17408\n"
    "ds_read_b64_tr_b16 v[202:203], v230 offset:17664\n"
    "ds_read_b128 v[88:91], v231 offset:12800\n"
    "s_add_u32 m0, s61, 66432\n"
    "s_nop 0\n"
    "buffer_load_dwordx4 v225, s[44:47], s66 offen offset:128 lds\n"
    "s_add_u32 m0, s61, 67584\n"
    "s_add_u32 s66, s79, 3735552\n"
    "buffer_load_dwordx4 v225, s[44:47], s66 offen lds\n"
    "v_mfma_f32_32x32x16_bf16 v[16:31], v[212:215], v[76:79], v[16:31]\n"
    "ds_read_b64_tr_b16 v[204:205], v230 offset:17920\n"
    "ds_read_b64_tr_b16 v[206:207], v230 offset:18176\n"
    "s_waitcnt lgkmcnt(14)\n"
    "ds_read_b128 v[92:95], v232 offset:12800\n"
    "s_add_u32 m0, s61, 68480\n"
    "s_nop 0\n"
    "buffer_load_dwordx4 v225, s[44:47], s66 offen offset:128 lds\n"
    "s_waitcnt lgkmcnt(12)\n"
    "v_mfma_f32_32x32x16_bf16 v[32:47], v[216:219], v[76:79], v[32:47]\n"
    "ds_read_b64_tr_b16 v[208:209], v230 offset:20480\n"
    "ds_read_b64_tr_b16 v[210:211], v230 offset:20736\n"
    "s_add_u32 m0, s61, 16384\n"
    "s_add_u32 s66, s62, 16384\n"
    "buffer_load_dwordx4 v243, s[36:39], s66 offen nt lds\n"
    "buffer_load_dwordx4 v244, s[36:39], s66 offen offset:2048 nt lds\n"
    "v_mfma_f32_32x32x16_bf16 v[48:63], v[220:223], v[76:79], v[48:63]\n"
    "ds_read_b64_tr_b16 v[212:213], v230 offset:20992\n"
    "s_waitcnt lgkmcnt(14)\n"
    "ds_read_b64_tr_b16 v[214:215], v230 offset:21248\n"
    "s_add_u32 m0, s61, 17280\n"
    "s_nop 0\n"
    "buffer_load_dwordx4 v243, s[36:39], s66 offen offset:128 nt lds\n"
    "buffer_load_dwordx4 v244, s[36:39], s66 offen offset:2176 nt lds\n"
    "s_waitcnt lgkmcnt(11)\n"
    "v_mfma_f32_32x32x16_bf16 v[0:15], v[192:195], v[80:83], v[0:15]\n"
    "ds_read_b64_tr_b16 v[216:217], v230 offset:21504\n"
    "ds_read_b64_tr_b16 v[218:219], v230 offset:21760\n"
    "v_mfma_f32_32x32x16_bf16 v[16:31], v[196:199], v[80:83], v[16:31]\n"
    "ds_read_b64_tr_b16 v[220:221], v230 offset:22016\n"
    "ds_read_b64_tr_b16 v[222:223], v230 offset:22272\n"
    "s_waitcnt lgkmcnt(9)\n"
    "v_mfma_f32_32x32x16_bf16 v[32:47], v[200:203], v[80:83], v[32:47]\n"
    "ds_read_b64_tr_b16 v[192:193], v230 offset:24576\n"
    "ds_read_b64_tr_b16 v[194:195], v230 offset:24832\n"
    "v_mfma_f32_32x32x16_bf16 v[48:63], v[204:207], v[80:83], v[48:63]\n"
    "ds_read_b64_tr_b16 v[196:197], v230 offset:25088\n"
    "ds_read_b64_tr_b16 v[198:199], v230 offset:25344\n"
    "s_waitcnt lgkmcnt(8)\n"
    "v_mfma_f32_32x32x16_bf16 v[0:15], v[208:211], v[84:87], v[0:15]\n"
    "ds_read_b64_tr_b16 v[200:201], v230 offset:25600\n"
    "ds_read_b64_tr_b16 v[202:203], v230 offset:25856\n"
    "v_mfma_f32_32x32x16_bf16 v[16:31], v[212:215], v[84:87], v[16:31]\n"
    "ds_read_b64_tr_b16 v[204:205], v230 offset:26112\n"
    "ds_read_b64_tr_b16 v[206:207], v230 offset:26368\n"
    "s_waitcnt lgkmcnt(8)\n"
    "v_mfma_f32_32x32x16_bf16 v[32:47], v[216:219], v[84:87], v[32:47]\n"
    "ds_read_b64_tr_b16 v[208:209], v230 offset:28672\n"
    "ds_read_b64_tr_b16 v[210:211], v230 offset:28928\n"
    "v_mfma_f32_32x32x16_bf16 v[48:63], v[220:223], v[84:87], v[48:63]\n"
    "ds_read_b64_tr_b16 v[212:213], v230 offset:29184\n"
    "ds_read_b64_tr_b16 v[214:215], v230 offset:29440\n"
    "s_waitcnt lgkmcnt(8)\n"
    "v_mfma_f32_32x32x16_bf16 v[0:15], v[192:195], v[88:91], v[0:15]\n"
    "ds_read_b64_tr_b16 v[216:217], v230 offset:29696\n"
    "ds_read_b64_tr_b16 v[218:219], v230 offset:29952\n"
    "v_mfma_f32_32x32x16_bf16 v[16:31], v[196:199], v[88:91], v[16:31]\n"
    "ds_read_b64_tr_b16 v[220:221], v230 offset:30208\n"
    "ds_read_b64_tr_b16 v[222:223], v230 offset:30464\n"
    "s_waitcnt lgkmcnt(8)\n"
    "v_mfma_f32_32x32x16_bf16 v[32:47], v[200:203], v[88:91], v[32:47]\n"
    "s_waitcnt vmcnt(12)\n"
    "s_barrier\n"
    "ds_read_b64_tr_b16 v[192:193], v224 offset:49152\n"
    "ds_read_b64_tr_b16 v[194:195], v224 offset:49408\n"
    "ds_read_b128 v[64:67], v231 offset:24576\n"
    "v_mfma_f32_32x32x16_bf16 v[48:63], v[204:207], v[88:91], v[48:63]\n"
    "ds_read_b64_tr_b16 v[196:197], v224 offset:49664\n"
    "ds_read_b64_tr_b16 v[198:199], v224 offset:49920\n"
    "ds_read_b128 v[68:71], v232 offset:24576\n"
    "s_waitcnt lgkmcnt(10)\n"
    "v_mfma_f32_32x32x16_bf16 v[0:15], v[208:211], v[92:95], v[0:15]\n"
    "ds_read_b64_tr_b16 v[200:201], v224 offset:50176\n"
    "ds_read_b64_tr_b16 v[202:203], v224 offset:50432\n"
    "ds_read_b128 v[72:75], v231 offset:25088\n"
    "v_mfma_f32_32x32x16_bf16 v[16:31], v[212:215], v[92:95], v[16:31]\n"
    "ds_read_b64_tr_b16 v[204:205], v224 offset:50688\n"
    "ds_read_b64_tr_b16 v[206:207], v224 offset:50944\n"
    "s_waitcnt lgkmcnt(14)\n"
    "ds_read_b128 v[76:79], v232 offset:25088\n"
    "s_waitcnt lgkmcnt(12)\n"
    "v_mfma_f32_32x32x16_bf16 v[32:47], v[216:219], v[92:95], v[32:47]\n"
    "ds_read_b64_tr_b16 v[208:209], v224 offset:53248\n"
    "ds_read_b64_tr_b16 v[210:211], v224 offset:53504\n"
    "v_mfma_f32_32x32x16_bf16 v[48:63], v[220:223], v[92:95], v[48:63]\n"
    "ds_read_b64_tr_b16 v[212:213], v224 offset:53760\n"
    "s_waitcnt lgkmcnt(14)\n"
    "ds_read_b64_tr_b16 v[214:215], v224 offset:54016\n"
    "s_waitcnt lgkmcnt(11)\n"
    "v_mfma_f32_32x32x16_bf16 v[0:15], v[192:195], v[64:67], v[0:15]\n"
    "ds_read_b64_tr_b16 v[216:217], v224 offset:54272\n"
    "ds_read_b64_tr_b16 v[218:219], v224 offset:54528\n"
    "v_mfma_f32_32x32x16_bf16 v[16:31], v[196:199], v[64:67], v[16:31]\n"
    "ds_read_b64_tr_b16 v[220:221], v224 offset:54784\n"
    "ds_read_b64_tr_b16 v[222:223], v224 offset:55040\n"
    "s_waitcnt lgkmcnt(9)\n"
    "v_mfma_f32_32x32x16_bf16 v[32:47], v[200:203], v[64:67], v[32:47]\n"
    "ds_read_b64_tr_b16 v[192:193], v224 offset:57344\n"
    "ds_read_b64_tr_b16 v[194:195], v224 offset:57600\n"
    "v_mfma_f32_32x32x16_bf16 v[48:63], v[204:207], v[64:67], v[48:63]\n"
    "ds_read_b64_tr_b16 v[196:197], v224 offset:57856\n"
    "ds_read_b64_tr_b16 v[198:199], v224 offset:58112\n"
    "s_waitcnt lgkmcnt(8)\n"
    "v_mfma_f32_32x32x16_bf16 v[0:15], v[208:211], v[68:71], v[0:15]\n"
    "ds_read_b64_tr_b16 v[200:201], v224 offset:58368\n"
    "ds_read_b64_tr_b16 v[202:203], v224 offset:58624\n"
    "v_mfma_f32_32x32x16_bf16 v[16:31], v[212:215], v[68:71], v[16:31]\n"
    "ds_read_b64_tr_b16 v[204:205], v224 offset:58880\n"
    "ds_read_b64_tr_b16 v[206:207], v224 offset:59136\n"
    "s_waitcnt lgkmcnt(8)\n"
    "v_mfma_f32_32x32x16_bf16 v[32:47], v[216:219], v[68:71], v[32:47]\n"
    "ds_read_b64_tr_b16 v[208:209], v224 offset:61440\n"
    "ds_read_b64_tr_b16 v[210:211], v224 offset:61696\n"
    "v_mfma_f32_32x32x16_bf16 v[48:63], v[220:223], v[68:71], v[48:63]\n"
    "ds_read_b64_tr_b16 v[212:213], v224 offset:61952\n"
    "ds_read_b64_tr_b16 v[214:215], v224 offset:62208\n"
    "s_waitcnt lgkmcnt(8)\n"
    "v_mfma_f32_32x32x16_bf16 v[0:15], v[192:195], v[72:75], v[0:15]\n"
    "ds_read_b64_tr_b16 v[216:217], v224 offset:62464\n"
    "ds_read_b64_tr_b16 v[218:219], v224 offset:62720\n"
    "v_mfma_f32_32x32x16_bf16 v[16:31], v[196:199], v[72:75], v[16:31]\n"
    "ds_read_b64_tr_b16 v[220:221], v224 offset:62976\n"
    "ds_read_b64_tr_b16 v[222:223], v224 offset:63232\n"
    "s_waitcnt lgkmcnt(8)\n"
    "v_mfma_f32_32x32x16_bf16 v[32:47], v[200:203], v[72:75], v[32:47]\n"
    "s_waitcnt vmcnt(4)\n"
    "s_barrier\n"
    "ds_read_b64_tr_b16 v[192:193], v229 offset:0\n"
    "ds_read_b64_tr_b16 v[194:195], v229 offset:256\n"
    "ds_read_b128 v[80:83], v231 offset:36864\n"
    "v_mfma_f32_32x32x16_bf16 v[48:63], v[204:207], v[72:75], v[48:63]\n"
    "ds_read_b64_tr_b16 v[196:197], v229 offset:512\n"
    "ds_read_b64_tr_b16 v[198:199], v229 offset:768\n"
    "ds_read_b128 v[84:87], v232 offset:36864\n"
    "s_waitcnt lgkmcnt(10)\n"
    "v_mfma_f32_32x32x16_bf16 v[0:15], v[208:211], v[76:79], v[0:15]\n"
    "ds_read_b64_tr_b16 v[200:201], v229 offset:1024\n"
    "ds_read_b64_tr_b16 v[202:203], v229 offset:1280\n"
    "ds_read_b128 v[88:91], v231 offset:37376\n"
    "v_mfma_f32_32x32x16_bf16 v[16:31], v[212:215], v[76:79], v[16:31]\n"
    "ds_read_b64_tr_b16 v[204:205], v229 offset:1536\n"
    "ds_read_b64_tr_b16 v[206:207], v229 offset:1792\n"
    "s_waitcnt lgkmcnt(14)\n"
    "ds_read_b128 v[92:95], v232 offset:37376\n"
    "s_waitcnt lgkmcnt(12)\n"
    "v_mfma_f32_32x32x16_bf16 v[32:47], v[216:219], v[76:79], v[32:47]\n"
    "ds_read_b64_tr_b16 v[208:209], v229 offset:4096\n"
    "ds_read_b64_tr_b16 v[210:211], v229 offset:4352\n"
    "v_mfma_f32_32x32x16_bf16 v[48:63], v[220:223], v[76:79], v[48:63]\n"
    "ds_read_b64_tr_b16 v[212:213], v229 offset:4608\n"
    "s_waitcnt lgkmcnt(14)\n"
    "ds_read_b64_tr_b16 v[214:215], v229 offset:4864\n"
    "s_waitcnt lgkmcnt(11)\n"
    "v_mfma_f32_32x32x16_bf16 v[0:15], v[192:195], v[80:83], v[0:15]\n"
    "ds_read_b64_tr_b16 v[216:217], v229 offset:5120\n"
    "ds_read_b64_tr_b16 v[218:219], v229 offset:5376\n"
    "v_mfma_f32_32x32x16_bf16 v[16:31], v[196:199], v[80:83], v[16:31]\n"
    "ds_read_b64_tr_b16 v[220:221], v229 offset:5632\n"
    "ds_read_b64_tr_b16 v[222:223], v229 offset:5888\n"
    "s_waitcnt lgkmcnt(9)\n"
    "v_mfma_f32_32x32x16_bf16 v[32:47], v[200:203], v[80:83], v[32:47]\n"
    "ds_read_b64_tr_b16 v[192:193], v229 offset:8192\n"
    "ds_read_b64_tr_b16 v[194:195], v229 offset:8448\n"
    "v_mfma_f32_32x32x16_bf16 v[48:63], v[204:207], v[80:83], v[48:63]\n"
    "ds_read_b64_tr_b16 v[196:197], v229 offset:8704\n"
    "ds_read_b64_tr_b16 v[198:199], v229 offset:8960\n"
    "s_waitcnt lgkmcnt(8)\n"
    "v_mfma_f32_32x32x16_bf16 v[0:15], v[208:211], v[84:87], v[0:15]\n"
    "ds_read_b64_tr_b16 v[200:201], v229 offset:9216\n"
    "ds_read_b64_tr_b16 v[202:203], v229 offset:9472\n"
    "v_mfma_f32_32x32x16_bf16 v[16:31], v[212:215], v[84:87], v[16:31]\n"
    "ds_read_b64_tr_b16 v[204:205], v229 offset:9728\n"
    "ds_read_b64_tr_b16 v[206:207], v229 offset:9984\n"
    "s_waitcnt lgkmcnt(8)\n"
    "v_mfma_f32_32x32x16_bf16 v[32:47], v[216:219], v[84:87], v[32:47]\n"
    "ds_read_b64_tr_b16 v[208:209], v229 offset:12288\n"
    "ds_read_b64_tr_b16 v[210:211], v229 offset:12544\n"
    "v_mfma_f32_32x32x16_bf16 v[48:63], v[220:223], v[84:87], v[48:63]\n"
    "ds_read_b64_tr_b16 v[212:213], v229 offset:12800\n"
    "ds_read_b64_tr_b16 v[214:215], v229 offset:13056\n"
    "s_waitcnt lgkmcnt(8)\n"
    "v_mfma_f32_32x32x16_bf16 v[0:15], v[192:195], v[88:91], v[0:15]\n"
    "ds_read_b64_tr_b16 v[216:217], v229 offset:13312\n"
    "ds_read_b64_tr_b16 v[218:219], v229 offset:13568\n"
    "v_mfma_f32_32x32x16_bf16 v[16:31], v[196:199], v[88:91], v[16:31]\n"
    "ds_read_b64_tr_b16 v[220:221], v229 offset:13824\n"
    "ds_read_b64_tr_b16 v[222:223], v229 offset:14080\n"
    "s_waitcnt lgkmcnt(8)\n"
    "v_mfma_f32_32x32x16_bf16 v[32:47], v[200:203], v[88:91], v[32:47]\n"
    "s_barrier\n"
    "ds_read_b128 v[192:195], v240 offset:0\n"
    "v_mfma_f32_32x32x16_bf16 v[48:63], v[204:207], v[88:91], v[48:63]\n"
    "ds_read_b128 v[196:199], v241 offset:0\n"
    "s_waitcnt lgkmcnt(6)\n"
    "v_mfma_f32_32x32x16_bf16 v[0:15], v[208:211], v[92:95], v[0:15]\n"
    "ds_read_b128 v[200:203], v240 offset:512\n"
    "v_mfma_f32_32x32x16_bf16 v[16:31], v[212:215], v[92:95], v[16:31]\n"
    "ds_read_b128 v[204:207], v241 offset:512\n"
    "s_waitcnt lgkmcnt(4)\n"
    "v_mfma_f32_32x32x16_bf16 v[32:47], v[216:219], v[92:95], v[32:47]\n"
    "ds_read_b128 v[208:211], v240 offset:1024\n"
    "v_mfma_f32_32x32x16_bf16 v[48:63], v[220:223], v[92:95], v[48:63]\n"
    "ds_read_b128 v[212:215], v241 offset:1024\n"
    "s_nop 3\n"
    "v_pk_mul_f32 v[0:1], v[0:1], s[72:73]\n"
    "v_pk_mul_f32 v[2:3], v[2:3], s[72:73]\n"
    "v_pk_mul_f32 v[4:5], v[4:5], s[72:73]\n"
    "v_pk_mul_f32 v[6:7], v[6:7], s[72:73]\n"
    "v_pk_mul_f32 v[8:9], v[8:9], s[72:73]\n"
    "v_pk_mul_f32 v[10:11], v[10:11], s[72:73]\n"
    "v_pk_mul_f32 v[12:13], v[12:13], s[72:73]\n"
    "v_pk_mul_f32 v[14:15], v[14:15], s[72:73]\n"
    "v_cvt_pk_bf16_f32 v96, v0, v1\n"
    "v_cvt_pk_bf16_f32 v97, v2, v3\n"
    "v_cvt_pk_bf16_f32 v98, v4, v5\n"
    "v_cvt_pk_bf16_f32 v99, v6, v7\n"
    "v_cvt_pk_bf16_f32 v100, v8, v9\n"
    "v_cvt_pk_bf16_f32 v101, v10, v11\n"
    "v_cvt_pk_bf16_f32 v102, v12, v13\n"
    "v_cvt_pk_bf16_f32 v103, v14, v15\n"
    "v_pk_mul_f32 v[16:17], v[16:17], s[72:73]\n"
    "v_pk_mul_f32 v[18:19], v[18:19], s[72:73]\n"
    "v_pk_mul_f32 v[20:21], v[20:21], s[72:73]\n"
    "v_pk_mul_f32 v[22:23], v[22:23], s[72:73]\n"
    "v_pk_mul_f32 v[24:25], v[24:25], s[72:73]\n"
    "v_pk_mul_f32 v[26:27], v[26:27], s[72:73]\n"
    "v_pk_mul_f32 v[28:29], v[28:29], s[72:73]\n"
    "v_pk_mul_f32 v[30:31], v[30:31], s[72:73]\n"
    "v_cvt_pk_bf16_f32 v104, v16, v17\n"
    "v_cvt_pk_bf16_f32 v105, v18, v19\n"
    "v_cvt_pk_bf16_f32 v106, v20, v21\n"
    "v_cvt_pk_bf16_f32 v107, v22, v23\n"
    "v_cvt_pk_bf16_f32 v108, v24, v25\n"
    "v_cvt_pk_bf16_f32 v109, v26, v27\n"
    "v_cvt_pk_bf16_f32 v110, v28, v29\n"
    "v_cvt_pk_bf16_f32 v111, v30, v31\n"
    "v_pk_mul_f32 v[32:33], v[32:33], s[72:73]\n"
    "v_pk_mul_f32 v[34:35], v[34:35], s[72:73]\n"
    "v_pk_mul_f32 v[36:37], v[36:37], s[72:73]\n"
    "v_pk_mul_f32 v[38:39], v[38:39], s[72:73]\n"
    "v_pk_mul_f32 v[40:41], v[40:41], s[72:73]\n"
    "v_pk_mul_f32 v[42:43], v[42:43], s[72:73]\n"
    "v_pk_mul_f32 v[44:45], v[44:45], s[72:73]\n"
    "v_pk_mul_f32 v[46:47], v[46:47], s[72:73]\n"
    "v_cvt_pk_bf16_f32 v112, v32, v33\n"
    "v_cvt_pk_bf16_f32 v113, v34, v35\n"
    "v_cvt_pk_bf16_f32 v114, v36, v37\n"
    "v_cvt_pk_bf16_f32 v115, v38, v39\n"
    "v_cvt_pk_bf16_f32 v116, v40, v41\n"
    "v_cvt_pk_bf16_f32 v117, v42, v43\n"
    "v_cvt_pk_bf16_f32 v118, v44, v45\n"
    "v_cvt_pk_bf16_f32 v119, v46, v47\n"
    "v_pk_mul_f32 v[48:49], v[48:49], s[72:73]\n"
    "v_pk_mul_f32 v[50:51], v[50:51], s[72:73]\n"
    "v_pk_mul_f32 v[52:53], v[52:53], s[72:73]\n"
    "v_pk_mul_f32 v[54:55], v[54:55], s[72:73]\n"
    "v_pk_mul_f32 v[56:57], v[56:57], s[72:73]\n"
    "v_pk_mul_f32 v[58:59], v[58:59], s[72:73]\n"
    "v_pk_mul_f32 v[60:61], v[60:61], s[72:73]\n"
    "v_pk_mul_f32 v[62:63], v[62:63], s[72:73]\n"
    "v_cvt_pk_bf16_f32 v120, v48, v49\n"
    "v_cvt_pk_bf16_f32 v121, v50, v51\n"
    "v_cvt_pk_bf16_f32 v122, v52, v53\n"
    "v_cvt_pk_bf16_f32 v123, v54, v55\n"
    "v_cvt_pk_bf16_f32 v124, v56, v57\n"
    "v_cvt_pk_bf16_f32 v125, v58, v59\n"
    "v_cvt_pk_bf16_f32 v126, v60, v61\n"
    "v_cvt_pk_bf16_f32 v127, v62, v63\n"
    "s_waitcnt lgkmcnt(4)\n"
    "v_mfma_f32_32x32x16_bf16 v[128:143], v[192:195], v[96:99], 0\n"
    "ds_read_b128 v[216:219], v240 offset:1536\n"
    "v_mfma_f32_32x32x16_bf16 v[128:143], v[196:199], v[100:103], v[128:143]\n"
    "ds_read_b128 v[220:223], v241 offset:1536\n"
    "s_waitcnt lgkmcnt(4)\n"
    "v_mfma_f32_32x32x16_bf16 v[128:143], v[200:203], v[104:107], v[128:143]\n"
    "ds_read_b128 v[192:195], v240 offset:8192\n"
    "v_mfma_f32_32x32x16_bf16 v[128:143], v[204:207], v[108:111], v[128:143]\n"
    "ds_read_b128 v[196:199], v241 offset:8192\n"
    "s_waitcnt lgkmcnt(4)\n"
    "v_mfma_f32_32x32x16_bf16 v[128:143], v[208:211], v[112:115], v[128:143]\n"
    "ds_read_b128 v[200:203], v240 offset:8704\n"
    "v_mfma_f32_32x32x16_bf16 v[128:143], v[212:215], v[116:119], v[128:143]\n"
    "ds_read_b128 v[204:207], v241 offset:8704\n"
    "s_waitcnt lgkmcnt(4)\n"
    "v_mfma_f32_32x32x16_bf16 v[128:143], v[216:219], v[120:123], v[128:143]\n"
    "ds_read_b128 v[208:211], v240 offset:9216\n"
    "v_mfma_f32_32x32x16_bf16 v[128:143], v[220:223], v[124:127], v[128:143]\n"
    "ds_read_b128 v[212:215], v241 offset:9216\n"
    "s_waitcnt lgkmcnt(4)\n"
    "v_mfma_f32_32x32x16_bf16 v[144:159], v[192:195], v[96:99], 0\n"
    "ds_read_b128 v[216:219], v240 offset:9728\n"
    "v_mfma_f32_32x32x16_bf16 v[144:159], v[196:199], v[100:103], v[144:159]\n"
    "ds_read_b128 v[220:223], v241 offset:9728\n"
    "s_waitcnt lgkmcnt(4)\n"
    "v_mfma_f32_32x32x16_bf16 v[144:159], v[200:203], v[104:107], v[144:159]\n"
    "s_barrier\n"
    "v_mfma_f32_32x32x16_bf16 v[144:159], v[204:207], v[108:111], v[144:159]\n"
    "s_waitcnt lgkmcnt(2)\n"
    "v_mfma_f32_32x32x16_bf16 v[144:159], v[208:211], v[112:115], v[144:159]\n"
    "v_max3_f32 v249, v128, v129, v130\n"
    "v_max3_f32 v249, v249, v131, v132\n"
    "v_max3_f32 v249, v249, v133, v134\n"
    "v_max3_f32 v249, v249, v135, v136\n"
    "v_max3_f32 v249, v249, v137, v138\n"
    "v_max3_f32 v249, v249, v139, v140\n"
    "v_mfma_f32_32x32x16_bf16 v[144:159], v[212:215], v[116:119], v[144:159]\n"
    "v_max3_f32 v249, v249, v141, v142\n"
    "v_max_f32 v249, v249, v143\n"
    "s_waitcnt lgkmcnt(0)\n"
    "v_mfma_f32_32x32x16_bf16 v[144:159], v[216:219], v[120:123], v[144:159]\n"
    "v_mfma_f32_32x32x16_bf16 v[144:159], v[220:223], v[124:127], v[144:159]\n"
    "s_barrier\n"
    "s_add_u32 m0, s61, 0\n"
    "s_add_u32 s66, s62, 32768\n"
    "buffer_load_dwordx4 v243, s[36:39], s66 offen nt lds\n"
    "buffer_load_dwordx4 v244, s[36:39], s66 offen offset:2048 nt lds\n"
    "s_add_u32 m0, s61, 896\n"
    "s_nop 0\n"
    "buffer_load_dwordx4 v243, s[36:39], s66 offen offset:128 nt lds\n"
    "buffer_load_dwordx4 v244, s[36:39], s66 offen offset:2176 nt lds\n"
    "s_nop 2\n"
    "v_max3_f32 v250, v144, v145, v146\n"
    "v_max3_f32 v250, v250, v147, v148\n"
    "v_max3_f32 v250, v250, v149, v150\n"
    "v_max3_f32 v250, v250, v151, v152\n"
    "v_max3_f32 v250, v250, v153, v154\n"
    "v_max3_f32 v250, v250, v155, v156\n"
    "v_max3_f32 v250, v250, v157, v158\n"
    "v_max_f32 v250, v250, v159\n"
    "v_max_f32 v249, v249, v250\n"
    "v_mov_b32 v251, v249\n"
    "s_waitcnt vmcnt(4)\n"
    "s_barrier\n"
    "ds_read_b128 v[192:195], v240 offset:16384\n"
    "ds_read_b128 v[196:199], v241 offset:16384\n"
    "ds_read_b128 v[200:203], v240 offset:16896\n"
    "ds_read_b128 v[204:207], v241 offset:16896\n"
    "ds_read_b128 v[208:211], v240 offset:17408\n"
    "ds_read_b128 v[212:215], v241 offset:17408\n"
    "v_permlane32_swap_b32 v249, v251\n"
    "v_max_f32 v249, v249, v251\n"
    "v_sub_f32 v176, 0, v249\n"
    "v_sub_f32 v177, 0, v249\n"
    "v_sub_f32 v178, 0, v249\n"
    "v_sub_f32 v179, 0, v249\n"
    "v_sub_f32 v180, 0, v249\n"
    "v_sub_f32 v181, 0, v249\n"
    "v_sub_f32 v182, 0, v249\n"
    "v_sub_f32 v183, 0, v249\n"
    "v_sub_f32 v184, 0, v249\n"
    "v_sub_f32 v185, 0, v249\n"
    "v_sub_f32 v186, 0, v249\n"
    "v_sub_f32 v187, 0, v249\n"
    "v_sub_f32 v188, 0, v249\n"
    "v_sub_f32 v189, 0, v249\n"
    "v_sub_f32 v190, 0, v249\n"
    "v_sub_f32 v191, 0, v249\n"
    "v_sub_f32 v128, v128, v249\n"
    "v_sub_f32 v129, v129, v249\n"
    "v_sub_f32 v130, v130, v249\n"
    "v_sub_f32 v131, v131, v249\n"
    "v_sub_f32 v132, v132, v249\n"
    "v_sub_f32 v133, v133, v249\n"
    "v_sub_f32 v134, v134, v249\n"
    "v_sub_f32 v135, v135, v249\n"
    "v_sub_f32 v136, v136, v249\n"
    "v_sub_f32 v137, v137, v249\n"
    "v_sub_f32 v138, v138, v249\n"
    "v_sub_f32 v139, v139, v249\n"
    "v_sub_f32 v140, v140, v249\n"
    "v_sub_f32 v141, v141, v249\n"
    "v_sub_f32 v142, v142, v249\n"
    "v_sub_f32 v143, v143, v249\n"
    "v_sub_f32 v144, v144, v249\n"
    "v_sub_f32 v145, v145, v249\n"
    "v_sub_f32 v146, v146, v249\n"
    "v_sub_f32 v147, v147, v249\n"
    "v_sub_f32 v148, v148, v249\n"
    "v_sub_f32 v149, v149, v249\n"
    "v_sub_f32 v150, v150, v249\n"
    "v_sub_f32 v151, v151, v249\n"
    "v_sub_f32 v152, v152, v249\n"
    "v_sub_f32 v153, v153, v249\n"
    "v_sub_f32 v154, v154, v249\n"
    "v_sub_f32 v155, v155, v249\n"
    "v_sub_f32 v156, v156, v249\n"
    "v_sub_f32 v157, v157, v249\n"
    "v_sub_f32 v158, v158, v249\n"
    "v_sub_f32 v159, v159, v249\n"
    "v_mov_b32 v247, 0\n"
    "v_mov_b32 v248, 0\n"
    "v_exp_f32 v128, v128\n"
    "v_exp_f32 v129, v129\n"
    "v_exp_f32 v130, v130\n"
    "v_add_f32 v247, v247, v128\n"
    "v_exp_f32 v131, v131\n"
    "v_add_f32 v248, v248, v129\n"
    "v_exp_f32 v132, v132\n"
    "v_add_f32 v247, v247, v130\n"
    "v_exp_f32 v133, v133\n"
    "v_add_f32 v248, v248, v131\n"
    "v_exp_f32 v134, v134\n"
    "v_add_f32 v247, v247, v132\n"
    "v_exp_f32 v135, v135\n"
    "v_add_f32 v248, v248, v133\n"
    "v_exp_f32 v136, v136\n"
    "v_add_f32 v247, v247, v134\n"
    "v_exp_f32 v137, v137\n"
    "v_add_f32 v248, v248, v135\n"
    "v_exp_f32 v138, v138\n"
    "v_add_f32 v247, v247, v136\n"
    "v_exp_f32 v139, v139\n"
    "v_add_f32 v248, v248, v137\n"
    "v_exp_f32 v140, v140\n"
    "v_add_f32 v247, v247, v138\n"
    "v_exp_f32 v141, v141\n"
    "v_add_f32 v248, v248, v139\n"
    "v_exp_f32 v142, v142\n"
    "v_add_f32 v247, v247, v140\n"
    "v_exp_f32 v143, v143\n"
    "v_add_f32 v248, v248, v141\n"
    "v_add_f32 v247, v247, v142\n"
    "v_add_f32 v248, v248, v143\n"
    "v_cvt_pk_bf16_f32 v160, v128, v129\n"
    "v_cvt_pk_bf16_f32 v161, v130, v131\n"
    "v_cvt_pk_bf16_f32 v162, v132, v133\n"
    "v_cvt_pk_bf16_f32 v163, v134, v135\n"
    "v_cvt_pk_bf16_f32 v164, v136, v137\n"
    "v_cvt_pk_bf16_f32 v165, v138, v139\n"
    "v_cvt_pk_bf16_f32 v166, v140, v141\n"
    "v_cvt_pk_bf16_f32 v167, v142, v143\n"
    "v_exp_f32 v144, v144\n"
    "v_exp_f32 v145, v145\n"
    "v_exp_f32 v146, v146\n"
    "v_add_f32 v247, v247, v144\n"
    "v_exp_f32 v147, v147\n"
    "v_add_f32 v248, v248, v145\n"
    "v_exp_f32 v148, v148\n"
    "v_add_f32 v247, v247, v146\n"
    "v_exp_f32 v149, v149\n"
    "v_add_f32 v248, v248, v147\n"
    "v_exp_f32 v150, v150\n"
    "v_add_f32 v247, v247, v148\n"
    "v_exp_f32 v151, v151\n"
    "v_mov_b32 v228, 0\n"
    "v_mov_b32 v229, 0\n"
    "v_mov_b32 v230, 0\n"
    "v_mov_b32 v231, 0\n"
    "s_nop 1\n"
    "v_mfma_f32_32x32x16_bf16 a[0:15], v[228:231], v[228:231], 0\n"
    "v_mfma_f32_32x32x16_bf16 a[16:31], v[228:231], v[228:231], 0\n"
    "v_mfma_f32_32x32x16_bf16 a[32:47], v[228:231], v[228:231], 0\n"
    "v_mfma_f32_32x32x16_bf16 a[48:63], v[228:231], v[228:231], 0\n"
    "v_mfma_f32_32x32x16_bf16 a[64:79], v[228:231], v[228:231], 0\n"
    "v_mfma_f32_32x32x16_bf16 a[80:95], v[228:231], v[228:231], 0\n"
    "v_mfma_f32_32x32x16_bf16 a[96:111], v[228:231], v[228:231], 0\n"
    "v_mfma_f32_32x32x16_bf16 a[112:127], v[228:231], v[228:231], 0\n"
    "v_mfma_f32_32x32x16_bf16 a[128:143], v[228:231], v[228:231], 0\n"
    "v_mfma_f32_32x32x16_bf16 a[144:159], v[228:231], v[228:231], 0\n"
    "v_mfma_f32_32x32x16_bf16 a[160:175], v[228:231], v[228:231], 0\n"
    "v_mfma_f32_32x32x16_bf16 a[176:191], v[228:231], v[228:231], 0\n"
    "v_mfma_f32_32x32x16_bf16 a[192:207], v[228:231], v[228:231], 0\n"
    "v_mfma_f32_32x32x16_bf16 a[208:223], v[228:231], v[228:231], 0\n"
    "v_mfma_f32_32x32x16_bf16 a[224:239], v[228:231], v[228:231], 0\n"
    "v_mfma_f32_32x32x16_bf16 a[240:255], v[228:231], v[228:231], 0\n"
    "s_waitcnt lgkmcnt(4)\n"
    "v_mfma_f32_32x32x16_bf16 v[128:143], v[192:195], v[96:99], v[176:191]\n"
    "ds_read_b128 v[216:219], v240 offset:17920\n"
    "v_add_f32 v248, v248, v149\n"
    "v_exp_f32 v152, v152\n"
    "v_add_f32 v247, v247, v150\n"
    "v_mfma_f32_32x32x16_bf16 v[128:143], v[196:199], v[100:103], v[128:143]\n"
    "ds_read_b128 v[220:223], v241 offset:17920\n"
    "v_exp_f32 v153, v153\n"
    "v_add_f32 v248, v248, v151\n"
    "s_waitcnt lgkmcnt(4)\n"
    "v_mfma_f32_32x32x16_bf16 v[128:143], v[200:203], v[104:107], v[128:143]\n"
    "ds_read_b128 v[192:195], v240 offset:24576\n"
    "v_exp_f32 v154, v154\n"
    "v_add_f32 v247, v247, v152\n"
    "v_exp_f32 v155, v155\n"
    "v_mfma_f32_32x32x16_bf16 v[128:143], v[204:207], v[108:111], v[128:143]\n"
    "ds_read_b128 v[196:199], v241 offset:24576\n"
    "v_add_f32 v248, v248, v153\n"
    "v_exp_f32 v156, v156\n"
    "v_add_f32 v247, v247, v154\n"
    "s_waitcnt lgkmcnt(4)\n"
    "v_mfma_f32_32x32x16_bf16 v[128:143], v[208:211], v[112:115], v[128:143]\n"
    "ds_read_b128 v[200:203], v240 offset:25088\n"
    "v_exp_f32 v157, v157\n"
    "v_add_f32 v248, v248, v155\n"
    "v_exp_f32 v158, v158\n"
    "v_mfma_f32_32x32x16_bf16 v[128:143], v[212:215], v[116:119], v[128:143]\n"
    "ds_read_b128 v[204:207], v241 offset:25088\n"
    "v_add_f32 v247, v247, v156\n"
    "v_exp_f32 v159, v159\n"
    "v_add_f32 v248, v248, v157\n"
    "s_waitcnt lgkmcnt(4)\n"
    "v_mfma_f32_32x32x16_bf16 v[128:143], v[216:219], v[120:123], v[128:143]\n"
    "ds_read_b128 v[208:211], v240 offset:25600\n"
    "v_add_f32 v247, v247, v158\n"
    "v_add_f32 v248, v248, v159\n"
    "v_cvt_pk_bf16_f32 v168, v144, v145\n"
    "v_cvt_pk_bf16_f32 v169, v146, v147\n"
    "v_cvt_pk_bf16_f32 v170, v148, v149\n"
    "v_mfma_f32_32x32x16_bf16 v[128:143], v[220:223], v[124:127], v[128:143]\n"
    "ds_read_b128 v[212:215], v241 offset:25600\n"
    "v_cvt_pk_bf16_f32 v171, v150, v151\n"
    "v_cvt_pk_bf16_f32 v172, v152, v153\n"
    "v_cvt_pk_bf16_f32 v173, v154, v155\n"
    "v_cvt_pk_bf16_f32 v174, v156, v157\n"
    "v_cvt_pk_bf16_f32 v175, v158, v159\n"
    "s_waitcnt lgkmcnt(4)\n"
    "v_mfma_f32_32x32x16_bf16 v[144:159], v[192:195], v[96:99], v[176:191]\n"
    "ds_read_b128 v[216:219], v240 offset:26112\n"
    "s_add_u32 m0, s61, 49152\n"
    "s_add_u32 s66, s62, 16384\n"
    "buffer_load_dwordx4 v245, s[40:43], s66 offen nt lds\n"
    "buffer_load_dwordx4 v245, s[40:43], s66 offen offset:2048 nt lds\n"
    "v_mfma_f32_32x32x16_bf16 v[144:159], v[196:199], v[100:103], v[144:159]\n"
    "ds_read_b128 v[220:223], v241 offset:26112\n"
    "s_add_u32 m0, s61, 50048\n"
    "s_nop 0\n"
    "buffer_load_dwordx4 v245, s[40:43], s66 offen offset:128 nt lds\n"
    "buffer_load_dwordx4 v245, s[40:43], s66 offen offset:2176 nt lds\n"
    "s_waitcnt lgkmcnt(4)\n"
    "v_mfma_f32_32x32x16_bf16 v[144:159], v[200:203], v[104:107], v[144:159]\n"
    "s_barrier\n"
    "ds_read_b64_tr_b16 v[192:193], v242 offset:0\n"
    "ds_read_b64_tr_b16 v[194:195], v242 offset:2048\n"
    "v_mfma_f32_32x32x16_bf16 v[144:159], v[204:207], v[108:111], v[144:159]\n"
    "ds_read_b64_tr_b16 v[196:197], v242 offset:512\n"
    "ds_read_b64_tr_b16 v[198:199], v242 offset:2560\n"
    "s_waitcnt lgkmcnt(6)\n"
    "v_mfma_f32_32x32x16_bf16 v[144:159], v[208:211], v[112:115], v[144:159]\n"
    "ds_read_b64_tr_b16 v[200:201], v242 offset:1024\n"
    "ds_read_b64_tr_b16 v[202:203], v242 offset:3072\n"
    "v_max3_f32 v249, v128, v129, v130\n"
    "v_max3_f32 v249, v249, v131, v132\n"
    "v_max3_f32 v249, v249, v133, v134\n"
    "v_max3_f32 v249, v249, v135, v136\n"
    "v_mfma_f32_32x32x16_bf16 v[144:159], v[212:215], v[116:119], v[144:159]\n"
    "ds_read_b64_tr_b16 v[204:205], v242 offset:1536\n"
    "ds_read_b64_tr_b16 v[206:207], v242 offset:3584\n"
    "v_max3_f32 v249, v249, v137, v138\n"
    "v_max3_f32 v249, v249, v139, v140\n"
    "v_max3_f32 v249, v249, v141, v142\n"
    "s_waitcnt lgkmcnt(8)\n"
    "v_mfma_f32_32x32x16_bf16 v[144:159], v[216:219], v[120:123], v[144:159]\n"
    "ds_read_b64_tr_b16 v[208:209], v242 offset:4096\n"
    "ds_read_b64_tr_b16 v[210:211], v242 offset:6144\n"
    "v_max_f32 v249, v249, v143\n"
    "v_mfma_f32_32x32x16_bf16 v[144:159], v[220:223], v[124:127], v[144:159]\n"
    "ds_read_b64_tr_b16 v[212:213], v242 offset:4608\n"
    "ds_read_b64_tr_b16 v[214:215], v242 offset:6656\n"
    "s_waitcnt lgkmcnt(8)\n"
    "v_mfma_f32_32x32x16_bf16 v[0:15], v[192:195], v[160:163], 0\n"
    "ds_read_b64_tr_b16 v[216:217], v242 offset:5120\n"
    "ds_read_b64_tr_b16 v[218:219], v242 offset:7168\n"
    "v_mfma_f32_32x32x16_bf16 v[16:31], v[196:199], v[160:163], 0\n"
    "ds_read_b64_tr_b16 v[220:221], v242 offset:5632\n"
    "ds_read_b64_tr_b16 v[222:223], v242 offset:7680\n"
    "s_waitcnt lgkmcnt(8)\n"
    "v_mfma_f32_32x32x16_bf16 v[32:47], v[200:203], v[160:163], 0\n"
    "ds_read_b64_tr_b16 v[192:193], v242 offset:8192\n"
    "ds_read_b64_tr_b16 v[194:195], v242 offset:10240\n"
    "v_max3_f32 v250, v144, v145, v146\n"
    "v_max3_f32 v250, v250, v147, v148\n"
    "v_max3_f32 v250, v250, v149, v150\n"
    "v_max3_f32 v250, v250, v151, v152\n"
    "v_max3_f32 v250, v250, v153, v154\n"
    "v_mfma_f32_32x32x16_bf16 v[48:63], v[204:207], v[160:163], 0\n"
    "ds_read_b64_tr_b16 v[196:197], v242 offset:8704\n"
    "ds_read_b64_tr_b16 v[198:199], v242 offset:10752\n"
    "v_max3_f32 v250, v250, v155, v156\n"
    "v_max3_f32 v250, v250, v157, v158\n"
    "v_max_f32 v250, v250, v159\n"
    "v_max_f32 v249, v249, v250\n"
    "s_waitcnt lgkmcnt(8)\n"
    "v_mfma_f32_32x32x16_bf16 v[0:15], v[208:211], v[164:167], v[0:15]\n"
    "ds_read_b64_tr_b16 v[200:201], v242 offset:9216\n"
    "ds_read_b64_tr_b16 v[202:203], v242 offset:11264\n"
    "v_mov_b32 v251, v249\n"
    "s_nop 1\n"
    "v_permlane32_swap_b32 v249, v251\n"
    "v_max_f32 v249, v249, v251\n"
    "v_cmp_lt_f32 vcc, 0x41000000, v249\n"
    "s_nop 0\n"
    "v_mfma_f32_32x32x16_bf16 v[16:31], v[212:215], v[164:167], v[16:31]\n"
    "ds_read_b64_tr_b16 v[204:205], v242 offset:9728\n"
    "ds_read_b64_tr_b16 v[206:207], v242 offset:11776\n"
    "s_cbranch_vccnz L_stub1_i0_1\n"
    "L_back1_i0_1:\n"
    "v_exp_f32 v128, v128\n"
    "s_waitcnt lgkmcnt(8)\n"
    "v_mfma_f32_32x32x16_bf16 v[32:47], v[216:219], v[164:167], v[32:47]\n"
    "ds_read_b64_tr_b16 v[208:209], v242 offset:12288\n"
    "ds_read_b64_tr_b16 v[210:211], v242 offset:14336\n"
    "v_exp_f32 v129, v129\n"
    "v_exp_f32 v130, v130\n"
    "v_mfma_f32_32x32x16_bf16 v[48:63], v[220:223], v[164:167], v[48:63]\n"
    "ds_read_b64_tr_b16 v[212:213], v242 offset:12800\n"
    "ds_read_b64_tr_b16 v[214:215], v242 offset:14848\n"
    "v_add_f32 v247, v247, v128\n"
    "v_exp_f32 v131, v131\n"
    "s_waitcnt lgkmcnt(8)\n"
    "v_mfma_f32_32x32x16_bf16 v[0:15], v[192:195], v[168:171], v[0:15]\n"
    "ds_read_b64_tr_b16 v[216:217], v242 offset:13312\n"
    "ds_read_b64_tr_b16 v[218:219], v242 offset:15360\n"
    "v_add_f32 v248, v248, v129\n"
    "v_exp_f32 v132, v132\n"
    "v_add_f32 v247, v247, v130\n"
    "v_mfma_f32_32x32x16_bf16 v[16:31], v[196:199], v[168:171], v[16:31]\n"
    "ds_read_b64_tr_b16 v[220:221], v242 offset:13824\n"
    "ds_read_b64_tr_b16 v[222:223], v242 offset:15872\n"
    "v_exp_f32 v133, v133\n"
    "v_add_f32 v248, v248, v131\n"
    "s_waitcnt lgkmcnt(8)\n"
    "v_mfma_f32_32x32x16_bf16 v[32:47], v[200:203], v[168:171], v[32:47]\n"
    "s_barrier\n"
    "v_exp_f32 v134, v134\n"
    "v_add_f32 v247, v247, v132\n"
    "v_exp_f32 v135, v135\n"
    "v_mfma_f32_32x32x16_bf16 v[48:63], v[204:207], v[168:171], v[48:63]\n"
    "s_add_u32 m0, s61, 16384\n"
    "s_add_u32 s66, s62, 49152\n"
    "buffer_load_dwordx4 v243, s[36:39], s66 offen nt lds\n"
    "buffer_load_dwordx4 v244, s[36:39], s66 offen offset:2048 nt lds\n"
    "v_add_f32 v248, v248, v133\n"
    "s_waitcnt lgkmcnt(4)\n"
    "v_mfma_f32_32x32x16_bf16 v[0:15], v[208:211], v[172:175], v[0:15]\n"
    "s_add_u32 m0, s61, 17280\n"
    "s_nop 0\n"
    "buffer_load_dwordx4 v243, s[36:39], s66 offen offset:128 nt lds\n"
    "buffer_load_dwordx4 v244, s[36:39], s66 offen offset:2176 nt lds\n"
    "v_exp_f32 v136, v136\n"
    "v_add_f32 v247, v247, v134\n"
    "v_mfma_f32_32x32x16_bf16 v[16:31], v[212:215], v[172:175], v[16:31]\n"
    "v_exp_f32 v137, v137\n"
    "v_add_f32 v248, v248, v135\n"
    "v_exp_f32 v138, v138\n"
    "s_waitcnt lgkmcnt(0)\n"
    "v_mfma_f32_32x32x16_bf16 v[32:47], v[216:219], v[172:175], v[32:47]\n"
    "v_add_f32 v247, v247, v136\n"
    "v_exp_f32 v139, v139\n"
    "v_add_f32 v248, v248, v137\n"
    "v_exp_f32 v140, v140\n"
    "v_mfma_f32_32x32x16_bf16 v[48:63], v[220:223], v[172:175], v[48:63]\n"
    "s_cmp_lg_u32 s69, 0\n"
    "s_cbranch_scc1 L_stub2_i0_1\n"
    "L_back2_i0_1:\n"
    "v_add_f32 v247, v247, v138\n"
    "v_exp_f32 v141, v141\n"
    "s_waitcnt vmcnt(8)\n"
    "s_barrier\n"
    "ds_read_b128 v[192:195], v240 offset:0\n"
    "ds_read_b128 v[196:199], v241 offset:0\n"
    "ds_read_b128 v[200:203], v240 offset:512\n"
    "ds_read_b128 v[204:207], v241 offset:512\n"
    "ds_read_b128 v[208:211], v240 offset:1024\n"
    "ds_read_b128 v[212:215], v241 offset:1024\n"
    "v_add_f32 v248, v248, v139\n"
    "v_exp_f32 v142, v142\n"
    "v_add_f32 v247, v247, v140\n"
    "v_exp_f32 v143, v143\n"
    "v_add_f32 v248, v248, v141\n"
    "v_add_f32 v247, v247, v142\n"
    "v_add_f32 v248, v248, v143\n"
    "v_cvt_pk_bf16_f32 v160, v128, v129\n"
    "v_cvt_pk_bf16_f32 v161, v130, v131\n"
    "v_cvt_pk_bf16_f32 v162, v132, v133\n"
    "v_cvt_pk_bf16_f32 v163, v134, v135\n"
    "v_cvt_pk_bf16_f32 v164, v136, v137\n"
    "v_cvt_pk_bf16_f32 v165, v138, v139\n"
    "v_cvt_pk_bf16_f32 v166, v140, v141\n"
    "v_cvt_pk_bf16_f32 v167, v142, v143\n"
    "v_exp_f32 v144, v144\n"
    "v_exp_f32 v145, v145\n"
    "v_exp_f32 v146, v146\n"
    "v_add_f32 v247, v247, v144\n"
    "v_exp_f32 v147, v147\n"
    "v_add_f32 v248, v248, v145\n"
    "v_exp_f32 v148, v148\n"
    "v_add_f32 v247, v247, v146\n"
    "v_exp_f32 v149, v149\n"
    "v_add_f32 v248, v248, v147\n"
    "v_exp_f32 v150, v150\n"
    "v_add_f32 v247, v247, v148\n"
    "v_exp_f32 v151, v151\n"
    "s_waitcnt lgkmcnt(4)\n"
    "v_mfma_f32_32x32x16_bf16 v[128:143], v[192:195], v[96:99], v[176:191]\n"
    "ds_read_b128 v[216:219], v240 offset:1536\n"
    "v_add_f32 v248, v248, v149\n"
    "v_exp_f32 v152, v152\n"
    "v_add_f32 v247, v247, v150\n"
    "v_mfma_f32_32x32x16_bf16 v[128:143], v[196:199], v[100:103], v[128:143]\n"
    "ds_read_b128 v[220:223], v241 offset:1536\n"
    "v_exp_f32 v153, v153\n"
    "v_add_f32 v248, v248, v151\n"
    "s_waitcnt lgkmcnt(4)\n"
    "v_mfma_f32_32x32x16_bf16 v[128:143], v[200:203], v[104:107], v[128:143]\n"
    "ds_read_b128 v[192:195], v240 offset:8192\n"
    "v_exp_f32 v154, v154\n"
    "v_add_f32 v247, v247, v152\n"
    "v_exp_f32 v155, v155\n"
    "v_mfma_f32_32x32x16_bf16 v[128:143], v[204:207], v[108:111], v[128:143]\n"
    "ds_read_b128 v[196:199], v241 offset:8192\n"
    "v_add_f32 v248, v248, v153\n"
    "v_exp_f32 v156, v156\n"
    "v_add_f32 v247, v247, v154\n"
    "s_waitcnt lgkmcnt(4)\n"
    "v_mfma_f32_32x32x16_bf16 v[128:143], v[208:211], v[112:115], v[128:143]\n"
    "ds_read_b128 v[200:203], v240 offset:8704\n"
    "v_exp_f32 v157, v157\n"
    "v_add_f32 v248, v248, v155\n"
    "v_exp_f32 v158, v158\n"
    "v_mfma_f32_32x32x16_bf16 v[128:143], v[212:215], v[116:119], v[128:143]\n"
    "ds_read_b128 v[204:207], v241 offset:8704\n"
    "v_add_f32 v247, v247, v156\n"
    "v_exp_f32 v159, v159\n"
    "v_add_f32 v248, v248, v157\n"
    "s_waitcnt lgkmcnt(4)\n"
    "v_mfma_f32_32x32x16_bf16 v[128:143], v[216:219], v[120:123], v[128:143]\n"
    "ds_read_b128 v[208:211], v240 offset:9216\n"
    "v_add_f32 v247, v247, v158\n"
    "v_add_f32 v248, v248, v159\n"
    "v_cvt_pk_bf16_f32 v168, v144, v145\n"
    "v_cvt_pk_bf16_f32 v169, v146, v147\n"
    "v_cvt_pk_bf16_f32 v170, v148, v149\n"
    "v_mfma_f32_32x32x16_bf16 v[128:143], v[220:223], v[124:127], v[128:143]\n"
    "ds_read_b128 v[212:215], v241 offset:9216\n"
    "v_cvt_pk_bf16_f32 v171, v150, v151\n"
    "v_cvt_pk_bf16_f32 v172, v152, v153\n"
    "v_cvt_pk_bf16_f32 v173, v154, v155\n"
    "v_cvt_pk_bf16_f32 v174, v156, v157\n"
    "v_cvt_pk_bf16_f32 v175, v158, v159\n"
    "s_waitcnt lgkmcnt(4)\n"
    "v_mfma_f32_32x32x16_bf16 v[144:159], v[192:195], v[96:99], v[176:191]\n"
    "ds_read_b128 v[216:219], v240 offset:9728\n"
    "s_add_u32 m0, s61, 32768\n"
    "s_add_u32 s66, s62, 32768\n"
    "buffer_load_dwordx4 v245, s[40:43], s66 offen nt lds\n"
    "buffer_load_dwordx4 v245, s[40:43], s66 offen offset:2048 nt lds\n"
    "v_mfma_f32_32x32x16_bf16 v[144:159], v[196:199], v[100:103], v[144:159]\n"
    "ds_read_b128 v[220:223], v241 offset:9728\n"
    "s_add_u32 m0, s61, 33664\n"
    "s_nop 0\n"
    "buffer_load_dwordx4 v245, s[40:43], s66 offen offset:128 nt lds\n"
    "buffer_load_dwordx4 v245, s[40:43], s66 offen offset:2176 nt lds\n"
    "s_waitcnt lgkmcnt(4)\n"
    "v_mfma_f32_32x32x16_bf16 v[144:159], v[200:203], v[104:107], v[144:159]\n"
    "s_waitcnt vmcnt(8)\n"
    "s_barrier\n"
    "ds_read_b64_tr_b16 v[192:193], v242 offset:16384\n"
    "ds_read_b64_tr_b16 v[194:195], v242 offset:18432\n"
    "v_mfma_f32_32x32x16_bf16 v[144:159], v[204:207], v[108:111], v[144:159]\n"
    "ds_read_b64_tr_b16 v[196:197], v242 offset:16896\n"
    "ds_read_b64_tr_b16 v[198:199], v242 offset:18944\n"
    "s_waitcnt lgkmcnt(6)\n"
    "v_mfma_f32_32x32x16_bf16 v[144:159], v[208:211], v[112:115], v[144:159]\n"
    "ds_read_b64_tr_b16 v[200:201], v242 offset:17408\n"
    "ds_read_b64_tr_b16 v[202:203], v242 offset:19456\n"
    "v_max3_f32 v249, v128, v129, v130\n"
    "v_max3_f32 v249, v249, v131, v132\n"
    "v_max3_f32 v249, v249, v133, v134\n"
    "v_max3_f32 v249, v249, v135, v136\n"
    "v_mfma_f32_32x32x16_bf16 v[144:159], v[212:215], v[116:119], v[144:159]\n"
    "ds_read_b64_tr_b16 v[204:205], v242 offset:17920\n"
    "ds_read_b64_tr_b16 v[206:207], v242 offset:19968\n"
    "v_max3_f32 v249, v249, v137, v138\n"
    "v_max3_f32 v249, v249, v139, v140\n"
    "v_max3_f32 v249, v249, v141, v142\n"
    "s_waitcnt lgkmcnt(8)\n"
    "v_mfma_f32_32x32x16_bf16 v[144:159], v[216:219], v[120:123], v[144:159]\n"
    "ds_read_b64_tr_b16 v[208:209], v242 offset:20480\n"
    "ds_read_b64_tr_b16 v[210:211], v242 offset:22528\n"
    "v_max_f32 v249, v249, v143\n"
    "v_mfma_f32_32x32x16_bf16 v[144:159], v[220:223], v[124:127], v[144:159]\n"
    "ds_read_b64_tr_b16 v[212:213], v242 offset:20992\n"
    "ds_read_b64_tr_b16 v[214:215], v242 offset:23040\n"
    "s_waitcnt lgkmcnt(8)\n"
    "v_mfma_f32_32x32x16_bf16 v[0:15], v[192:195], v[160:163], v[0:15]\n"
    "ds_read_b64_tr_b16 v[216:217], v242 offset:21504\n"
    "ds_read_b64_tr_b16 v[218:219], v242 offset:23552\n"
    "v_mfma_f32_32x32x16_bf16 v[16:31], v[196:199], v[160:163], v[16:31]\n"
    "ds_read_b64_tr_b16 v[220:221], v242 offset:22016\n"
    "ds_read_b64_tr_b16 v[222:223], v242 offset:24064\n"
    "s_waitcnt lgkmcnt(8)\n"
    "v_mfma_f32_32x32x16_bf16 v[32:47], v[200:203], v[160:163], v[32:47]\n"
    "ds_read_b64_tr_b16 v[192:193], v242 offset:24576\n"
    "ds_read_b64_tr_b16 v[194:195], v242 offset:26624\n"
    "v_max3_f32 v250, v144, v145, v146\n"
    "v_max3_f32 v250, v250, v147, v148\n"
    "v_max3_f32 v250, v250, v149, v150\n"
    "v_max3_f32 v250, v250, v151, v152\n"
    "v_max3_f32 v250, v250, v153, v154\n"
    "v_mfma_f32_32x32x16_bf16 v[48:63], v[204:207], v[160:163], v[48:63]\n"
    "ds_read_b64_tr_b16 v[196:197], v242 offset:25088\n"
    "ds_read_b64_tr_b16 v[198:199], v242 offset:27136\n"
    "v_max3_f32 v250, v250, v155, v156\n"
    "v_max3_f32 v250, v250, v157, v158\n"
    "v_max_f32 v250, v250, v159\n"
    "v_max_f32 v249, v249, v250\n"
    "s_waitcnt lgkmcnt(8)\n"
    "v_mfma_f32_32x32x16_bf16 v[0:15], v[208:211], v[164:167], v[0:15]\n"
    "ds_read_b64_tr_b16 v[200:201], v242 offset:25600\n"
    "ds_read_b64_tr_b16 v[202:203], v242 offset:27648\n"
    "v_mov_b32 v251, v249\n"
    "s_nop 1\n"
    "v_permlane32_swap_b32 v249, v251\n"
    "v_max_f32 v249, v249, v251\n"
    "v_cmp_lt_f32 vcc, 0x41000000, v249\n"
    "s_nop 0\n"
    "v_mfma_f32_32x32x16_bf16 v[16:31], v[212:215], v[164:167], v[16:31]\n"
    "ds_read_b64_tr_b16 v[204:205], v242 offset:26112\n"
    "ds_read_b64_tr_b16 v[206:207], v242 offset:28160\n"
    "s_cbranch_vccnz L_stub1_i0_2\n"
    "L_back1_i0_2:\n"
    "v_exp_f32 v128, v128\n"
    "s_waitcnt lgkmcnt(8)\n"
    "v_mfma_f32_32x32x16_bf16 v[32:47], v[216:219], v[164:167], v[32:47]\n"
    "ds_read_b64_tr_b16 v[208:209], v242 offset:28672\n"
    "ds_read_b64_tr_b16 v[210:211], v242 offset:30720\n"
    "v_exp_f32 v129, v129\n"
    "v_exp_f32 v130, v130\n"
    "v_mfma_f32_32x32x16_bf16 v[48:63], v[220:223], v[164:167], v[48:63]\n"
    "ds_read_b64_tr_b16 v[212:213], v242 offset:29184\n"
    "ds_read_b64_tr_b16 v[214:215], v242 offset:31232\n"
    "v_add_f32 v247, v247, v128\n"
    "v_exp_f32 v131, v131\n"
    "s_waitcnt lgkmcnt(8)\n"
    "v_mfma_f32_32x32x16_bf16 v[0:15], v[192:195], v[168:171], v[0:15]\n"
    "ds_read_b64_tr_b16 v[216:217], v242 offset:29696\n"
    "ds_read_b64_tr_b16 v[218:219], v242 offset:31744\n"
    "v_add_f32 v248, v248, v129\n"
    "v_exp_f32 v132, v132\n"
    "v_add_f32 v247, v247, v130\n"
    "v_mfma_f32_32x32x16_bf16 v[16:31], v[196:199], v[168:171], v[16:31]\n"
    "ds_read_b64_tr_b16 v[220:221], v242 offset:30208\n"
    "ds_read_b64_tr_b16 v[222:223], v242 offset:32256\n"
    "v_exp_f32 v133, v133\n"
    "v_add_f32 v248, v248, v131\n"
    "s_waitcnt lgkmcnt(8)\n"
    "v_mfma_f32_32x32x16_bf16 v[32:47], v[200:203], v[168:171], v[32:47]\n"
    "s_barrier\n"
    "v_exp_f32 v134, v134\n"
    "v_add_f32 v247, v247, v132\n"
    "v_exp_f32 v135, v135\n"
    "v_mfma_f32_32x32x16_bf16 v[48:63], v[204:207], v[168:171], v[48:63]\n"
    "s_add_u32 m0, s61, 0\n"
    "s_add_u32 s66, s62, 65536\n"
    "buffer_load_dwordx4 v243, s[36:39], s66 offen nt lds\n"
    "buffer_load_dwordx4 v244, s[36:39], s66 offen offset:2048 nt lds\n"
    "v_add_f32 v248, v248, v133\n"
    "s_waitcnt lgkmcnt(4)\n"
    "v_mfma_f32_32x32x16_bf16 v[0:15], v[208:211], v[172:175], v[0:15]\n"
    "s_add_u32 m0, s61, 896\n"
    "s_nop 0\n"
    "buffer_load_dwordx4 v243, s[36:39], s66 offen offset:128 nt lds\n"
    "buffer_load_dwordx4 v244, s[36:39], s66 offen offset:2176 nt lds\n"
    "v_exp_f32 v136, v136\n"
    "v_add_f32 v247, v247, v134\n"
    "v_mfma_f32_32x32x16_bf16 v[16:31], v[212:215], v[172:175], v[16:31]\n"
    "v_exp_f32 v137, v137\n"
    "v_add_f32 v248, v248, v135\n"
    "v_exp_f32 v138, v138\n"
    "s_waitcnt lgkmcnt(0)\n"
    "v_mfma_f32_32x32x16_bf16 v[32:47], v[216:219], v[172:175], v[32:47]\n"
    "v_add_f32 v247, v247, v136\n"
    "v_exp_f32 v139, v139\n"
    "v_add_f32 v248, v248, v137\n"
    "v_exp_f32 v140, v140\n"
    "v_mfma_f32_32x32x16_bf16 v[48:63], v[220:223], v[172:175], v[48:63]\n"
    "s_cmp_lg_u32 s69, 0\n"
    "s_cbranch_scc1 L_stub2_i0_2\n"
    "L_back2_i0_2:\n"
    "v_add_f32 v247, v247, v138\n"
    "v_exp_f32 v141, v141\n"
    "s_waitcnt vmcnt(8)\n"
    "s_barrier\n"
    "ds_read_b128 v[192:195], v240 offset:16384\n"
    "ds_read_b128 v[196:199], v241 offset:16384\n"
    "ds_read_b128 v[200:203], v240 offset:16896\n"
    "ds_read_b128 v[204:207], v241 offset:16896\n"
    "ds_read_b128 v[208:211], v240 offset:17408\n"
    "ds_read_b128 v[212:215], v241 offset:17408\n"
    "v_add_f32 v248, v248, v139\n"
    "v_exp_f32 v142, v142\n"
    "v_add_f32 v247, v247, v140\n"
    "v_exp_f32 v143, v143\n"
    "v_add_f32 v248, v248, v141\n"
    "v_add_f32 v247, v247, v142\n"
    "v_add_f32 v248, v248, v143\n"
    "v_cvt_pk_bf16_f32 v160, v128, v129\n"
    "v_cvt_pk_bf16_f32 v161, v130, v131\n"
    "v_cvt_pk_bf16_f32 v162, v132, v133\n"
    "v_cvt_pk_bf16_f32 v163, v134, v135\n"
    "v_cvt_pk_bf16_f32 v164, v136, v137\n"
    "v_cvt_pk_bf16_f32 v165, v138, v139\n"
    "v_cvt_pk_bf16_f32 v166, v140, v141\n"
    "v_cvt_pk_bf16_f32 v167, v142, v143\n"
    "v_exp_f32 v144, v144\n"
    "v_exp_f32 v145, v145\n"
    "v_exp_f32 v146, v146\n"
    "v_add_f32 v247, v247, v144\n"
    "v_exp_f32 v147, v147\n"
    "v_add_f32 v248, v248, v145\n"
    "v_exp_f32 v148, v148\n"
    "v_add_f32 v247, v247, v146\n"
    "v_exp_f32 v149, v149\n"
    "v_add_f32 v248, v248, v147\n"
    "v_exp_f32 v150, v150\n"
    "v_add_f32 v247, v247, v148\n"
    "v_exp_f32 v151, v151\n"
    "s_waitcnt lgkmcnt(4)\n"
    "v_mfma_f32_32x32x16_bf16 v[128:143], v[192:195], v[96:99], v[176:191]\n"
    "ds_read_b128 v[216:219], v240 offset:17920\n"
    "v_add_f32 v248, v248, v149\n"
    "v_exp_f32 v152, v152\n"
    "v_add_f32 v247, v247, v150\n"
    "v_mfma_f32_32x32x16_bf16 v[128:143], v[196:199], v[100:103], v[128:143]\n"
    "ds_read_b128 v[220:223], v241 offset:17920\n"
    "v_exp_f32 v153, v153\n"
    "v_add_f32 v248, v248, v151\n"
    "s_waitcnt lgkmcnt(4)\n"
    "v_mfma_f32_32x32x16_bf16 v[128:143], v[200:203], v[104:107], v[128:143]\n"
    "ds_read_b128 v[192:195], v240 offset:24576\n"
    "v_exp_f32 v154, v154\n"
    "v_add_f32 v247, v247, v152\n"
    "v_exp_f32 v155, v155\n"
    "v_mfma_f32_32x32x16_bf16 v[128:143], v[204:207], v[108:111], v[128:143]\n"
    "ds_read_b128 v[196:199], v241 offset:24576\n"
    "v_add_f32 v248, v248, v153\n"
    "v_exp_f32 v156, v156\n"
    "v_add_f32 v247, v247, v154\n"
    "s_waitcnt lgkmcnt(4)\n"
    "v_mfma_f32_32x32x16_bf16 v[128:143], v[208:211], v[112:115], v[128:143]\n"
    "ds_read_b128 v[200:203], v240 offset:25088\n"
    "v_exp_f32 v157, v157\n"
    "v_add_f32 v248, v248, v155\n"
    "v_exp_f32 v158, v158\n"
    "v_mfma_f32_32x32x16_bf16 v[128:143], v[212:215], v[116:119], v[128:143]\n"
    "ds_read_b128 v[204:207], v241 offset:25088\n"
    "v_add_f32 v247, v247, v156\n"
    "v_exp_f32 v159, v159\n"
    "v_add_f32 v248, v248, v157\n"
    "s_waitcnt lgkmcnt(4)\n"
    "v_mfma_f32_32x32x16_bf16 v[128:143], v[216:219], v[120:123], v[128:143]\n"
    "ds_read_b128 v[208:211], v240 offset:25600\n"
    "v_add_f32 v247, v247, v158\n"
    "v_add_f32 v248, v248, v159\n"
    "v_cvt_pk_bf16_f32 v168, v144, v145\n"
    "v_cvt_pk_bf16_f32 v169, v146, v147\n"
    "v_cvt_pk_bf16_f32 v170, v148, v149\n"
    "v_mfma_f32_32x32x16_bf16 v[128:143], v[220:223], v[124:127], v[128:143]\n"
    "ds_read_b128 v[212:215], v241 offset:25600\n"
    "v_cvt_pk_bf16_f32 v171, v150, v151\n"
    "v_cvt_pk_bf16_f32 v172, v152, v153\n"
    "v_cvt_pk_bf16_f32 v173, v154, v155\n"
    "v_cvt_pk_bf16_f32 v174, v156, v157\n"
    "v_cvt_pk_bf16_f32 v175, v158, v159\n"
    "s_waitcnt lgkmcnt(4)\n"
    "v_mfma_f32_32x32x16_bf16 v[144:159], v[192:195], v[96:99], v[176:191]\n"
    "ds_read_b128 v[216:219], v240 offset:26112\n"
    "s_add_u32 m0, s61, 49152\n"
    "s_add_u32 s66, s62, 49152\n"
    "buffer_load_dwordx4 v245, s[40:43], s66 offen nt lds\n"
    "buffer_load_dwordx4 v245, s[40:43], s66 offen offset:2048 nt lds\n"
    "v_mfma_f32_32x32x16_bf16 v[144:159], v[196:199], v[100:103], v[144:159]\n"
    "ds_read_b128 v[220:223], v241 offset:26112\n"
    "s_add_u32 m0, s61, 50048\n"
    "s_nop 0\n"
    "buffer_load_dwordx4 v245, s[40:43], s66 offen offset:128 nt lds\n"
    "buffer_load_dwordx4 v245, s[40:43], s66 offen offset:2176 nt lds\n"
    "s_waitcnt lgkmcnt(4)\n"
    "v_mfma_f32_32x32x16_bf16 v[144:159], v[200:203], v[104:107], v[144:159]\n"
    "s_waitcnt vmcnt(8)\n"
    "s_barrier\n"
    "ds_read_b64_tr_b16 v[192:193], v242 offset:0\n"
    "ds_read_b64_tr_b16 v[194:195], v242 offset:2048\n"
    "v_mfma_f32_32x32x16_bf16 v[144:159], v[204:207], v[108:111], v[144:159]\n"
    "ds_read_b64_tr_b16 v[196:197], v242 offset:512\n"
    "ds_read_b64_tr_b16 v[198:199], v242 offset:2560\n"
    "s_waitcnt lgkmcnt(6)\n"
    "v_mfma_f32_32x32x16_bf16 v[144:159], v[208:211], v[112:115], v[144:159]\n"
    "ds_read_b64_tr_b16 v[200:201], v242 offset:1024\n"
    "ds_read_b64_tr_b16 v[202:203], v242 offset:3072\n"
    "v_max3_f32 v249, v128, v129, v130\n"
    "v_max3_f32 v249, v249, v131, v132\n"
    "v_max3_f32 v249, v249, v133, v134\n"
    "v_max3_f32 v249, v249, v135, v136\n"
    "v_mfma_f32_32x32x16_bf16 v[144:159], v[212:215], v[116:119], v[144:159]\n"
    "ds_read_b64_tr_b16 v[204:205], v242 offset:1536\n"
    "ds_read_b64_tr_b16 v[206:207], v242 offset:3584\n"
    "v_max3_f32 v249, v249, v137, v138\n"
    "v_max3_f32 v249, v249, v139, v140\n"
    "v_max3_f32 v249, v249, v141, v142\n"
    "s_waitcnt lgkmcnt(8)\n"
    "v_mfma_f32_32x32x16_bf16 v[144:159], v[216:219], v[120:123], v[144:159]\n"
    "ds_read_b64_tr_b16 v[208:209], v242 offset:4096\n"
    "ds_read_b64_tr_b16 v[210:211], v242 offset:6144\n"
    "v_max_f32 v249, v249, v143\n"
    "v_mfma_f32_32x32x16_bf16 v[144:159], v[220:223], v[124:127], v[144:159]\n"
    "ds_read_b64_tr_b16 v[212:213], v242 offset:4608\n"
    "ds_read_b64_tr_b16 v[214:215], v242 offset:6656\n"
    "s_waitcnt lgkmcnt(8)\n"
    "v_mfma_f32_32x32x16_bf16 v[0:15], v[192:195], v[160:163], v[0:15]\n"
    "ds_read_b64_tr_b16 v[216:217], v242 offset:5120\n"
    "ds_read_b64_tr_b16 v[218:219], v242 offset:7168\n"
    "v_mfma_f32_32x32x16_bf16 v[16:31], v[196:199], v[160:163], v[16:31]\n"
    "ds_read_b64_tr_b16 v[220:221], v242 offset:5632\n"
    "ds_read_b64_tr_b16 v[222:223], v242 offset:7680\n"
    "s_waitcnt lgkmcnt(8)\n"
    "v_mfma_f32_32x32x16_bf16 v[32:47], v[200:203], v[160:163], v[32:47]\n"
    "ds_read_b64_tr_b16 v[192:193], v242 offset:8192\n"
    "ds_read_b64_tr_b16 v[194:195], v242 offset:10240\n"
    "v_max3_f32 v250, v144, v145, v146\n"
    "v_max3_f32 v250, v250, v147, v148\n"
    "v_max3_f32 v250, v250, v149, v150\n"
    "v_max3_f32 v250, v250, v151, v152\n"
    "v_max3_f32 v250, v250, v153, v154\n"
    "v_mfma_f32_32x32x16_bf16 v[48:63], v[204:207], v[160:163], v[48:63]\n"
    "ds_read_b64_tr_b16 v[196:197], v242 offset:8704\n"
    "ds_read_b64_tr_b16 v[198:199], v242 offset:10752\n"
    "v_max3_f32 v250, v250, v155, v156\n"
    "v_max3_f32 v250, v250, v157, v158\n"
    "v_max_f32 v250, v250, v159\n"
    "v_max_f32 v249, v249, v250\n"
    "s_waitcnt lgkmcnt(8)\n"
    "v_mfma_f32_32x32x16_bf16 v[0:15], v[208:211], v[164:167], v[0:15]\n"
    "ds_read_b64_tr_b16 v[200:201], v242 offset:9216\n"
    "ds_read_b64_tr_b16 v[202:203], v242 offset:11264\n"
    "v_mov_b32 v251, v249\n"
    "s_nop 1\n"
    "v_permlane32_swap_b32 v249, v251\n"
    "v_max_f32 v249, v249, v251\n"
    "v_cmp_lt_f32 vcc, 0x41000000, v249\n"
    "s_nop 0\n"
    "v_mfma_f32_32x32x16_bf16 v[16:31], v[212:215], v[164:167], v[16:31]\n"
    "ds_read_b64_tr_b16 v[204:205], v242 offset:9728\n"
    "ds_read_b64_tr_b16 v[206:207], v242 offset:11776\n"
    "s_cbranch_vccnz L_stub1_i0_3\n"
    "L_back1_i0_3:\n"
    "v_exp_f32 v128, v128\n"
    "s_waitcnt lgkmcnt(8)\n"
    "v_mfma_f32_32x32x16_bf16 v[32:47], v[216:219], v[164:167], v[32:47]\n"
    "ds_read_b64_tr_b16 v[208:209], v242 offset:12288\n"
    "ds_read_b64_tr_b16 v[210:211], v242 offset:14336\n"
    "v_exp_f32 v129, v129\n"
    "v_exp_f32 v130, v130\n"
    "v_mfma_f32_32x32x16_bf16 v[48:63], v[220:223], v[164:167], v[48:63]\n"
    "ds_read_b64_tr_b16 v[212:213], v242 offset:12800\n"
    "ds_read_b64_tr_b16 v[214:215], v242 offset:14848\n"
    "v_add_f32 v247, v247, v128\n"
    "v_exp_f32 v131, v131\n"
    "s_waitcnt lgkmcnt(8)\n"
    "v_mfma_f32_32x32x16_bf16 v[0:15], v[192:195], v[168:171], v[0:15]\n"
    "ds_read_b64_tr_b16 v[216:217], v242 offset:13312\n"
    "ds_read_b64_tr_b16 v[218:219], v242 offset:15360\n"
    "v_add_f32 v248, v248, v129\n"
    "v_exp_f32 v132, v132\n"
    "v_add_f32 v247, v247, v130\n"
    "v_mfma_f32_32x32x16_bf16 v[16:31], v[196:199], v[168:171], v[16:31]\n"
    "ds_read_b64_tr_b16 v[220:221], v242 offset:13824\n"
    "ds_read_b64_tr_b16 v[222:223], v242 offset:15872\n"
    "v_exp_f32 v133, v133\n"
    "v_add_f32 v248, v248, v131\n"
    "s_waitcnt lgkmcnt(8)\n"
    "v_mfma_f32_32x32x16_bf16 v[32:47], v[200:203], v[168:171], v[32:47]\n"
    "s_barrier\n"
    "v_exp_f32 v134, v134\n"
    "v_add_f32 v247, v247, v132\n"
    "v_exp_f32 v135, v135\n"
    "v_mfma_f32_32x32x16_bf16 v[48:63], v[204:207], v[168:171], v[48:63]\n"
    "s_add_u32 m0, s61, 16384\n"
    "s_add_u32 s66, s62, 81920\n"
    "buffer_load_dwordx4 v243, s[36:39], s66 offen nt lds\n"
    "buffer_load_dwordx4 v244, s[36:39], s66 offen offset:2048 nt lds\n"
    "v_add_f32 v248, v248, v133\n"
    "s_waitcnt lgkmcnt(4)\n"
    "v_mfma_f32_32x32x16_bf16 v[0:15], v[208:211], v[172:175], v[0:15]\n"
    "s_add_u32 m0, s61, 17280\n"
    "s_nop 0\n"
    "buffer_load_dwordx4 v243, s[36:39], s66 offen offset:128 nt lds\n"
    "buffer_load_dwordx4 v244, s[36:39], s66 offen offset:2176 nt lds\n"
    "v_exp_f32 v136, v136\n"
    "v_add_f32 v247, v247, v134\n"
    "v_mfma_f32_32x32x16_bf16 v[16:31], v[212:215], v[172:175], v[16:31]\n"
    "v_exp_f32 v137, v137\n"
    "v_add_f32 v248, v248, v135\n"
    "v_exp_f32 v138, v138\n"
    "s_waitcnt lgkmcnt(0)\n"
    "v_mfma_f32_32x32x16_bf16 v[32:47], v[216:219], v[172:175], v[32:47]\n"
    "v_add_f32 v247, v247, v136\n"
    "v_exp_f32 v139, v139\n"
    "v_add_f32 v248, v248, v137\n"
    "v_exp_f32 v140, v140\n"
    "v_mfma_f32_32x32x16_bf16 v[48:63], v[220:223], v[172:175], v[48:63]\n"
    "s_cmp_lg_u32 s69, 0\n"
    "s_cbranch_scc1 L_stub2_i0_3\n"
    "L_back2_i0_3:\n"
    "v_add_f32 v247, v247, v138\n"
    "v_exp_f32 v141, v141\n"
    "s_waitcnt vmcnt(8)\n"
    "s_barrier\n"
    "ds_read_b128 v[192:195], v240 offset:0\n"
    "ds_read_b128 v[196:199], v241 offset:0\n"
    "ds_read_b128 v[200:203], v240 offset:512\n"
    "ds_read_b128 v[204:207], v241 offset:512\n"
    "ds_read_b128 v[208:211], v240 offset:1024\n"
    "ds_read_b128 v[212:215], v241 offset:1024\n"
    "v_add_f32 v248, v248, v139\n"
    "v_exp_f32 v142, v142\n"
    "v_add_f32 v247, v247, v140\n"
    "v_exp_f32 v143, v143\n"
    "v_add_f32 v248, v248, v141\n"
    "v_add_f32 v247, v247, v142\n"
    "v_add_f32 v248, v248, v143\n"
    "v_cvt_pk_bf16_f32 v160, v128, v129\n"
    "v_cvt_pk_bf16_f32 v161, v130, v131\n"
    "v_cvt_pk_bf16_f32 v162, v132, v133\n"
    "v_cvt_pk_bf16_f32 v163, v134, v135\n"
    "v_cvt_pk_bf16_f32 v164, v136, v137\n"
    "v_cvt_pk_bf16_f32 v165, v138, v139\n"
    "v_cvt_pk_bf16_f32 v166, v140, v141\n"
    "v_cvt_pk_bf16_f32 v167, v142, v143\n"
    "v_exp_f32 v144, v144\n"
    "v_exp_f32 v145, v145\n"
    "v_exp_f32 v146, v146\n"
    "v_add_f32 v247, v247, v144\n"
    "v_exp_f32 v147, v147\n"
    "v_add_f32 v248, v248, v145\n"
    "v_exp_f32 v148, v148\n"
    "v_add_f32 v247, v247, v146\n"
    "v_exp_f32 v149, v149\n"
    "v_add_f32 v248, v248, v147\n"
    "v_exp_f32 v150, v150\n"
    "v_add_f32 v247, v247, v148\n"
    "v_exp_f32 v151, v151\n"
    "s_waitcnt lgkmcnt(4)\n"
    "v_mfma_f32_32x32x16_bf16 v[128:143], v[192:195], v[96:99], v[176:191]\n"
    "ds_read_b128 v[216:219], v240 offset:1536\n"
    "v_add_f32 v248, v248, v149\n"
    "v_exp_f32 v152, v152\n"
    "v_add_f32 v247, v247, v150\n"
    "v_mfma_f32_32x32x16_bf16 v[128:143], v[196:199], v[100:103], v[128:143]\n"
    "ds_read_b128 v[220:223], v241 offset:1536\n"
    "v_exp_f32 v153, v153\n"
    "v_add_f32 v248, v248, v151\n"
    "s_waitcnt lgkmcnt(4)\n"
    "v_mfma_f32_32x32x16_bf16 v[128:143], v[200:203], v[104:107], v[128:143]\n"
    "ds_read_b128 v[192:195], v240 offset:8192\n"
    "v_exp_f32 v154, v154\n"
    "v_add_f32 v247, v247, v152\n"
    "v_exp_f32 v155, v155\n"
    "v_mfma_f32_32x32x16_bf16 v[128:143], v[204:207], v[108:111], v[128:143]\n"
    "ds_read_b128 v[196:199], v241 offset:8192\n"
    "v_add_f32 v248, v248, v153\n"
    "v_exp_f32 v156, v156\n"
    "v_add_f32 v247, v247, v154\n"
    "s_waitcnt lgkmcnt(4)\n"
    "v_mfma_f32_32x32x16_bf16 v[128:143], v[208:211], v[112:115], v[128:143]\n"
    "ds_read_b128 v[200:203], v240 offset:8704\n"
    "v_exp_f32 v157, v157\n"
    "v_add_f32 v248, v248, v155\n"
    "v_exp_f32 v158, v158\n"
    "v_mfma_f32_32x32x16_bf16 v[128:143], v[212:215], v[116:119], v[128:143]\n"
    "ds_read_b128 v[204:207], v241 offset:8704\n"
    "v_add_f32 v247, v247, v156\n"
    "v_exp_f32 v159, v159\n"
    "v_add_f32 v248, v248, v157\n"
    "s_waitcnt lgkmcnt(4)\n"
    "v_mfma_f32_32x32x16_bf16 v[128:143], v[216:219], v[120:123], v[128:143]\n"
    "ds_read_b128 v[208:211], v240 offset:9216\n"
    "v_add_f32 v247, v247, v158\n"
    "v_add_f32 v248, v248, v159\n"
    "v_cvt_pk_bf16_f32 v168, v144, v145\n"
    "v_cvt_pk_bf16_f32 v169, v146, v147\n"
    "v_cvt_pk_bf16_f32 v170, v148, v149\n"
    "v_mfma_f32_32x32x16_bf16 v[128:143], v[220:223], v[124:127], v[128:143]\n"
    "ds_read_b128 v[212:215], v241 offset:9216\n"
    "v_cvt_pk_bf16_f32 v171, v150, v151\n"
    "v_cvt_pk_bf16_f32 v172, v152, v153\n"
    "v_cvt_pk_bf16_f32 v173, v154, v155\n"
    "v_cvt_pk_bf16_f32 v174, v156, v157\n"
    "v_cvt_pk_bf16_f32 v175, v158, v159\n"
    "s_waitcnt lgkmcnt(4)\n"
    "v_mfma_f32_32x32x16_bf16 v[144:159], v[192:195], v[96:99], v[176:191]\n"
    "ds_read_b128 v[216:219], v240 offset:9728\n"
    "s_add_u32 m0, s61, 32768\n"
    "s_add_u32 s66, s62, 65536\n"
    "buffer_load_dwordx4 v245, s[40:43], s66 offen nt lds\n"
    "buffer_load_dwordx4 v245, s[40:43], s66 offen offset:2048 nt lds\n"
    "v_mfma_f32_32x32x16_bf16 v[144:159], v[196:199], v[100:103], v[144:159]\n"
    "ds_read_b128 v[220:223], v241 offset:9728\n"
    "s_add_u32 m0, s61, 33664\n"
    "s_nop 0\n"
    "buffer_load_dwordx4 v245, s[40:43], s66 offen offset:128 nt lds\n"
    "buffer_load_dwordx4 v245, s[40:43], s66 offen offset:2176 nt lds\n"
    "s_waitcnt lgkmcnt(4)\n"
    "v_mfma_f32_32x32x16_bf16 v[144:159], v[200:203], v[104:107], v[144:159]\n"
    "s_waitcnt vmcnt(8)\n"
    "s_barrier\n"
    "ds_read_b64_tr_b16 v[192:193], v242 offset:16384\n"
    "ds_read_b64_tr_b16 v[194:195], v242 offset:18432\n"
    "v_mfma_f32_32x32x16_bf16 v[144:159], v[204:207], v[108:111], v[144:159]\n"
    "ds_read_b64_tr_b16 v[196:197], v242 offset:16896\n"
    "ds_read_b64_tr_b16 v[198:199], v242 offset:18944\n"
    "s_waitcnt lgkmcnt(6)\n"
    "v_mfma_f32_32x32x16_bf16 v[144:159], v[208:211], v[112:115], v[144:159]\n"
    "ds_read_b64_tr_b16 v[200:201], v242 offset:17408\n"
    "ds_read_b64_tr_b16 v[202:203], v242 offset:19456\n"
    "v_max3_f32 v249, v128, v129, v130\n"
    "v_max3_f32 v249, v249, v131, v132\n"
    "v_max3_f32 v249, v249, v133, v134\n"
    "v_max3_f32 v249, v249, v135, v136\n"
    "v_mfma_f32_32x32x16_bf16 v[144:159], v[212:215], v[116:119], v[144:159]\n"
    "ds_read_b64_tr_b16 v[204:205], v242 offset:17920\n"
    "ds_read_b64_tr_b16 v[206:207], v242 offset:19968\n"
    "v_max3_f32 v249, v249, v137, v138\n"
    "v_max3_f32 v249, v249, v139, v140\n"
    "v_max3_f32 v249, v249, v141, v142\n"
    "s_waitcnt lgkmcnt(8)\n"
    "v_mfma_f32_32x32x16_bf16 v[144:159], v[216:219], v[120:123], v[144:159]\n"
    "ds_read_b64_tr_b16 v[208:209], v242 offset:20480\n"
    "ds_read_b64_tr_b16 v[210:211], v242 offset:22528\n"
    "v_max_f32 v249, v249, v143\n"
    "v_mfma_f32_32x32x16_bf16 v[144:159], v[220:223], v[124:127], v[144:159]\n"
    "ds_read_b64_tr_b16 v[212:213], v242 offset:20992\n"
    "ds_read_b64_tr_b16 v[214:215], v242 offset:23040\n"
    "s_waitcnt lgkmcnt(8)\n"
    "v_mfma_f32_32x32x16_bf16 v[0:15], v[192:195], v[160:163], v[0:15]\n"
    "ds_read_b64_tr_b16 v[216:217], v242 offset:21504\n"
    "ds_read_b64_tr_b16 v[218:219], v242 offset:23552\n"
    "v_mfma_f32_32x32x16_bf16 v[16:31], v[196:199], v[160:163], v[16:31]\n"
    "ds_read_b64_tr_b16 v[220:221], v242 offset:22016\n"
    "ds_read_b64_tr_b16 v[222:223], v242 offset:24064\n"
    "s_waitcnt lgkmcnt(8)\n"
    "v_mfma_f32_32x32x16_bf16 v[32:47], v[200:203], v[160:163], v[32:47]\n"
    "ds_read_b64_tr_b16 v[192:193], v242 offset:24576\n"
    "ds_read_b64_tr_b16 v[194:195], v242 offset:26624\n"
    "v_max3_f32 v250, v144, v145, v146\n"
    "v_max3_f32 v250, v250, v147, v148\n"
    "v_max3_f32 v250, v250, v149, v150\n"
    "v_max3_f32 v250, v250, v151, v152\n"
    "v_max3_f32 v250, v250, v153, v154\n"
    "v_mfma_f32_32x32x16_bf16 v[48:63], v[204:207], v[160:163], v[48:63]\n"
    "ds_read_b64_tr_b16 v[196:197], v242 offset:25088\n"
    "ds_read_b64_tr_b16 v[198:199], v242 offset:27136\n"
    "v_max3_f32 v250, v250, v155, v156\n"
    "v_max3_f32 v250, v250, v157, v158\n"
    "v_max_f32 v250, v250, v159\n"
    "v_max_f32 v249, v249, v250\n"
    "s_waitcnt lgkmcnt(8)\n"
    "v_mfma_f32_32x32x16_bf16 v[0:15], v[208:211], v[164:167], v[0:15]\n"
    "ds_read_b64_tr_b16 v[200:201], v242 offset:25600\n"
    "ds_read_b64_tr_b16 v[202:203], v242 offset:27648\n"
    "v_mov_b32 v251, v249\n"
    "s_nop 1\n"
    "v_permlane32_swap_b32 v249, v251\n"
    "v_max_f32 v249, v249, v251\n"
    "v_cmp_lt_f32 vcc, 0x41000000, v249\n"
    "s_nop 0\n"
    "v_mfma_f32_32x32x16_bf16 v[16:31], v[212:215], v[164:167], v[16:31]\n"
    "ds_read_b64_tr_b16 v[204:205], v242 offset:26112\n"
    "ds_read_b64_tr_b16 v[206:207], v242 offset:28160\n"
    "s_cbranch_vccnz L_stub1_i0_4\n"
    "L_back1_i0_4:\n"
    "v_exp_f32 v128, v128\n"
    "s_waitcnt lgkmcnt(8)\n"
    "v_mfma_f32_32x32x16_bf16 v[32:47], v[216:219], v[164:167], v[32:47]\n"
    "ds_read_b64_tr_b16 v[208:209], v242 offset:28672\n"
    "ds_read_b64_tr_b16 v[210:211], v242 offset:30720\n"
    "v_exp_f32 v129, v129\n"
    "v_exp_f32 v130, v130\n"
    "v_mfma_f32_32x32x16_bf16 v[48:63], v[220:223], v[164:167], v[48:63]\n"
    "ds_read_b64_tr_b16 v[212:213], v242 offset:29184\n"
    "ds_read_b64_tr_b16 v[214:215], v242 offset:31232\n"
    "v_add_f32 v247, v247, v128\n"
    "v_exp_f32 v131, v131\n"
    "s_waitcnt lgkmcnt(8)\n"
    "v_mfma_f32_32x32x16_bf16 v[0:15], v[192:195], v[168:171], v[0:15]\n"
    "ds_read_b64_tr_b16 v[216:217], v242 offset:29696\n"
    "ds_read_b64_tr_b16 v[218:219], v242 offset:31744\n"
    "v_add_f32 v248, v248, v129\n"
    "v_exp_f32 v132, v132\n"
    "v_add_f32 v247, v247, v130\n"
    "v_mfma_f32_32x32x16_bf16 v[16:31], v[196:199], v[168:171], v[16:31]\n"
    "ds_read_b64_tr_b16 v[220:221], v242 offset:30208\n"
    "ds_read_b64_tr_b16 v[222:223], v242 offset:32256\n"
    "v_exp_f32 v133, v133\n"
    "v_add_f32 v248, v248, v131\n"
    "s_waitcnt lgkmcnt(8)\n"
    "v_mfma_f32_32x32x16_bf16 v[32:47], v[200:203], v[168:171], v[32:47]\n"
    "s_barrier\n"
    "v_exp_f32 v134, v134\n"
    "v_add_f32 v247, v247, v132\n"
    "v_exp_f32 v135, v135\n"
    "v_mfma_f32_32x32x16_bf16 v[48:63], v[204:207], v[168:171], v[48:63]\n"
    "s_add_u32 m0, s61, 0\n"
    "s_add_u32 s66, s62, 98304\n"
    "buffer_load_dwordx4 v243, s[36:39], s66 offen nt lds\n"
    "buffer_load_dwordx4 v244, s[36:39], s66 offen offset:2048 nt lds\n"
    "v_add_f32 v248, v248, v133\n"
    "s_waitcnt lgkmcnt(4)\n"
    "v_mfma_f32_32x32x16_bf16 v[0:15], v[208:211], v[172:175], v[0:15]\n"
    "s_add_u32 m0, s61, 896\n"
    "s_nop 0\n"
    "buffer_load_dwordx4 v243, s[36:39], s66 offen offset:128 nt lds\n"
    "buffer_load_dwordx4 v244, s[36:39], s66 offen offset:2176 nt lds\n"
    "v_exp_f32 v136, v136\n"
    "v_add_f32 v247, v247, v134\n"
    "v_mfma_f32_32x32x16_bf16 v[16:31], v[212:215], v[172:175], v[16:31]\n"
    "v_exp_f32 v137, v137\n"
    "v_add_f32 v248, v248, v135\n"
    "v_exp_f32 v138, v138\n"
    "s_waitcnt lgkmcnt(0)\n"
    "v_mfma_f32_32x32x16_bf16 v[32:47], v[216:219], v[172:175], v[32:47]\n"
    "v_add_f32 v247, v247, v136\n"
    "v_exp_f32 v139, v139\n"
    "v_add_f32 v248, v248, v137\n"
    "v_exp_f32 v140, v140\n"
    "v_mfma_f32_32x32x16_bf16 v[48:63], v[220:223], v[172:175], v[48:63]\n"
    "s_cmp_lg_u32 s69, 0\n"
    "s_cbranch_scc1 L_stub2_i0_4\n"
    "L_back2_i0_4:\n"
    "v_add_f32 v247, v247, v138\n"
    "v_exp_f32 v141, v141\n"
    "s_waitcnt vmcnt(8)\n"
    "s_barrier\n"
    "ds_read_b128 v[192:195], v240 offset:16384\n"
    "ds_read_b128 v[196:199], v241 offset:16384\n"
    "ds_read_b128 v[200:203], v240 offset:16896\n"
    "ds_read_b128 v[204:207], v241 offset:16896\n"
    "ds_read_b128 v[208:211], v240 offset:17408\n"
    "ds_read_b128 v[212:215], v241 offset:17408\n"
    "v_add_f32 v248, v248, v139\n"
    "v_exp_f32 v142, v142\n"
    "v_add_f32 v247, v247, v140\n"
    "v_exp_f32 v143, v143\n"
    "v_add_f32 v248, v248, v141\n"
    "v_add_f32 v247, v247, v142\n"
    "v_add_f32 v248, v248, v143\n"
    "v_cvt_pk_bf16_f32 v160, v128, v129\n"
    "v_cvt_pk_bf16_f32 v161, v130, v131\n"
    "v_cvt_pk_bf16_f32 v162, v132, v133\n"
    "v_cvt_pk_bf16_f32 v163, v134, v135\n"
    "v_cvt_pk_bf16_f32 v164, v136, v137\n"
    "v_cvt_pk_bf16_f32 v165, v138, v139\n"
    "v_cvt_pk_bf16_f32 v166, v140, v141\n"
    "v_cvt_pk_bf16_f32 v167, v142, v143\n"
    "v_exp_f32 v144, v144\n"
    "v_exp_f32 v145, v145\n"
    "v_exp_f32 v146, v146\n"
    "v_add_f32 v247, v247, v144\n"
    "v_exp_f32 v147, v147\n"
    "v_add_f32 v248, v248, v145\n"
    "v_exp_f32 v148, v148\n"
    "v_add_f32 v247, v247, v146\n"
    "v_exp_f32 v149, v149\n"
    "v_add_f32 v248, v248, v147\n"
    "v_exp_f32 v150, v150\n"
    "v_add_f32 v247, v247, v148\n"
    "v_exp_f32 v151, v151\n"
    "s_waitcnt lgkmcnt(4)\n"
    "v_mfma_f32_32x32x16_bf16 v[128:143], v[192:195], v[96:99], v[176:191]\n"
    "ds_read_b128 v[216:219], v240 offset:17920\n"
    "v_add_f32 v248, v248, v149\n"
    "v_exp_f32 v152, v152\n"
    "v_add_f32 v247, v247, v150\n"
    "v_mfma_f32_32x32x16_bf16 v[128:143], v[196:199], v[100:103], v[128:143]\n"
    "ds_read_b128 v[220:223], v241 offset:17920\n"
    "v_exp_f32 v153, v153\n"
    "v_add_f32 v248, v248, v151\n"
    "s_waitcnt lgkmcnt(4)\n"
    "v_mfma_f32_32x32x16_bf16 v[128:143], v[200:203], v[104:107], v[128:143]\n"
    "ds_read_b128 v[192:195], v240 offset:24576\n"
    "v_exp_f32 v154, v154\n"
    "v_add_f32 v247, v247, v152\n"
    "v_exp_f32 v155, v155\n"
    "v_mfma_f32_32x32x16_bf16 v[128:143], v[204:207], v[108:111], v[128:143]\n"
    "ds_read_b128 v[196:199], v241 offset:24576\n"
    "v_add_f32 v248, v248, v153\n"
    "v_exp_f32 v156, v156\n"
    "v_add_f32 v247, v247, v154\n"
    "s_waitcnt lgkmcnt(4)\n"
    "v_mfma_f32_32x32x16_bf16 v[128:143], v[208:211], v[112:115], v[128:143]\n"
    "ds_read_b128 v[200:203], v240 offset:25088\n"
    "v_exp_f32 v157, v157\n"
    "v_add_f32 v248, v248, v155\n"
    "v_exp_f32 v158, v158\n"
    "v_mfma_f32_32x32x16_bf16 v[128:143], v[212:215], v[116:119], v[128:143]\n"
    "ds_read_b128 v[204:207], v241 offset:25088\n"
    "v_add_f32 v247, v247, v156\n"
    "v_exp_f32 v159, v159\n"
    "v_add_f32 v248, v248, v157\n"
    "s_waitcnt lgkmcnt(4)\n"
    "v_mfma_f32_32x32x16_bf16 v[128:143], v[216:219], v[120:123], v[128:143]\n"
    "ds_read_b128 v[208:211], v240 offset:25600\n"
    "v_add_f32 v247, v247, v158\n"
    "v_add_f32 v248, v248, v159\n"
    "v_cvt_pk_bf16_f32 v168, v144, v145\n"
    "v_cvt_pk_bf16_f32 v169, v146, v147\n"
    "v_cvt_pk_bf16_f32 v170, v148, v149\n"
    "v_mfma_f32_32x32x16_bf16 v[128:143], v[220:223], v[124:127], v[128:143]\n"
    "ds_read_b128 v[212:215], v241 offset:25600\n"
    "v_cvt_pk_bf16_f32 v171, v150, v151\n"
    "v_cvt_pk_bf16_f32 v172, v152, v153\n"
    "v_cvt_pk_bf16_f32 v173, v154, v155\n"
    "v_cvt_pk_bf16_f32 v174, v156, v157\n"
    "v_cvt_pk_bf16_f32 v175, v158, v159\n"
    "s_waitcnt lgkmcnt(4)\n"
    "v_mfma_f32_32x32x16_bf16 v[144:159], v[192:195], v[96:99], v[176:191]\n"
    "ds_read_b128 v[216:219], v240 offset:26112\n"
    "s_add_u32 m0, s61, 49152\n"
    "s_add_u32 s66, s62, 81920\n"
    "buffer_load_dwordx4 v245, s[40:43], s66 offen nt lds\n"
    "buffer_load_dwordx4 v245, s[40:43], s66 offen offset:2048 nt lds\n"
    "v_mfma_f32_32x32x16_bf16 v[144:159], v[196:199], v[100:103], v[144:159]\n"
    "ds_read_b128 v[220:223], v241 offset:26112\n"
    "s_add_u32 m0, s61, 50048\n"
    "s_nop 0\n"
    "buffer_load_dwordx4 v245, s[40:43], s66 offen offset:128 nt lds\n"
    "buffer_load_dwordx4 v245, s[40:43], s66 offen offset:2176 nt lds\n"
    "s_waitcnt lgkmcnt(4)\n"
    "v_mfma_f32_32x32x16_bf16 v[144:159], v[200:203], v[104:107], v[144:159]\n"
    "s_waitcnt vmcnt(8)\n"
    "s_barrier\n"
    "ds_read_b64_tr_b16 v[192:193], v242 offset:0\n"
    "ds_read_b64_tr_b16 v[194:195], v242 offset:2048\n"
    "v_mfma_f32_32x32x16_bf16 v[144:159], v[204:207], v[108:111], v[144:159]\n"
    "ds_read_b64_tr_b16 v[196:197], v242 offset:512\n"
    "ds_read_b64_tr_b16 v[198:199], v242 offset:2560\n"
    "s_waitcnt lgkmcnt(6)\n"
    "v_mfma_f32_32x32x16_bf16 v[144:159], v[208:211], v[112:115], v[144:159]\n"
    "ds_read_b64_tr_b16 v[200:201], v242 offset:1024\n"
    "ds_read_b64_tr_b16 v[202:203], v242 offset:3072\n"
    "v_max3_f32 v249, v128, v129, v130\n"
    "v_max3_f32 v249, v249, v131, v132\n"
    "v_max3_f32 v249, v249, v133, v134\n"
    "v_max3_f32 v249, v249, v135, v136\n"
    "v_mfma_f32_32x32x16_bf16 v[144:159], v[212:215], v[116:119], v[144:159]\n"
    "ds_read_b64_tr_b16 v[204:205], v242 offset:1536\n"
    "ds_read_b64_tr_b16 v[206:207], v242 offset:3584\n"
    "v_max3_f32 v249, v249, v137, v138\n"
    "v_max3_f32 v249, v249, v139, v140\n"
    "v_max3_f32 v249, v249, v141, v142\n"
    "s_waitcnt lgkmcnt(8)\n"
    "v_mfma_f32_32x32x16_bf16 v[144:159], v[216:219], v[120:123], v[144:159]\n"
    "ds_read_b64_tr_b16 v[208:209], v242 offset:4096\n"
    "ds_read_b64_tr_b16 v[210:211], v242 offset:6144\n"
    "v_max_f32 v249, v249, v143\n"
    "v_mfma_f32_32x32x16_bf16 v[144:159], v[220:223], v[124:127], v[144:159]\n"
    "ds_read_b64_tr_b16 v[212:213], v242 offset:4608\n"
    "ds_read_b64_tr_b16 v[214:215], v242 offset:6656\n"
    "s_waitcnt lgkmcnt(8)\n"
    "v_mfma_f32_32x32x16_bf16 v[0:15], v[192:195], v[160:163], v[0:15]\n"
    "ds_read_b64_tr_b16 v[216:217], v242 offset:5120\n"
    "ds_read_b64_tr_b16 v[218:219], v242 offset:7168\n"
    "v_mfma_f32_32x32x16_bf16 v[16:31], v[196:199], v[160:163], v[16:31]\n"
    "ds_read_b64_tr_b16 v[220:221], v242 offset:5632\n"
    "ds_read_b64_tr_b16 v[222:223], v242 offset:7680\n"
    "s_waitcnt lgkmcnt(8)\n"
    "v_mfma_f32_32x32x16_bf16 v[32:47], v[200:203], v[160:163], v[32:47]\n"
    "ds_read_b64_tr_b16 v[192:193], v242 offset:8192\n"
    "ds_read_b64_tr_b16 v[194:195], v242 offset:10240\n"
    "v_max3_f32 v250, v144, v145, v146\n"
    "v_max3_f32 v250, v250, v147, v148\n"
    "v_max3_f32 v250, v250, v149, v150\n"
    "v_max3_f32 v250, v250, v151, v152\n"
    "v_max3_f32 v250, v250, v153, v154\n"
    "v_mfma_f32_32x32x16_bf16 v[48:63], v[204:207], v[160:163], v[48:63]\n"
    "ds_read_b64_tr_b16 v[196:197], v242 offset:8704\n"
    "ds_read_b64_tr_b16 v[198:199], v242 offset:10752\n"
    "v_max3_f32 v250, v250, v155, v156\n"
    "v_max3_f32 v250, v250, v157, v158\n"
    "v_max_f32 v250, v250, v159\n"
    "v_max_f32 v249, v249, v250\n"
    "s_waitcnt lgkmcnt(8)\n"
    "v_mfma_f32_32x32x16_bf16 v[0:15], v[208:211], v[164:167], v[0:15]\n"
    "ds_read_b64_tr_b16 v[200:201], v242 offset:9216\n"
    "ds_read_b64_tr_b16 v[202:203], v242 offset:11264\n"
    "v_mov_b32 v251, v249\n"
    "s_nop 1\n"
    "v_permlane32_swap_b32 v249, v251\n"
    "v_max_f32 v249, v249, v251\n"
    "v_cmp_lt_f32 vcc, 0x41000000, v249\n"
    "s_nop 0\n"
    "v_mfma_f32_32x32x16_bf16 v[16:31], v[212:215], v[164:167], v[16:31]\n"
    "ds_read_b64_tr_b16 v[204:205], v242 offset:9728\n"
    "ds_read_b64_tr_b16 v[206:207], v242 offset:11776\n"
    "s_cbranch_vccnz L_stub1_i0_5\n"
    "L_back1_i0_5:\n"
    "v_exp_f32 v128, v128\n"
    "s_waitcnt lgkmcnt(8)\n"
    "v_mfma_f32_32x32x16_bf16 v[32:47], v[216:219], v[164:167], v[32:47]\n"
    "ds_read_b64_tr_b16 v[208:209], v242 offset:12288\n"
    "ds_read_b64_tr_b16 v[210:211], v242 offset:14336\n"
    "v_exp_f32 v129, v129\n"
    "v_exp_f32 v130, v130\n"
    "v_mfma_f32_32x32x16_bf16 v[48:63], v[220:223], v[164:167], v[48:63]\n"
    "ds_read_b64_tr_b16 v[212:213], v242 offset:12800\n"
    "ds_read_b64_tr_b16 v[214:215], v242 offset:14848\n"
    "v_add_f32 v247, v247, v128\n"
    "v_exp_f32 v131, v131\n"
    "s_waitcnt lgkmcnt(8)\n"
    "v_mfma_f32_32x32x16_bf16 v[0:15], v[192:195], v[168:171], v[0:15]\n"
    "ds_read_b64_tr_b16 v[216:217], v242 offset:13312\n"
    "ds_read_b64_tr_b16 v[218:219], v242 offset:15360\n"
    "v_add_f32 v248, v248, v129\n"
    "v_exp_f32 v132, v132\n"
    "v_add_f32 v247, v247, v130\n"
    "v_mfma_f32_32x32x16_bf16 v[16:31], v[196:199], v[168:171], v[16:31]\n"
    "ds_read_b64_tr_b16 v[220:221], v242 offset:13824\n"
    "ds_read_b64_tr_b16 v[222:223], v242 offset:15872\n"
    "v_exp_f32 v133, v133\n"
    "v_add_f32 v248, v248, v131\n"
    "s_waitcnt lgkmcnt(8)\n"
    "v_mfma_f32_32x32x16_bf16 v[32:47], v[200:203], v[168:171], v[32:47]\n"
    "s_barrier\n"
    "v_exp_f32 v134, v134\n"
    "v_add_f32 v247, v247, v132\n"
    "v_exp_f32 v135, v135\n"
    "v_mfma_f32_32x32x16_bf16 v[48:63], v[204:207], v[168:171], v[48:63]\n"
    "s_add_u32 m0, s61, 16384\n"
    "s_add_u32 s66, s62, 114688\n"
    "buffer_load_dwordx4 v243, s[36:39], s66 offen nt lds\n"
    "buffer_load_dwordx4 v244, s[36:39], s66 offen offset:2048 nt lds\n"
    "v_add_f32 v248, v248, v133\n"
    "s_waitcnt lgkmcnt(4)\n"
    "v_mfma_f32_32x32x16_bf16 v[0:15], v[208:211], v[172:175], v[0:15]\n"
    "s_add_u32 m0, s61, 17280\n"
    "s_nop 0\n"
    "buffer_load_dwordx4 v243, s[36:39], s66 offen offset:128 nt lds\n"
    "buffer_load_dwordx4 v244, s[36:39], s66 offen offset:2176 nt lds\n"
    "v_exp_f32 v136, v136\n"
    "v_add_f32 v247, v247, v134\n"
    "v_mfma_f32_32x32x16_bf16 v[16:31], v[212:215], v[172:175], v[16:31]\n"
    "v_exp_f32 v137, v137\n"
    "v_add_f32 v248, v248, v135\n"
    "v_exp_f32 v138, v138\n"
    "s_waitcnt lgkmcnt(0)\n"
    "v_mfma_f32_32x32x16_bf16 v[32:47], v[216:219], v[172:175], v[32:47]\n"
    "v_add_f32 v247, v247, v136\n"
    "v_exp_f32 v139, v139\n"
    "v_add_f32 v248, v248, v137\n"
    "v_exp_f32 v140, v140\n"
    "v_mfma_f32_32x32x16_bf16 v[48:63], v[220:223], v[172:175], v[48:63]\n"
    "s_cmp_lg_u32 s69, 0\n"
    "s_cbranch_scc1 L_stub2_i0_5\n"
    "L_back2_i0_5:\n"
    "v_add_f32 v247, v247, v138\n"
    "v_exp_f32 v141, v141\n"
    "s_waitcnt vmcnt(8)\n"
    "s_barrier\n"
    "ds_read_b128 v[192:195], v240 offset:0\n"
    "ds_read_b128 v[196:199], v241 offset:0\n"
    "ds_read_b128 v[200:203], v240 offset:512\n"
    "ds_read_b128 v[204:207], v241 offset:512\n"
    "ds_read_b128 v[208:211], v240 offset:1024\n"
    "ds_read_b128 v[212:215], v241 offset:1024\n"
    "v_add_f32 v248, v248, v139\n"
    "v_exp_f32 v142, v142\n"
    "v_add_f32 v247, v247, v140\n"
    "v_exp_f32 v143, v143\n"
    "v_add_f32 v248, v248, v141\n"
    "v_add_f32 v247, v247, v142\n"
    "v_add_f32 v248, v248, v143\n"
    "v_cvt_pk_bf16_f32 v160, v128, v129\n"
    "v_cvt_pk_bf16_f32 v161, v130, v131\n"
    "v_cvt_pk_bf16_f32 v162, v132, v133\n"
    "v_cvt_pk_bf16_f32 v163, v134, v135\n"
    "v_cvt_pk_bf16_f32 v164, v136, v137\n"
    "v_cvt_pk_bf16_f32 v165, v138, v139\n"
    "v_cvt_pk_bf16_f32 v166, v140, v141\n"
    "v_cvt_pk_bf16_f32 v167, v142, v143\n"
    "v_exp_f32 v144, v144\n"
    "v_exp_f32 v145, v145\n"
    "v_exp_f32 v146, v146\n"
    "v_add_f32 v247, v247, v144\n"
    "v_exp_f32 v147, v147\n"
    "v_add_f32 v248, v248, v145\n"
    "v_exp_f32 v148, v148\n"
    "v_add_f32 v247, v247, v146\n"
    "v_exp_f32 v149, v149\n"
    "v_add_f32 v248, v248, v147\n"
    "v_exp_f32 v150, v150\n"
    "v_add_f32 v247, v247, v148\n"
    "v_exp_f32 v151, v151\n"
    "s_waitcnt lgkmcnt(4)\n"
    "v_mfma_f32_32x32x16_bf16 v[128:143], v[192:195], v[96:99], v[176:191]\n"
    "ds_read_b128 v[216:219], v240 offset:1536\n"
    "v_add_f32 v248, v248, v149\n"
    "v_exp_f32 v152, v152\n"
    "v_add_f32 v247, v247, v150\n"
    "v_mfma_f32_32x32x16_bf16 v[128:143], v[196:199], v[100:103], v[128:143]\n"
    "ds_read_b128 v[220:223], v241 offset:1536\n"
    "v_exp_f32 v153, v153\n"
    "v_add_f32 v248, v248, v151\n"
    "s_waitcnt lgkmcnt(4)\n"
    "v_mfma_f32_32x32x16_bf16 v[128:143], v[200:203], v[104:107], v[128:143]\n"
    "ds_read_b128 v[192:195], v240 offset:8192\n"
    "v_exp_f32 v154, v154\n"
    "v_add_f32 v247, v247, v152\n"
    "v_exp_f32 v155, v155\n"
    "v_mfma_f32_32x32x16_bf16 v[128:143], v[204:207], v[108:111], v[128:143]\n"
    "ds_read_b128 v[196:199], v241 offset:8192\n"
    "v_add_f32 v248, v248, v153\n"
    "v_exp_f32 v156, v156\n"
    "v_add_f32 v247, v247, v154\n"
    "s_waitcnt lgkmcnt(4)\n"
    "v_mfma_f32_32x32x16_bf16 v[128:143], v[208:211], v[112:115], v[128:143]\n"
    "ds_read_b128 v[200:203], v240 offset:8704\n"
    "v_exp_f32 v157, v157\n"
    "v_add_f32 v248, v248, v155\n"
    "v_exp_f32 v158, v158\n"
    "v_mfma_f32_32x32x16_bf16 v[128:143], v[212:215], v[116:119], v[128:143]\n"
    "ds_read_b128 v[204:207], v241 offset:8704\n"
    "v_add_f32 v247, v247, v156\n"
    "v_exp_f32 v159, v159\n"
    "v_add_f32 v248, v248, v157\n"
    "s_waitcnt lgkmcnt(4)\n"
    "v_mfma_f32_32x32x16_bf16 v[128:143], v[216:219], v[120:123], v[128:143]\n"
    "ds_read_b128 v[208:211], v240 offset:9216\n"
    "v_add_f32 v247, v247, v158\n"
    "v_add_f32 v248, v248, v159\n"
    "v_cvt_pk_bf16_f32 v168, v144, v145\n"
    "v_cvt_pk_bf16_f32 v169, v146, v147\n"
    "v_cvt_pk_bf16_f32 v170, v148, v149\n"
    "v_mfma_f32_32x32x16_bf16 v[128:143], v[220:223], v[124:127], v[128:143]\n"
    "ds_read_b128 v[212:215], v241 offset:9216\n"
    "v_cvt_pk_bf16_f32 v171, v150, v151\n"
    "v_cvt_pk_bf16_f32 v172, v152, v153\n"
    "v_cvt_pk_bf16_f32 v173, v154, v155\n"
    "v_cvt_pk_bf16_f32 v174, v156, v157\n"
    "v_cvt_pk_bf16_f32 v175, v158, v159\n"
    "s_waitcnt lgkmcnt(4)\n"
    "v_mfma_f32_32x32x16_bf16 v[144:159], v[192:195], v[96:99], v[176:191]\n"
    "ds_read_b128 v[216:219], v240 offset:9728\n"
    "s_add_u32 m0, s61, 32768\n"
    "s_add_u32 s66, s62, 98304\n"
    "buffer_load_dwordx4 v245, s[40:43], s66 offen nt lds\n"
    "buffer_load_dwordx4 v245, s[40:43], s66 offen offset:2048 nt lds\n"
    "v_mfma_f32_32x32x16_bf16 v[144:159], v[196:199], v[100:103], v[144:159]\n"
    "ds_read_b128 v[220:223], v241 offset:9728\n"
    "s_add_u32 m0, s61, 33664\n"
    "s_nop 0\n"
    "buffer_load_dwordx4 v245, s[40:43], s66 offen offset:128 nt lds\n"
    "buffer_load_dwordx4 v245, s[40:43], s66 offen offset:2176 nt lds\n"
    "s_waitcnt lgkmcnt(4)\n"
    "v_mfma_f32_32x32x16_bf16 v[144:159], v[200:203], v[104:107], v[144:159]\n"
    "s_waitcnt vmcnt(8)\n"
    "s_barrier\n"
    "ds_read_b64_tr_b16 v[192:193], v242 offset:16384\n"
    "ds_read_b64_tr_b16 v[194:195], v242 offset:18432\n"
    "v_mfma_f32_32x32x16_bf16 v[144:159], v[204:207], v[108:111], v[144:159]\n"
    "ds_read_b64_tr_b16 v[196:197], v242 offset:16896\n"
    "ds_read_b64_tr_b16 v[198:199], v242 offset:18944\n"
    "s_waitcnt lgkmcnt(6)\n"
    "v_mfma_f32_32x32x16_bf16 v[144:159], v[208:211], v[112:115], v[144:159]\n"
    "ds_read_b64_tr_b16 v[200:201], v242 offset:17408\n"
    "ds_read_b64_tr_b16 v[202:203], v242 offset:19456\n"
    "v_max3_f32 v249, v128, v129, v130\n"
    "v_max3_f32 v249, v249, v131, v132\n"
    "v_max3_f32 v249, v249, v133, v134\n"
    "v_max3_f32 v249, v249, v135, v136\n"
    "v_mfma_f32_32x32x16_bf16 v[144:159], v[212:215], v[116:119], v[144:159]\n"
    "ds_read_b64_tr_b16 v[204:205], v242 offset:17920\n"
    "ds_read_b64_tr_b16 v[206:207], v242 offset:19968\n"
    "v_max3_f32 v249, v249, v137, v138\n"
    "v_max3_f32 v249, v249, v139, v140\n"
    "v_max3_f32 v249, v249, v141, v142\n"
    "s_waitcnt lgkmcnt(8)\n"
    "v_mfma_f32_32x32x16_bf16 v[144:159], v[216:219], v[120:123], v[144:159]\n"
    "ds_read_b64_tr_b16 v[208:209], v242 offset:20480\n"
    "ds_read_b64_tr_b16 v[210:211], v242 offset:22528\n"
    "v_max_f32 v249, v249, v143\n"
    "v_mfma_f32_32x32x16_bf16 v[144:159], v[220:223], v[124:127], v[144:159]\n"
    "ds_read_b64_tr_b16 v[212:213], v242 offset:20992\n"
    "ds_read_b64_tr_b16 v[214:215], v242 offset:23040\n"
    "s_waitcnt lgkmcnt(8)\n"
    "v_mfma_f32_32x32x16_bf16 v[0:15], v[192:195], v[160:163], v[0:15]\n"
    "ds_read_b64_tr_b16 v[216:217], v242 offset:21504\n"
    "ds_read_b64_tr_b16 v[218:219], v242 offset:23552\n"
    "v_mfma_f32_32x32x16_bf16 v[16:31], v[196:199], v[160:163], v[16:31]\n"
    "ds_read_b64_tr_b16 v[220:221], v242 offset:22016\n"
    "ds_read_b64_tr_b16 v[222:223], v242 offset:24064\n"
    "s_waitcnt lgkmcnt(8)\n"
    "v_mfma_f32_32x32x16_bf16 v[32:47], v[200:203], v[160:163], v[32:47]\n"
    "ds_read_b64_tr_b16 v[192:193], v242 offset:24576\n"
    "ds_read_b64_tr_b16 v[194:195], v242 offset:26624\n"
    "v_max3_f32 v250, v144, v145, v146\n"
    "v_max3_f32 v250, v250, v147, v148\n"
    "v_max3_f32 v250, v250, v149, v150\n"
    "v_max3_f32 v250, v250, v151, v152\n"
    "v_max3_f32 v250, v250, v153, v154\n"
    "v_mfma_f32_32x32x16_bf16 v[48:63], v[204:207], v[160:163], v[48:63]\n"
    "ds_read_b64_tr_b16 v[196:197], v242 offset:25088\n"
    "ds_read_b64_tr_b16 v[198:199], v242 offset:27136\n"
    "v_max3_f32 v250, v250, v155, v156\n"
    "v_max3_f32 v250, v250, v157, v158\n"
    "v_max_f32 v250, v250, v159\n"
    "v_max_f32 v249, v249, v250\n"
    "s_waitcnt lgkmcnt(8)\n"
    "v_mfma_f32_32x32x16_bf16 v[0:15], v[208:211], v[164:167], v[0:15]\n"
    "ds_read_b64_tr_b16 v[200:201], v242 offset:25600\n"
    "ds_read_b64_tr_b16 v[202:203], v242 offset:27648\n"
    "v_mov_b32 v251, v249\n"
    "s_nop 1\n"
    "v_permlane32_swap_b32 v249, v251\n"
    "v_max_f32 v249, v249, v251\n"
    "v_cmp_lt_f32 vcc, 0x41000000, v249\n"
    "s_nop 0\n"
    "v_mfma_f32_32x32x16_bf16 v[16:31], v[212:215], v[164:167], v[16:31]\n"
    "ds_read_b64_tr_b16 v[204:205], v242 offset:26112\n"
    "ds_read_b64_tr_b16 v[206:207], v242 offset:28160\n"
    "s_cbranch_vccnz L_stub1_i0_6\n"
    "L_back1_i0_6:\n"
    "v_exp_f32 v128, v128\n"
    "s_waitcnt lgkmcnt(8)\n"
    "v_mfma_f32_32x32x16_bf16 v[32:47], v[216:219], v[164:167], v[32:47]\n"
    "ds_read_b64_tr_b16 v[208:209], v242 offset:28672\n"
    "ds_read_b64_tr_b16 v[210:211], v242 offset:30720\n"
    "v_exp_f32 v129, v129\n"
    "v_exp_f32 v130, v130\n"
    "v_mfma_f32_32x32x16_bf16 v[48:63], v[220:223], v[164:167], v[48:63]\n"
    "ds_read_b64_tr_b16 v[212:213], v242 offset:29184\n"
    "ds_read_b64_tr_b16 v[214:215], v242 offset:31232\n"
    "v_add_f32 v247, v247, v128\n"
    "v_exp_f32 v131, v131\n"
    "s_waitcnt lgkmcnt(8)\n"
    "v_mfma_f32_32x32x16_bf16 v[0:15], v[192:195], v[168:171], v[0:15]\n"
    "ds_read_b64_tr_b16 v[216:217], v242 offset:29696\n"
    "ds_read_b64_tr_b16 v[218:219], v242 offset:31744\n"
    "v_add_f32 v248, v248, v129\n"
    "v_exp_f32 v132, v132\n"
    "v_add_f32 v247, v247, v130\n"
    "v_mfma_f32_32x32x16_bf16 v[16:31], v[196:199], v[168:171], v[16:31]\n"
    "ds_read_b64_tr_b16 v[220:221], v242 offset:30208\n"
    "ds_read_b64_tr_b16 v[222:223], v242 offset:32256\n"
    "v_exp_f32 v133, v133\n"
    "v_add_f32 v248, v248, v131\n"
    "s_waitcnt lgkmcnt(8)\n"
    "v_mfma_f32_32x32x16_bf16 v[32:47], v[200:203], v[168:171], v[32:47]\n"
    "s_barrier\n"
    "v_exp_f32 v134, v134\n"
    "v_add_f32 v247, v247, v132\n"
    "v_exp_f32 v135, v135\n"
    "v_mfma_f32_32x32x16_bf16 v[48:63], v[204:207], v[168:171], v[48:63]\n"
    "s_add_u32 m0, s61, 0\n"
    "s_add_u32 s66, s63, 0\n"
    "buffer_load_dwordx4 v243, s[36:39], s66 offen nt lds\n"
    "buffer_load_dwordx4 v244, s[36:39], s66 offen offset:2048 nt lds\n"
    "v_add_f32 v248, v248, v133\n"
    "s_waitcnt lgkmcnt(4)\n"
    "v_mfma_f32_32x32x16_bf16 v[0:15], v[208:211], v[172:175], v[0:15]\n"
    "s_add_u32 m0, s61, 896\n"
    "s_nop 0\n"
    "buffer_load_dwordx4 v243, s[36:39], s66 offen offset:128 nt lds\n"
    "buffer_load_dwordx4 v244, s[36:39], s66 offen offset:2176 nt lds\n"
    "v_exp_f32 v136, v136\n"
    "v_add_f32 v247, v247, v134\n"
    "v_mfma_f32_32x32x16_bf16 v[16:31], v[212:215], v[172:175], v[16:31]\n"
    "v_exp_f32 v137, v137\n"
    "v_add_f32 v248, v248, v135\n"
    "v_exp_f32 v138, v138\n"
    "s_waitcnt lgkmcnt(0)\n"
    "v_mfma_f32_32x32x16_bf16 v[32:47], v[216:219], v[172:175], v[32:47]\n"
    "v_add_f32 v247, v247, v136\n"
    "v_exp_f32 v139, v139\n"
    "v_add_f32 v248, v248, v137\n"
    "v_exp_f32 v140, v140\n"
    "v_mfma_f32_32x32x16_bf16 v[48:63], v[220:223], v[172:175], v[48:63]\n"
    "s_cmp_lg_u32 s69, 0\n"
    "s_cbranch_scc1 L_stub2_i0_6\n"
    "L_back2_i0_6:\n"
    "v_add_f32 v247, v247, v138\n"
    "v_exp_f32 v141, v141\n"
    "s_waitcnt vmcnt(8)\n"
    "s_barrier\n"
    "ds_read_b128 v[192:195], v240 offset:16384\n"
    "ds_read_b128 v[196:199], v241 offset:16384\n"
    "ds_read_b128 v[200:203], v240 offset:16896\n"
    "ds_read_b128 v[204:207], v241 offset:16896\n"
    "ds_read_b128 v[208:211], v240 offset:17408\n"
    "ds_read_b128 v[212:215], v241 offset:17408\n"
    "v_add_f32 v248, v248, v139\n"
    "v_exp_f32 v142, v142\n"
    "v_add_f32 v247, v247, v140\n"
    "v_exp_f32 v143, v143\n"
    "v_add_f32 v248, v248, v141\n"
    "v_add_f32 v247, v247, v142\n"
    "v_add_f32 v248, v248, v143\n"
    "v_cvt_pk_bf16_f32 v160, v128, v129\n"
    "v_cvt_pk_bf16_f32 v161, v130, v131\n"
    "v_cvt_pk_bf16_f32 v162, v132, v133\n"
    "v_cvt_pk_bf16_f32 v163, v134, v135\n"
    "v_cvt_pk_bf16_f32 v164, v136, v137\n"
    "v_cvt_pk_bf16_f32 v165, v138, v139\n"
    "v_cvt_pk_bf16_f32 v166, v140, v141\n"
    "v_cvt_pk_bf16_f32 v167, v142, v143\n"
    "v_exp_f32 v144, v144\n"
    "v_exp_f32 v145, v145\n"
    "v_exp_f32 v146, v146\n"
    "v_add_f32 v247, v247, v144\n"
    "v_exp_f32 v147, v147\n"
    "v_add_f32 v248, v248, v145\n"
    "v_exp_f32 v148, v148\n"
    "v_add_f32 v247, v247, v146\n"
    "v_exp_f32 v149, v149\n"
    "v_add_f32 v248, v248, v147\n"
    "v_exp_f32 v150, v150\n"
    "v_add_f32 v247, v247, v148\n"
    "v_exp_f32 v151, v151\n"
    "s_waitcnt lgkmcnt(4)\n"
    "v_mfma_f32_32x32x16_bf16 v[128:143], v[192:195], v[96:99], v[176:191]\n"
    "ds_read_b128 v[216:219], v240 offset:17920\n"
    "v_add_f32 v248, v248, v149\n"
    "v_exp_f32 v152, v152\n"
    "v_add_f32 v247, v247, v150\n"
    "v_mfma_f32_32x32x16_bf16 v[128:143], v[196:199], v[100:103], v[128:143]\n"
    "ds_read_b128 v[220:223], v241 offset:17920\n"
    "v_exp_f32 v153, v153\n"
    "v_add_f32 v248, v248, v151\n"
    "s_waitcnt lgkmcnt(4)\n"
    "v_mfma_f32_32x32x16_bf16 v[128:143], v[200:203], v[104:107], v[128:143]\n"
    "ds_read_b128 v[192:195], v240 offset:24576\n"
    "v_exp_f32 v154, v154\n"
    "v_add_f32 v247, v247, v152\n"
    "v_exp_f32 v155, v155\n"
    "v_mfma_f32_32x32x16_bf16 v[128:143], v[204:207], v[108:111], v[128:143]\n"
    "ds_read_b128 v[196:199], v241 offset:24576\n"
    "v_add_f32 v248, v248, v153\n"
    "v_exp_f32 v156, v156\n"
    "v_add_f32 v247, v247, v154\n"
    "s_waitcnt lgkmcnt(4)\n"
    "v_mfma_f32_32x32x16_bf16 v[128:143], v[208:211], v[112:115], v[128:143]\n"
    "ds_read_b128 v[200:203], v240 offset:25088\n"
    "v_exp_f32 v157, v157\n"
    "v_add_f32 v248, v248, v155\n"
    "v_exp_f32 v158, v158\n"
    "v_mfma_f32_32x32x16_bf16 v[128:143], v[212:215], v[116:119], v[128:143]\n"
    "ds_read_b128 v[204:207], v241 offset:25088\n"
    "v_add_f32 v247, v247, v156\n"
    "v_exp_f32 v159, v159\n"
    "v_add_f32 v248, v248, v157\n"
    "s_waitcnt lgkmcnt(4)\n"
    "v_mfma_f32_32x32x16_bf16 v[128:143], v[216:219], v[120:123], v[128:143]\n"
    "ds_read_b128 v[208:211], v240 offset:25600\n"
    "v_add_f32 v247, v247, v158\n"
    "v_add_f32 v248, v248, v159\n"
    "v_cvt_pk_bf16_f32 v168, v144, v145\n"
    "v_cvt_pk_bf16_f32 v169, v146, v147\n"
    "v_cvt_pk_bf16_f32 v170, v148, v149\n"
    "v_mfma_f32_32x32x16_bf16 v[128:143], v[220:223], v[124:127], v[128:143]\n"
    "ds_read_b128 v[212:215], v241 offset:25600\n"
    "v_cvt_pk_bf16_f32 v171, v150, v151\n"
    "v_cvt_pk_bf16_f32 v172, v152, v153\n"
    "v_cvt_pk_bf16_f32 v173, v154, v155\n"
    "v_cvt_pk_bf16_f32 v174, v156, v157\n"
    "v_cvt_pk_bf16_f32 v175, v158, v159\n"
    "s_waitcnt lgkmcnt(4)\n"
    "v_mfma_f32_32x32x16_bf16 v[144:159], v[192:195], v[96:99], v[176:191]\n"
    "ds_read_b128 v[216:219], v240 offset:26112\n"
    "s_add_u32 m0, s61, 49152\n"
    "s_add_u32 s66, s62, 114688\n"
    "buffer_load_dwordx4 v245, s[40:43], s66 offen nt lds\n"
    "buffer_load_dwordx4 v245, s[40:43], s66 offen offset:2048 nt lds\n"
    "v_mfma_f32_32x32x16_bf16 v[144:159], v[196:199], v[100:103], v[144:159]\n"
    "ds_read_b128 v[220:223], v241 offset:26112\n"
    "s_add_u32 m0, s61, 50048\n"
    "s_nop 0\n"
    "buffer_load_dwordx4 v245, s[40:43], s66 offen offset:128 nt lds\n"
    "buffer_load_dwordx4 v245, s[40:43], s66 offen offset:2176 nt lds\n"
    "s_waitcnt lgkmcnt(4)\n"
    "v_mfma_f32_32x32x16_bf16 v[144:159], v[200:203], v[104:107], v[144:159]\n"
    "s_waitcnt vmcnt(8)\n"
    "s_barrier\n"
    "ds_read_b64_tr_b16 v[192:193], v242 offset:0\n"
    "ds_read_b64_tr_b16 v[194:195], v242 offset:2048\n"
    "v_mfma_f32_32x32x16_bf16 v[144:159], v[204:207], v[108:111], v[144:159]\n"
    "ds_read_b64_tr_b16 v[196:197], v242 offset:512\n"
    "ds_read_b64_tr_b16 v[198:199], v242 offset:2560\n"
    "s_waitcnt lgkmcnt(6)\n"
    "v_mfma_f32_32x32x16_bf16 v[144:159], v[208:211], v[112:115], v[144:159]\n"
    "ds_read_b64_tr_b16 v[200:201], v242 offset:1024\n"
    "ds_read_b64_tr_b16 v[202:203], v242 offset:3072\n"
    "v_max3_f32 v249, v128, v129, v130\n"
    "v_max3_f32 v249, v249, v131, v132\n"
    "v_max3_f32 v249, v249, v133, v134\n"
    "v_max3_f32 v249, v249, v135, v136\n"
    "v_mfma_f32_32x32x16_bf16 v[144:159], v[212:215], v[116:119], v[144:159]\n"
    "ds_read_b64_tr_b16 v[204:205], v242 offset:1536\n"
    "ds_read_b64_tr_b16 v[206:207], v242 offset:3584\n"
    "v_max3_f32 v249, v249, v137, v138\n"
    "v_max3_f32 v249, v249, v139, v140\n"
    "v_max3_f32 v249, v249, v141, v142\n"
    "s_waitcnt lgkmcnt(8)\n"
    "v_mfma_f32_32x32x16_bf16 v[144:159], v[216:219], v[120:123], v[144:159]\n"
    "ds_read_b64_tr_b16 v[208:209], v242 offset:4096\n"
    "ds_read_b64_tr_b16 v[210:211], v242 offset:6144\n"
    "v_max_f32 v249, v249, v143\n"
    "v_mfma_f32_32x32x16_bf16 v[144:159], v[220:223], v[124:127], v[144:159]\n"
    "ds_read_b64_tr_b16 v[212:213], v242 offset:4608\n"
    "ds_read_b64_tr_b16 v[214:215], v242 offset:6656\n"
    "s_waitcnt lgkmcnt(8)\n"
    "v_mfma_f32_32x32x16_bf16 v[0:15], v[192:195], v[160:163], v[0:15]\n"
    "ds_read_b64_tr_b16 v[216:217], v242 offset:5120\n"
    "ds_read_b64_tr_b16 v[218:219], v242 offset:7168\n"
    "v_mfma_f32_32x32x16_bf16 v[16:31], v[196:199], v[160:163], v[16:31]\n"
    "ds_read_b64_tr_b16 v[220:221], v242 offset:5632\n"
    "ds_read_b64_tr_b16 v[222:223], v242 offset:7680\n"
    "s_waitcnt lgkmcnt(8)\n"
    "v_mfma_f32_32x32x16_bf16 v[32:47], v[200:203], v[160:163], v[32:47]\n"
    "ds_read_b64_tr_b16 v[192:193], v242 offset:8192\n"
    "ds_read_b64_tr_b16 v[194:195], v242 offset:10240\n"
    "v_max3_f32 v250, v144, v145, v146\n"
    "v_max3_f32 v250, v250, v147, v148\n"
    "v_max3_f32 v250, v250, v149, v150\n"
    "v_max3_f32 v250, v250, v151, v152\n"
    "v_max3_f32 v250, v250, v153, v154\n"
    "v_mfma_f32_32x32x16_bf16 v[48:63], v[204:207], v[160:163], v[48:63]\n"
    "ds_read_b64_tr_b16 v[196:197], v242 offset:8704\n"
    "ds_read_b64_tr_b16 v[198:199], v242 offset:10752\n"
    "v_max3_f32 v250, v250, v155, v156\n"
    "v_max3_f32 v250, v250, v157, v158\n"
    "v_max_f32 v250, v250, v159\n"
    "v_max_f32 v249, v249, v250\n"
    "s_waitcnt lgkmcnt(8)\n"
    "v_mfma_f32_32x32x16_bf16 v[0:15], v[208:211], v[164:167], v[0:15]\n"
    "ds_read_b64_tr_b16 v[200:201], v242 offset:9216\n"
    "ds_read_b64_tr_b16 v[202:203], v242 offset:11264\n"
    "v_mov_b32 v251, v249\n"
    "s_nop 1\n"
    "v_permlane32_swap_b32 v249, v251\n"
    "v_max_f32 v249, v249, v251\n"
    "v_cmp_lt_f32 vcc, 0x41000000, v249\n"
    "s_nop 0\n"
    "v_mfma_f32_32x32x16_bf16 v[16:31], v[212:215], v[164:167], v[16:31]\n"
    "ds_read_b64_tr_b16 v[204:205], v242 offset:9728\n"
    "ds_read_b64_tr_b16 v[206:207], v242 offset:11776\n"
    "s_cbranch_vccnz L_stub1_i0_7\n"
    "L_back1_i0_7:\n"
    "v_exp_f32 v128, v128\n"
    "s_waitcnt lgkmcnt(8)\n"
    "v_mfma_f32_32x32x16_bf16 v[32:47], v[216:219], v[164:167], v[32:47]\n"
    "ds_read_b64_tr_b16 v[208:209], v242 offset:12288\n"
    "ds_read_b64_tr_b16 v[210:211], v242 offset:14336\n"
    "v_exp_f32 v129, v129\n"
    "v_exp_f32 v130, v130\n"
    "v_mfma_f32_32x32x16_bf16 v[48:63], v[220:223], v[164:167], v[48:63]\n"
    "ds_read_b64_tr_b16 v[212:213], v242 offset:12800\n"
    "ds_read_b64_tr_b16 v[214:215], v242 offset:14848\n"
    "v_add_f32 v247, v247, v128\n"
    "v_exp_f32 v131, v131\n"
    "s_waitcnt lgkmcnt(8)\n"
    "v_mfma_f32_32x32x16_bf16 v[0:15], v[192:195], v[168:171], v[0:15]\n"
    "ds_read_b64_tr_b16 v[216:217], v242 offset:13312\n"
    "ds_read_b64_tr_b16 v[218:219], v242 offset:15360\n"
    "v_add_f32 v248, v248, v129\n"
    "v_exp_f32 v132, v132\n"
    "v_add_f32 v247, v247, v130\n"
    "v_mfma_f32_32x32x16_bf16 v[16:31], v[196:199], v[168:171], v[16:31]\n"
    "ds_read_b64_tr_b16 v[220:221], v242 offset:13824\n"
    "ds_read_b64_tr_b16 v[222:223], v242 offset:15872\n"
    "v_exp_f32 v133, v133\n"
    "v_add_f32 v248, v248, v131\n"
    "s_waitcnt lgkmcnt(8)\n"
    "v_mfma_f32_32x32x16_bf16 v[32:47], v[200:203], v[168:171], v[32:47]\n"
    "s_barrier\n"
    "v_exp_f32 v134, v134\n"
    "v_add_f32 v247, v247, v132\n"
    "v_exp_f32 v135, v135\n"
    "v_mfma_f32_32x32x16_bf16 v[48:63], v[204:207], v[168:171], v[48:63]\n"
    "s_add_u32 m0, s61, 16384\n"
    "s_add_u32 s66, s63, 16384\n"
    "buffer_load_dwordx4 v243, s[36:39], s66 offen nt lds\n"
    "buffer_load_dwordx4 v244, s[36:39], s66 offen offset:2048 nt lds\n"
    "v_add_f32 v248, v248, v133\n"
    "s_waitcnt lgkmcnt(4)\n"
    "v_mfma_f32_32x32x16_bf16 v[0:15], v[208:211], v[172:175], v[0:15]\n"
    "s_add_u32 m0, s61, 17280\n"
    "s_nop 0\n"
    "buffer_load_dwordx4 v243, s[36:39], s66 offen offset:128 nt lds\n"
    "buffer_load_dwordx4 v244, s[36:39], s66 offen offset:2176 nt lds\n"
    "v_exp_f32 v136, v136\n"
    "v_add_f32 v247, v247, v134\n"
    "v_mfma_f32_32x32x16_bf16 v[16:31], v[212:215], v[172:175], v[16:31]\n"
    "v_exp_f32 v137, v137\n"
    "v_add_f32 v248, v248, v135\n"
    "v_exp_f32 v138, v138\n"
    "s_waitcnt lgkmcnt(0)\n"
    "v_mfma_f32_32x32x16_bf16 v[32:47], v[216:219], v[172:175], v[32:47]\n"
    "v_add_f32 v247, v247, v136\n"
    "v_exp_f32 v139, v139\n"
    "v_add_f32 v248, v248, v137\n"
    "v_exp_f32 v140, v140\n"
    "v_mfma_f32_32x32x16_bf16 v[48:63], v[220:223], v[172:175], v[48:63]\n"
    "s_cmp_lg_u32 s69, 0\n"
    "s_cbranch_scc1 L_stub2_i0_7\n"
    "L_back2_i0_7:\n"
    "v_add_f32 v247, v247, v138\n"
    "v_exp_f32 v141, v141\n"
    "s_waitcnt vmcnt(8)\n"
    "s_barrier\n"
    "ds_read_b128 v[192:195], v240 offset:0\n"
    "ds_read_b128 v[196:199], v241 offset:0\n"
    "ds_read_b128 v[200:203], v240 offset:512\n"
    "ds_read_b128 v[204:207], v241 offset:512\n"
    "ds_read_b128 v[208:211], v240 offset:1024\n"
    "ds_read_b128 v[212:215], v241 offset:1024\n"
    "ds_read_b128 v[96:99], v254 offset:0\n"
    "ds_read_b128 v[100:103], v254 offset:1024\n"
    "ds_read_b128 v[104:107], v254 offset:2048\n"
    "ds_read_b128 v[108:111], v254 offset:3072\n"
    "ds_read_b128 v[112:115], v254 offset:4096\n"
    "ds_read_b128 v[116:119], v254 offset:5120\n"
    "ds_read_b128 v[120:123], v254 offset:6144\n"
    "ds_read_b128 v[124:127], v254 offset:7168\n"
    "v_add_f32 v248, v248, v139\n"
    "v_exp_f32 v142, v142\n"
    "v_add_f32 v247, v247, v140\n"
    "v_exp_f32 v143, v143\n"
    "v_add_f32 v248, v248, v141\n"
    "v_add_f32 v247, v247, v142\n"
    "v_add_f32 v248, v248, v143\n"
    "v_cvt_pk_bf16_f32 v160, v128, v129\n"
    "v_cvt_pk_bf16_f32 v161, v130, v131\n"
    "v_cvt_pk_bf16_f32 v162, v132, v133\n"
    "v_cvt_pk_bf16_f32 v163, v134, v135\n"
    "v_cvt_pk_bf16_f32 v164, v136, v137\n"
    "v_cvt_pk_bf16_f32 v165, v138, v139\n"
    "v_cvt_pk_bf16_f32 v166, v140, v141\n"
    "v_cvt_pk_bf16_f32 v167, v142, v143\n"
    "v_exp_f32 v144, v144\n"
    "v_exp_f32 v145, v145\n"
    "v_exp_f32 v146, v146\n"
    "v_add_f32 v247, v247, v144\n"
    "v_exp_f32 v147, v147\n"
    "v_add_f32 v248, v248, v145\n"
    "v_exp_f32 v148, v148\n"
    "v_add_f32 v247, v247, v146\n"
    "v_exp_f32 v149, v149\n"
    "v_add_f32 v248, v248, v147\n"
    "v_exp_f32 v150, v150\n"
    "v_add_f32 v247, v247, v148\n"
    "v_exp_f32 v151, v151\n"
    "s_mov_b32 s64, s65\n"
    "s_mov_b32 s62, s63\n"
    "s_add_u32 s63, s63, 131072\n"
    "s_add_u32 s65, s65, 131072\n"
    "s_sub_u32 s68, s68, 1\n"
    "s_cmp_eq_u32 s68, 1\n"
    "s_cselect_b32 s63, 268435456, s63\n"
    "s_cselect_b32 s65, 268435456, s65\n"
    "L_att_loop:\n"
    "s_waitcnt lgkmcnt(7)\n"
    "v_mfma_f32_32x32x16_bf16 v[128:143], v[192:195], v[96:99], 0\n"
    "ds_read_b128 v[216:219], v240 offset:1536\n"
    "v_add_f32 v248, v248, v149\n"
    "v_exp_f32 v152, v152\n"
    "v_add_f32 v247, v247, v150\n"
    "s_waitcnt lgkmcnt(7)\n"
    "v_mfma_f32_32x32x16_bf16 v[128:143], v[196:199], v[100:103], v[128:143]\n"
    "ds_read_b128 v[220:223], v241 offset:1536\n"
    "v_exp_f32 v153, v153\n"
    "v_add_f32 v248, v248, v151\n"
    "s_waitcnt lgkmcnt(7)\n"
    "v_mfma_f32_32x32x16_bf16 v[128:143], v[200:203], v[104:107], v[128:143]\n"
    "ds_read_b128 v[192:195], v240 offset:8192\n"
    "v_exp_f32 v154, v154\n"
    "v_add_f32 v247, v247, v152\n"
    "v_exp_f32 v155, v155\n"
    "s_waitcnt lgkmcnt(7)\n"
    "v_mfma_f32_32x32x16_bf16 v[128:143], v[204:207], v[108:111], v[128:143]\n"
    "ds_read_b128 v[196:199], v241 offset:8192\n"
    "v_add_f32 v248, v248, v153\n"
    "v_exp_f32 v156, v156\n"
    "v_add_f32 v247, v247, v154\n"
    "s_waitcnt lgkmcnt(7)\n"
    "v_mfma_f32_32x32x16_bf16 v[128:143], v[208:211], v[112:115], v[128:143]\n"
    "ds_read_b128 v[200:203], v240 offset:8704\n"
    "v_exp_f32 v157, v157\n"
    "v_add_f32 v248, v248, v155\n"
    "v_exp_f32 v158, v158\n"
    "s_waitcnt lgkmcnt(7)\n"
    "v_mfma_f32_32x32x16_bf16 v[128:143], v[212:215], v[116:119], v[128:143]\n"
    "ds_read_b128 v[204:207], v241 offset:8704\n"
    "v_add_f32 v247, v247, v156\n"
    "v_exp_f32 v159, v159\n"
    "v_add_f32 v248, v248, v157\n"
    "s_waitcnt lgkmcnt(4)\n"
    "v_mfma_f32_32x32x16_bf16 v[128:143], v[216:219], v[120:123], v[128:143]\n"
    "ds_read_b128 v[208:211], v240 offset:9216\n"
    "v_add_f32 v247, v247, v158\n"
    "v_add_f32 v248, v248, v159\n"
    "v_cvt_pk_bf16_f32 v168, v144, v145\n"
    "v_cvt_pk_bf16_f32 v169, v146, v147\n"
    "v_cvt_pk_bf16_f32 v170, v148, v149\n"
    "v_mfma_f32_32x32x16_bf16 v[128:143], v[220:223], v[124:127], v[128:143]\n"
    "ds_read_b128 v[212:215], v241 offset:9216\n"
    "v_cvt_pk_bf16_f32 v171, v150, v151\n"
    "v_cvt_pk_bf16_f32 v172, v152, v153\n"
    "v_cvt_pk_bf16_f32 v173, v154, v155\n"
    "v_cvt_pk_bf16_f32 v174, v156, v157\n"
    "v_cvt_pk_bf16_f32 v175, v158, v159\n"
    "s_waitcnt lgkmcnt(4)\n"
    "v_mfma_f32_32x32x16_bf16 v[144:159], v[192:195], v[96:99], 0\n"
    "ds_read_b128 v[216:219], v240 offset:9728\n"
    "s_add_u32 m0, s61, 32768\n"
    "s_add_u32 s66, s62, 0\n"
    "buffer_load_dwordx4 v245, s[40:43], s66 offen nt lds\n"
    "buffer_load_dwordx4 v245, s[40:43], s66 offen offset:2048 nt lds\n"
    "v_add_f32 v253, v247, v248\n"
    "v_mfma_f32_32x32x16_bf16 v[144:159], v[196:199], v[100:103], v[144:159]\n"
    "ds_read_b128 v[220:223], v241 offset:9728\n"
    "s_add_u32 m0, s61, 33664\n"
    "s_nop 0\n"
    "buffer_load_dwordx4 v245, s[40:43], s66 offen offset:128 nt lds\n"
    "buffer_load_dwordx4 v245, s[40:43], s66 offen offset:2176 nt lds\n"
    "v_mov_b32 v251, v253\n"
    "s_waitcnt lgkmcnt(4)\n"
    "v_mfma_f32_32x32x16_bf16 v[144:159], v[200:203], v[104:107], v[144:159]\n"
    "s_waitcnt vmcnt(8)\n"
    "s_barrier\n"
    "ds_read_b64_tr_b16 v[192:193], v242 offset:16384\n"
    "ds_read_b64_tr_b16 v[194:195], v242 offset:18432\n"
    "v_permlane32_swap_b32 v253, v251\n"
    "v_add_f32 v253, v253, v251\n"
    "v_mfma_f32_32x32x16_bf16 v[144:159], v[204:207], v[108:111], v[144:159]\n"
    "ds_read_b64_tr_b16 v[196:197], v242 offset:20480\n"
    "ds_read_b64_tr_b16 v[198:199], v242 offset:22528\n"
    "v_rcp_f32 v253, v253\n"
    "s_waitcnt lgkmcnt(6)\n"
    "v_mfma_f32_32x32x16_bf16 v[144:159], v[208:211], v[112:115], v[144:159]\n"
    "ds_read_b64_tr_b16 v[200:201], v242 offset:24576\n"
    "ds_read_b64_tr_b16 v[202:203], v242 offset:26624\n"
    "v_max3_f32 v249, v128, v129, v130\n"
    "v_max3_f32 v249, v249, v131, v132\n"
    "v_max3_f32 v249, v249, v133, v134\n"
    "v_max3_f32 v249, v249, v135, v136\n"
    "v_max3_f32 v249, v249, v137, v138\n"
    "v_mfma_f32_32x32x16_bf16 v[144:159], v[212:215], v[116:119], v[144:159]\n"
    "ds_read_b64_tr_b16 v[204:205], v242 offset:28672\n"
    "ds_read_b64_tr_b16 v[206:207], v242 offset:30720\n"
    "v_max3_f32 v249, v249, v139, v140\n"
    "v_max3_f32 v249, v249, v141, v142\n"
    "v_max_f32 v249, v249, v143\n"
    "s_waitcnt lgkmcnt(8)\n"
    "v_mfma_f32_32x32x16_bf16 v[144:159], v[216:219], v[120:123], v[144:159]\n"
    "ds_read_b64_tr_b16 v[208:209], v242 offset:16896\n"
    "ds_read_b64_tr_b16 v[210:211], v242 offset:18944\n"
    "v_mfma_f32_32x32x16_bf16 v[144:159], v[220:223], v[124:127], v[144:159]\n"
    "ds_read_b64_tr_b16 v[212:213], v242 offset:20992\n"
    "ds_read_b64_tr_b16 v[214:215], v242 offset:23040\n"
    "s_waitcnt lgkmcnt(8)\n"
    "v_mfma_f32_32x32x16_bf16 v[0:15], v[192:195], v[160:163], v[0:15]\n"
    "ds_read_b64_tr_b16 v[216:217], v242 offset:25088\n"
    "ds_read_b64_tr_b16 v[218:219], v242 offset:27136\n"
    "v_mfma_f32_32x32x16_bf16 v[0:15], v[196:199], v[164:167], v[0:15]\n"
    "ds_read_b64_tr_b16 v[220:221], v242 offset:29184\n"
    "ds_read_b64_tr_b16 v[222:223], v242 offset:31232\n"
    "s_waitcnt lgkmcnt(8)\n"
    "v_mfma_f32_32x32x16_bf16 v[0:15], v[200:203], v[168:171], v[0:15]\n"
    "ds_read_b64_tr_b16 v[192:193], v242 offset:17408\n"
    "ds_read_b64_tr_b16 v[194:195], v242 offset:19456\n"
    "v_max3_f32 v250, v144, v145, v146\n"
    "v_max3_f32 v250, v250, v147, v148\n"
    "v_max3_f32 v250, v250, v149, v150\n"
    "v_max3_f32 v250, v250, v151, v152\n"
    "v_max3_f32 v250, v250, v153, v154\n"
    "v_max3_f32 v250, v250, v155, v156\n"
    "v_max3_f32 v250, v250, v157, v158\n"
    "v_mfma_f32_32x32x16_bf16 v[0:15], v[204:207], v[172:175], v[0:15]\n"
    "ds_read_b64_tr_b16 v[196:197], v242 offset:21504\n"
    "ds_read_b64_tr_b16 v[198:199], v242 offset:23552\n"
    "v_max_f32 v250, v250, v159\n"
    "v_max_f32 v249, v249, v250\n"
    "v_mov_b32 v251, v249\n"
    "s_nop 1\n"
    "v_permlane32_swap_b32 v249, v251\n"
    "v_max_f32 v249, v249, v251\n"
    "v_sub_f32 v176, 0, v249\n"
    "s_waitcnt lgkmcnt(8)\n"
    "v_mfma_f32_32x32x16_bf16 v[16:31], v[208:211], v[160:163], v[16:31]\n"
    "ds_read_b64_tr_b16 v[200:201], v242 offset:25600\n"
    "ds_read_b64_tr_b16 v[202:203], v242 offset:27648\n"
    "v_sub_f32 v177, 0, v249\n"
    "v_sub_f32 v178, 0, v249\n"
    "v_sub_f32 v179, 0, v249\n"
    "v_sub_f32 v180, 0, v249\n"
    "v_sub_f32 v181, 0, v249\n"
    "v_sub_f32 v182, 0, v249\n"
    "v_sub_f32 v183, 0, v249\n"
    "v_mfma_f32_32x32x16_bf16 v[16:31], v[212:215], v[164:167], v[16:31]\n"
    "ds_read_b64_tr_b16 v[204:205], v242 offset:29696\n"
    "ds_read_b64_tr_b16 v[206:207], v242 offset:31744\n"
    "v_sub_f32 v184, 0, v249\n"
    "v_sub_f32 v185, 0, v249\n"
    "v_sub_f32 v186, 0, v249\n"
    "v_sub_f32 v187, 0, v249\n"
    "v_sub_f32 v188, 0, v249\n"
    "v_sub_f32 v189, 0, v249\n"
    "s_waitcnt lgkmcnt(8)\n"
    "v_mfma_f32_32x32x16_bf16 v[16:31], v[216:219], v[168:171], v[16:31]\n"
    "ds_read_b64_tr_b16 v[208:209], v242 offset:17920\n"
    "ds_read_b64_tr_b16 v[210:211], v242 offset:19968\n"
    "v_mul_f32 v0, v0, v253\n"
    "v_mul_f32 v1, v1, v253\n"
    "v_cvt_pk_bf16_f32 v64, v0, v1\n"
    "v_mul_f32 v2, v2, v253\n"
    "v_mul_f32 v3, v3, v253\n"
    "v_cvt_pk_bf16_f32 v65, v2, v3\n"
    "v_sub_f32 v190, 0, v249\n"
    "v_mfma_f32_32x32x16_bf16 v[16:31], v[220:223], v[172:175], v[16:31]\n"
    "ds_read_b64_tr_b16 v[212:213], v242 offset:22016\n"
    "ds_read_b64_tr_b16 v[214:215], v242 offset:24064\n"
    "v_mul_f32 v4, v4, v253\n"
    "v_mul_f32 v5, v5, v253\n"
    "v_cvt_pk_bf16_f32 v66, v4, v5\n"
    "v_mul_f32 v6, v6, v253\n"
    "v_mul_f32 v7, v7, v253\n"
    "v_sub_f32 v191, 0, v249\n"
    "s_waitcnt lgkmcnt(8)\n"
    "v_mfma_f32_32x32x16_bf16 v[32:47], v[192:195], v[160:163], v[32:47]\n"
    "ds_read_b64_tr_b16 v[216:217], v242 offset:26112\n"
    "ds_read_b64_tr_b16 v[218:219], v242 offset:28160\n"
    "v_cvt_pk_bf16_f32 v67, v6, v7\n"
    "v_mul_f32 v8, v8, v253\n"
    "v_mul_f32 v9, v9, v253\n"
    "v_cvt_pk_bf16_f32 v68, v8, v9\n"
    "v_mul_f32 v10, v10, v253\n"
    "v_mul_f32 v11, v11, v253\n"
    "v_sub_f32 v128, v128, v249\n"
    "v_mfma_f32_32x32x16_bf16 v[32:47], v[196:199], v[164:167], v[32:47]\n"
    "ds_read_b64_tr_b16 v[220:221], v242 offset:30208\n"
    "ds_read_b64_tr_b16 v[222:223], v242 offset:32256\n"
    "v_cvt_pk_bf16_f32 v69, v10, v11\n"
    "v_mul_f32 v12, v12, v253\n"
    "v_mul_f32 v13, v13, v253\n"
    "v_cvt_pk_bf16_f32 v70, v12, v13\n"
    "v_mul_f32 v14, v14, v253\n"
    "v_sub_f32 v129, v129, v249\n"
    "s_waitcnt lgkmcnt(8)\n"
    "v_mfma_f32_32x32x16_bf16 v[32:47], v[200:203], v[168:171], v[32:47]\n"
    "s_barrier\n"
    "ds_read_b64_tr_b16 v[192:193], v242 offset:32768\n"
    "ds_read_b64_tr_b16 v[194:195], v242 offset:40960\n"
    "v_mul_f32 v15, v15, v253\n"
    "v_cvt_pk_bf16_f32 v71, v14, v15\n"
    "v_sub_f32 v130, v130, v249\n"
    "v_sub_f32 v131, v131, v249\n"
    "v_sub_f32 v132, v132, v249\n"
    "v_sub_f32 v133, v133, v249\n"
    "v_mfma_f32_32x32x16_bf16 v[32:47], v[204:207], v[172:175], v[32:47]\n"
    "ds_read_b64_tr_b16 v[196:197], v242 offset:33280\n"
    "ds_read_b64_tr_b16 v[198:199], v242 offset:41472\n"
    "s_add_u32 m0, s61, 0\n"
    "s_add_u32 s66, s62, 32768\n"
    "buffer_load_dwordx4 v243, s[36:39], s66 offen nt lds\n"
    "buffer_load_dwordx4 v244, s[36:39], s66 offen offset:2048 nt lds\n"
    "v_sub_f32 v134, v134, v249\n"
    "v_sub_f32 v135, v135, v249\n"
    "s_waitcnt lgkmcnt(8)\n"
    "v_mfma_f32_32x32x16_bf16 v[48:63], v[208:211], v[160:163], v[48:63]\n"
    "ds_read_b64_tr_b16 v[200:201], v242 offset:33792\n"
    "ds_read_b64_tr_b16 v[202:203], v242 offset:41984\n"
    "s_add_u32 m0, s61, 896\n"
    "s_nop 0\n"
    "buffer_load_dwordx4 v243, s[36:39], s66 offen offset:128 nt lds\n"
    "buffer_load_dwordx4 v244, s[36:39], s66 offen offset:2176 nt lds\n"
    "v_sub_f32 v136, v136, v249\n"
    "v_sub_f32 v137, v137, v249\n"
    "v_sub_f32 v138, v138, v249\n"
    "v_sub_f32 v139, v139, v249\n"
    "v_mfma_f32_32x32x16_bf16 v[48:63], v[212:215], v[164:167], v[48:63]\n"
    "ds_read_b64_tr_b16 v[204:205], v242 offset:34304\n"
    "ds_read_b64_tr_b16 v[206:207], v242 offset:42496\n"
    "v_sub_f32 v140, v140, v249\n"
    "v_sub_f32 v141, v141, v249\n"
    "v_sub_f32 v142, v142, v249\n"
    "v_sub_f32 v143, v143, v249\n"
    "v_sub_f32 v144, v144, v249\n"
    "v_sub_f32 v145, v145, v249\n"
    "s_waitcnt lgkmcnt(8)\n"
    "v_mfma_f32_32x32x16_bf16 v[48:63], v[216:219], v[168:171], v[48:63]\n"
    "ds_read_b64_tr_b16 v[208:209], v242 offset:34816\n"
    "ds_read_b64_tr_b16 v[210:211], v242 offset:43008\n"
    "v_sub_f32 v146, v146, v249\n"
    "v_sub_f32 v147, v147, v249\n"
    "v_sub_f32 v148, v148, v249\n"
    "v_sub_f32 v149, v149, v249\n"
    "v_sub_f32 v150, v150, v249\n"
    "v_sub_f32 v151, v151, v249\n"
    "v_sub_f32 v152, v152, v249\n"
    "v_mfma_f32_32x32x16_bf16 v[48:63], v[220:223], v[172:175], v[48:63]\n"
    "ds_read_b64_tr_b16 v[212:213], v242 offset:35328\n"
    "ds_read_b64_tr_b16 v[214:215], v242 offset:43520\n"
    "v_sub_f32 v153, v153, v249\n"
    "v_sub_f32 v154, v154, v249\n"
    "v_sub_f32 v155, v155, v249\n"
    "v_sub_f32 v156, v156, v249\n"
    "v_sub_f32 v157, v157, v249\n"
    "v_sub_f32 v158, v158, v249\n"
    "s_waitcnt lgkmcnt(8)\n"
    "v_mfma_f32_32x32x16_bf16 a[0:15], v[192:195], v[64:67], a[0:15]\n"
    "ds_read_b64_tr_b16 v[216:217], v242 offset:35840\n"
    "ds_read_b64_tr_b16 v[218:219], v242 offset:44032\n"
    "v_sub_f32 v159, v159, v249\n"
    "v_mov_b32 v247, 0\n"
    "v_mov_b32 v248, 0\n"
    "v_exp_f32 v128, v128\n"
    "v_mul_f32 v16, v16, v253\n"
    "v_mul_f32 v17, v17, v253\n"
    "v_mfma_f32_32x32x16_bf16 a[16:31], v[196:199], v[64:67], a[16:31]\n"
    "ds_read_b64_tr_b16 v[220:221], v242 offset:36352\n"
    "ds_read_b64_tr_b16 v[222:223], v242 offset:44544\n"
    "v_exp_f32 v129, v129\n"
    "v_exp_f32 v130, v130\n"
    "v_add_f32 v247, v247, v128\n"
    "v_cvt_pk_bf16_f32 v72, v16, v17\n"
    "s_waitcnt lgkmcnt(8)\n"
    "v_mfma_f32_32x32x16_bf16 a[32:47], v[200:203], v[64:67], a[32:47]\n"
    "ds_read_b64_tr_b16 v[192:193], v242 offset:36864\n"
    "ds_read_b64_tr_b16 v[194:195], v242 offset:45056\n"
    "v_exp_f32 v131, v131\n"
    "v_add_f32 v248, v248, v129\n"
    "v_exp_f32 v132, v132\n"
    "v_add_f32 v247, v247, v130\n"
    "v_mul_f32 v18, v18, v253\n"
    "v_mfma_f32_32x32x16_bf16 a[48:63], v[204:207], v[64:67], a[48:63]\n"
    "ds_read_b64_tr_b16 v[196:197], v242 offset:37376\n"
    "ds_read_b64_tr_b16 v[198:199], v242 offset:45568\n"
    "v_exp_f32 v133, v133\n"
    "v_add_f32 v248, v248, v131\n"
    "v_exp_f32 v134, v134\n"
    "v_mul_f32 v19, v19, v253\n"
    "s_waitcnt lgkmcnt(8)\n"
    "v_mfma_f32_32x32x16_bf16 a[64:79], v[208:211], v[64:67], a[64:79]\n"
    "ds_read_b64_tr_b16 v[200:201], v242 offset:37888\n"
    "ds_read_b64_tr_b16 v[202:203], v242 offset:46080\n"
    "v_add_f32 v247, v247, v132\n"
    "v_exp_f32 v135, v135\n"
    "v_add_f32 v248, v248, v133\n"
    "v_exp_f32 v136, v136\n"
    "v_cvt_pk_bf16_f32 v73, v18, v19\n"
    "v_mfma_f32_32x32x16_bf16 a[80:95], v[212:215], v[64:67], a[80:95]\n"
    "ds_read_b64_tr_b16 v[204:205], v242 offset:38400\n"
    "ds_read_b64_tr_b16 v[206:207], v242 offset:46592\n"
    "v_add_f32 v247, v247, v134\n"
    "v_exp_f32 v137, v137\n"
    "v_add_f32 v248, v248, v135\n"
    "v_mul_f32 v20, v20, v253\n"
    "v_mul_f32 v21, v21, v253\n"
    "s_waitcnt lgkmcnt(8)\n"
    "v_mfma_f32_32x32x16_bf16 a[96:111], v[216:219], v[64:67], a[96:111]\n"
    "ds_read_b64_tr_b16 v[208:209], v242 offset:38912\n"
    "ds_read_b64_tr_b16 v[210:211], v242 offset:47104\n"
    "v_exp_f32 v138, v138\n"
    "v_add_f32 v247, v247, v136\n"
    "v_exp_f32 v139, v139\n"
    "v_add_f32 v248, v248, v137\n"
    "v_cvt_pk_bf16_f32 v74, v20, v21\n"
    "v_mfma_f32_32x32x16_bf16 a[112:127], v[220:223], v[64:67], a[112:127]\n"
    "ds_read_b64_tr_b16 v[212:213], v242 offset:39424\n"
    "ds_read_b64_tr_b16 v[214:215], v242 offset:47616\n"
    "v_exp_f32 v140, v140\n"
    "v_add_f32 v247, v247, v138\n"
    "v_exp_f32 v141, v141\n"
    "v_mul_f32 v22, v22, v253\n"
    "s_waitcnt lgkmcnt(8)\n"
    "v_mfma_f32_32x32x16_bf16 a[128:143], v[192:195], v[64:67], a[128:143]\n"
    "ds_read_b64_tr_b16 v[216:217], v242 offset:39936\n"
    "ds_read_b64_tr_b16 v[218:219], v242 offset:48128\n"
    "v_add_f32 v248, v248, v139\n"
    "v_exp_f32 v142, v142\n"
    "v_add_f32 v247, v247, v140\n"
    "v_exp_f32 v143, v143\n"
    "v_mul_f32 v23, v23, v253\n"
    "v_mfma_f32_32x32x16_bf16 a[144:159], v[196:199], v[64:67], a[144:159]\n"
    "ds_read_b64_tr_b16 v[220:221], v242 offset:40448\n"
    "ds_read_b64_tr_b16 v[222:223], v242 offset:48640\n"
    "v_add_f32 v248, v248, v141\n"
    "v_add_f32 v247, v247, v142\n"
    "v_add_f32 v248, v248, v143\n"
    "v_cvt_pk_bf16_f32 v160, v128, v129\n"
    "v_cvt_pk_bf16_f32 v161, v130, v131\n"
    "v_cvt_pk_bf16_f32 v75, v22, v23\n"
    "s_waitcnt lgkmcnt(8)\n"
    "v_mfma_f32_32x32x16_bf16 a[160:175], v[200:203], v[64:67], a[160:175]\n"
    "s_waitcnt vmcnt(8)\n"
    "s_barrier\n"
    "ds_read_b128 v[192:195], v240 offset:16384\n"
    "v_cvt_pk_bf16_f32 v162, v132, v133\n"
    "v_cvt_pk_bf16_f32 v163, v134, v135\n"
    "v_cvt_pk_bf16_f32 v164, v136, v137\n"
    "v_cvt_pk_bf16_f32 v165, v138, v139\n"
    "v_cvt_pk_bf16_f32 v166, v140, v141\n"
    "v_cvt_pk_bf16_f32 v167, v142, v143\n"
    "v_mul_f32 v24, v24, v253\n"
    "v_mfma_f32_32x32x16_bf16 a[176:191], v[204:207], v[64:67], a[176:191]\n"
    "ds_read_b128 v[196:199], v241 offset:16384\n"
    "v_exp_f32 v144, v144\n"
    "v_exp_f32 v145, v145\n"
    "v_exp_f32 v146, v146\n"
    "v_mul_f32 v25, v25, v253\n"
    "s_waitcnt lgkmcnt(6)\n"
    "v_mfma_f32_32x32x16_bf16 a[192:207], v[208:211], v[64:67], a[192:207]\n"
    "ds_read_b128 v[200:203], v240 offset:16896\n"
    "v_add_f32 v247, v247, v144\n"
    "v_exp_f32 v147, v147\n"
    "v_add_f32 v248, v248, v145\n"
    "v_exp_f32 v148, v148\n"
    "v_add_f32 v247, v247, v146\n"
    "v_cvt_pk_bf16_f32 v76, v24, v25\n"
    "v_mfma_f32_32x32x16_bf16 a[208:223], v[212:215], v[64:67], a[208:223]\n"
    "ds_read_b128 v[204:207], v241 offset:16896\n"
    "v_exp_f32 v149, v149\n"
    "v_add_f32 v248, v248, v147\n"
    "v_exp_f32 v150, v150\n"
    "v_add_f32 v247, v247, v148\n"
    "v_mul_f32 v26, v26, v253\n"
    "s_waitcnt lgkmcnt(4)\n"
    "v_mfma_f32_32x32x16_bf16 a[224:239], v[216:219], v[64:67], a[224:239]\n"
    "ds_read_b128 v[208:211], v240 offset:17408\n"
    "v_exp_f32 v151, v151\n"
    "v_mul_f32 v27, v27, v253\n"
    "v_cvt_pk_bf16_f32 v77, v26, v27\n"
    "v_mul_f32 v28, v28, v253\n"
    "v_mul_f32 v29, v29, v253\n"
    "v_cvt_pk_bf16_f32 v78, v28, v29\n"
    "v_mul_f32 v30, v30, v253\n"
    "v_mfma_f32_32x32x16_bf16 a[240:255], v[220:223], v[64:67], a[240:255]\n"
    "ds_read_b128 v[212:215], v241 offset:17408\n"
    "v_mul_f32 v31, v31, v253\n"
    "v_cvt_pk_bf16_f32 v79, v30, v31\n"
    "v_mul_f32 v32, v32, v253\n"
    "v_mul_f32 v33, v33, v253\n"
    "v_cvt_pk_bf16_f32 v80, v32, v33\n"
    "v_mul_f32 v34, v34, v253\n"
    "v_mul_f32 v35, v35, v253\n"
    "v_cvt_pk_bf16_f32 v81, v34, v35\n"
    "v_mul_f32 v36, v36, v253\n"
    "v_mul_f32 v37, v37, v253\n"
    "v_cvt_pk_bf16_f32 v82, v36, v37\n"
    "v_mul_f32 v38, v38, v253\n"
    "v_mul_f32 v39, v39, v253\n"
    "v_cvt_pk_bf16_f32 v83, v38, v39\n"
    "v_mul_f32 v40, v40, v253\n"
    "v_mul_f32 v41, v41, v253\n"
    "v_cvt_pk_bf16_f32 v84, v40, v41\n"
    "v_mul_f32 v42, v42, v253\n"
    "v_mul_f32 v43, v43, v253\n"
    "v_cvt_pk_bf16_f32 v85, v42, v43\n"
    "v_mul_f32 v44, v44, v253\n"
    "v_mul_f32 v45, v45, v253\n"
    "v_cvt_pk_bf16_f32 v86, v44, v45\n"
    "v_mul_f32 v46, v46, v253\n"
    "v_mul_f32 v47, v47, v253\n"
    "v_cvt_pk_bf16_f32 v87, v46, v47\n"
    "s_waitcnt lgkmcnt(4)\n"
    "v_mfma_f32_32x32x16_bf16 v[128:143], v[192:195], v[96:99], v[176:191]\n"
    "ds_read_b128 v[216:219], v240 offset:17920\n"
    "v_add_f32 v248, v248, v149\n"
    "v_exp_f32 v152, v152\n"
    "v_add_f32 v247, v247, v150\n"
    "v_mfma_f32_32x32x16_bf16 v[128:143], v[196:199], v[100:103], v[128:143]\n"
    "ds_read_b128 v[220:223], v241 offset:17920\n"
    "v_exp_f32 v153, v153\n"
    "v_add_f32 v248, v248, v151\n"
    "s_waitcnt lgkmcnt(4)\n"
    "v_mfma_f32_32x32x16_bf16 v[128:143], v[200:203], v[104:107], v[128:143]\n"
    "ds_read_b128 v[192:195], v240 offset:24576\n"
    "v_exp_f32 v154, v154\n"
    "v_add_f32 v247, v247, v152\n"
    "v_exp_f32 v155, v155\n"
    "v_mfma_f32_32x32x16_bf16 v[128:143], v[204:207], v[108:111], v[128:143]\n"
    "ds_read_b128 v[196:199], v241 offset:24576\n"
    "v_add_f32 v248, v248, v153\n"
    "v_exp_f32 v156, v156\n"
    "v_add_f32 v247, v247, v154\n"
    "s_waitcnt lgkmcnt(4)\n"
    "v_mfma_f32_32x32x16_bf16 v[128:143], v[208:211], v[112:115], v[128:143]\n"
    "ds_read_b128 v[200:203], v240 offset:25088\n"
    "v_exp_f32 v157, v157\n"
    "v_add_f32 v248, v248, v155\n"
    "v_exp_f32 v158, v158\n"
    "v_mfma_f32_32x32x16_bf16 v[128:143], v[212:215], v[116:119], v[128:143]\n"
    "ds_read_b128 v[204:207], v241 offset:25088\n"
    "v_add_f32 v247, v247, v156\n"
    "v_exp_f32 v159, v159\n"
    "v_add_f32 v248, v248, v157\n"
    "s_waitcnt lgkmcnt(4)\n"
    "v_mfma_f32_32x32x16_bf16 v[128:143], v[216:219], v[120:123], v[128:143]\n"
    "ds_read_b128 v[208:211], v240 offset:25600\n"
    "v_add_f32 v247, v247, v158\n"
    "v_add_f32 v248, v248, v159\n"
    "v_cvt_pk_bf16_f32 v168, v144, v145\n"
    "v_cvt_pk_bf16_f32 v169, v146, v147\n"
    "v_cvt_pk_bf16_f32 v170, v148, v149\n"
    "v_mfma_f32_32x32x16_bf16 v[128:143], v[220:223], v[124:127], v[128:143]\n"
    "ds_read_b128 v[212:215], v241 offset:25600\n"
    "v_cvt_pk_bf16_f32 v171, v150, v151\n"
    "v_cvt_pk_bf16_f32 v172, v152, v153\n"
    "v_cvt_pk_bf16_f32 v173, v154, v155\n"
    "v_cvt_pk_bf16_f32 v174, v156, v157\n"
    "v_cvt_pk_bf16_f32 v175, v158, v159\n"
    "s_waitcnt lgkmcnt(4)\n"
    "v_mfma_f32_32x32x16_bf16 v[144:159], v[192:195], v[96:99], v[176:191]\n"
    "ds_read_b128 v[216:219], v240 offset:26112\n"
    "s_add_u32 m0, s61, 49152\n"
    "s_add_u32 s66, s62, 16384\n"
    "buffer_load_dwordx4 v245, s[40:43], s66 offen nt lds\n"
    "buffer_load_dwordx4 v245, s[40:43], s66 offen offset:2048 nt lds\n"
    "v_mul_f32 v48, v48, v253\n"
    "v_mul_f32 v49, v49, v253\n"
    "v_cvt_pk_bf16_f32 v88, v48, v49\n"
    "v_mfma_f32_32x32x16_bf16 v[144:159], v[196:199], v[100:103], v[144:159]\n"
    "ds_read_b128 v[220:223], v241 offset:26112\n"
    "s_add_u32 m0, s61, 50048\n"
    "s_nop 0\n"
    "buffer_load_dwordx4 v245, s[40:43], s66 offen offset:128 nt lds\n"
    "buffer_load_dwordx4 v245, s[40:43], s66 offen offset:2176 nt lds\n"
    "v_mul_f32 v50, v50, v253\n"
    "v_mul_f32 v51, v51, v253\n"
    "v_cvt_pk_bf16_f32 v89, v50, v51\n"
    "s_waitcnt lgkmcnt(4)\n"
    "v_mfma_f32_32x32x16_bf16 v[144:159], v[200:203], v[104:107], v[144:159]\n"
    "s_waitcnt vmcnt(8)\n"
    "s_barrier\n"
    "ds_read_b64_tr_b16 v[192:193], v242 offset:0\n"
    "ds_read_b64_tr_b16 v[194:195], v242 offset:2048\n"
    "v_mul_f32 v52, v52, v253\n"
    "v_mul_f32 v53, v53, v253\n"
    "v_cvt_pk_bf16_f32 v90, v52, v53\n"
    "v_mul_f32 v54, v54, v253\n"
    "v_mul_f32 v55, v55, v253\n"
    "v_mfma_f32_32x32x16_bf16 v[144:159], v[204:207], v[108:111], v[144:159]\n"
    "ds_read_b64_tr_b16 v[196:197], v242 offset:512\n"
    "ds_read_b64_tr_b16 v[198:199], v242 offset:2560\n"
    "v_cvt_pk_bf16_f32 v91, v54, v55\n"
    "v_mul_f32 v56, v56, v253\n"
    "v_mul_f32 v57, v57, v253\n"
    "v_cvt_pk_bf16_f32 v92, v56, v57\n"
    "v_mul_f32 v58, v58, v253\n"
    "s_waitcnt lgkmcnt(6)\n"
    "v_mfma_f32_32x32x16_bf16 v[144:159], v[208:211], v[112:115], v[144:159]\n"
    "ds_read_b64_tr_b16 v[200:201], v242 offset:1024\n"
    "ds_read_b64_tr_b16 v[202:203], v242 offset:3072\n"
    "v_mul_f32 v59, v59, v253\n"
    "v_cvt_pk_bf16_f32 v93, v58, v59\n"
    "v_mul_f32 v60, v60, v253\n"
    "v_mul_f32 v61, v61, v253\n"
    "v_cvt_pk_bf16_f32 v94, v60, v61\n"
    "v_mul_f32 v62, v62, v253\n"
    "v_mfma_f32_32x32x16_bf16 v[144:159], v[212:215], v[116:119], v[144:159]\n"
    "ds_read_b64_tr_b16 v[204:205], v242 offset:1536\n"
    "ds_read_b64_tr_b16 v[206:207], v242 offset:3584\n"
    "v_mul_f32 v63, v63, v253\n"
    "v_cvt_pk_bf16_f32 v95, v62, v63\n"
    "v_max3_f32 v249, v128, v129, v130\n"
    "s_waitcnt lgkmcnt(8)\n"
    "v_mfma_f32_32x32x16_bf16 v[144:159], v[216:219], v[120:123], v[144:159]\n"
    "ds_read_b64_tr_b16 v[208:209], v242 offset:4096\n"
    "ds_read_b64_tr_b16 v[210:211], v242 offset:6144\n"
    "v_max3_f32 v249, v249, v131, v132\n"
    "v_max3_f32 v249, v249, v133, v134\n"
    "v_max3_f32 v249, v249, v135, v136\n"
    "v_max3_f32 v249, v249, v137, v138\n"
    "v_mfma_f32_32x32x16_bf16 v[144:159], v[220:223], v[124:127], v[144:159]\n"
    "ds_read_b64_tr_b16 v[212:213], v242 offset:4608\n"
    "ds_read_b64_tr_b16 v[214:215], v242 offset:6656\n"
    "v_max3_f32 v249, v249, v139, v140\n"
    "v_max3_f32 v249, v249, v141, v142\n"
    "v_max_f32 v249, v249, v143\n"
    "s_waitcnt lgkmcnt(8)\n"
    "v_mfma_f32_32x32x16_bf16 v[0:15], v[192:195], v[160:163], 0\n"
    "ds_read_b64_tr_b16 v[216:217], v242 offset:5120\n"
    "ds_read_b64_tr_b16 v[218:219], v242 offset:7168\n"
    "v_mfma_f32_32x32x16_bf16 v[16:31], v[196:199], v[160:163], 0\n"
    "ds_read_b64_tr_b16 v[220:221], v242 offset:5632\n"
    "ds_read_b64_tr_b16 v[222:223], v242 offset:7680\n"
    "s_waitcnt lgkmcnt(8)\n"
    "v_mfma_f32_32x32x16_bf16 v[32:47], v[200:203], v[160:163], 0\n"
    "ds_read_b64_tr_b16 v[192:193], v242 offset:8192\n"
    "ds_read_b64_tr_b16 v[194:195], v242 offset:10240\n"
    "v_max3_f32 v250, v144, v145, v146\n"
    "v_max3_f32 v250, v250, v147, v148\n"
    "v_max3_f32 v250, v250, v149, v150\n"
    "v_max3_f32 v250, v250, v151, v152\n"
    "v_max3_f32 v250, v250, v153, v154\n"
    "v_mfma_f32_32x32x16_bf16 v[48:63], v[204:207], v[160:163], 0\n"
    "ds_read_b64_tr_b16 v[196:197], v242 offset:8704\n"
    "ds_read_b64_tr_b16 v[198:199], v242 offset:10752\n"
    "v_max3_f32 v250, v250, v155, v156\n"
    "v_max3_f32 v250, v250, v157, v158\n"
    "v_max_f32 v250, v250, v159\n"
    "v_max_f32 v249, v249, v250\n"
    "s_waitcnt lgkmcnt(8)\n"
    "v_mfma_f32_32x32x16_bf16 v[0:15], v[208:211], v[164:167], v[0:15]\n"
    "ds_read_b64_tr_b16 v[200:201], v242 offset:9216\n"
    "ds_read_b64_tr_b16 v[202:203], v242 offset:11264\n"
    "v_mov_b32 v251, v249\n"
    "s_nop 1\n"
    "v_permlane32_swap_b32 v249, v251\n"
    "v_max_f32 v249, v249, v251\n"
    "v_cmp_lt_f32 vcc, 0x41000000, v249\n"
    "s_nop 0\n"
    "v_mfma_f32_32x32x16_bf16 v[16:31], v[212:215], v[164:167], v[16:31]\n"
    "ds_read_b64_tr_b16 v[204:205], v242 offset:9728\n"
    "ds_read_b64_tr_b16 v[206:207], v242 offset:11776\n"
    "s_cbranch_vccnz L_stub1_lp_1\n"
    "L_back1_lp_1:\n"
    "v_exp_f32 v128, v128\n"
    "s_waitcnt lgkmcnt(8)\n"
    "v_mfma_f32_32x32x16_bf16 v[32:47], v[216:219], v[164:167], v[32:47]\n"
    "ds_read_b64_tr_b16 v[208:209], v242 offset:12288\n"
    "ds_read_b64_tr_b16 v[210:211], v242 offset:14336\n"
    "v_exp_f32 v129, v129\n"
    "v_exp_f32 v130, v130\n"
    "v_mfma_f32_32x32x16_bf16 v[48:63], v[220:223], v[164:167], v[48:63]\n"
    "ds_read_b64_tr_b16 v[212:213], v242 offset:12800\n"
    "ds_read_b64_tr_b16 v[214:215], v242 offset:14848\n"
    "v_add_f32 v247, v247, v128\n"
    "v_exp_f32 v131, v131\n"
    "s_waitcnt lgkmcnt(8)\n"
    "v_mfma_f32_32x32x16_bf16 v[0:15], v[192:195], v[168:171], v[0:15]\n"
    "ds_read_b64_tr_b16 v[216:217], v242 offset:13312\n"
    "ds_read_b64_tr_b16 v[218:219], v242 offset:15360\n"
    "v_add_f32 v248, v248, v129\n"
    "v_exp_f32 v132, v132\n"
    "v_add_f32 v247, v247, v130\n"
    "v_mfma_f32_32x32x16_bf16 v[16:31], v[196:199], v[168:171], v[16:31]\n"
    "ds_read_b64_tr_b16 v[220:221], v242 offset:13824\n"
    "ds_read_b64_tr_b16 v[222:223], v242 offset:15872\n"
    "v_exp_f32 v133, v133\n"
    "v_add_f32 v248, v248, v131\n"
    "s_waitcnt lgkmcnt(8)\n"
    "v_mfma_f32_32x32x16_bf16 v[32:47], v[200:203], v[168:171], v[32:47]\n"
    "s_barrier\n"
    "ds_read_b64_tr_b16 v[192:193], v242 offset:49152\n"
    "ds_read_b64_tr_b16 v[194:195], v242 offset:57344\n"
    "v_exp_f32 v134, v134\n"
    "v_add_f32 v247, v247, v132\n"
    "v_mfma_f32_32x32x16_bf16 v[48:63], v[204:207], v[168:171], v[48:63]\n"
    "ds_read_b64_tr_b16 v[196:197], v242 offset:49664\n"
    "ds_read_b64_tr_b16 v[198:199], v242 offset:57856\n"
    "v_exp_f32 v135, v135\n"
    "v_add_f32 v248, v248, v133\n"
    "s_waitcnt lgkmcnt(8)\n"
    "v_mfma_f32_32x32x16_bf16 v[0:15], v[208:211], v[172:175], v[0:15]\n"
    "ds_read_b64_tr_b16 v[200:201], v242 offset:50176\n"
    "ds_read_b64_tr_b16 v[202:203], v242 offset:58368\n"
    "s_add_u32 m0, s61, 16384\n"
    "s_add_u32 s66, s62, 49152\n"
    "buffer_load_dwordx4 v243, s[36:39], s66 offen nt lds\n"
    "buffer_load_dwordx4 v244, s[36:39], s66 offen offset:2048 nt lds\n"
    "v_mfma_f32_32x32x16_bf16 v[16:31], v[212:215], v[172:175], v[16:31]\n"
    "ds_read_b64_tr_b16 v[204:205], v242 offset:50688\n"
    "ds_read_b64_tr_b16 v[206:207], v242 offset:58880\n"
    "s_add_u32 m0, s61, 17280\n"
    "s_nop 0\n"
    "buffer_load_dwordx4 v243, s[36:39], s66 offen offset:128 nt lds\n"
    "buffer_load_dwordx4 v244, s[36:39], s66 offen offset:2176 nt lds\n"
    "s_waitcnt lgkmcnt(8)\n"
    "v_mfma_f32_32x32x16_bf16 v[32:47], v[216:219], v[172:175], v[32:47]\n"
    "ds_read_b64_tr_b16 v[208:209], v242 offset:51200\n"
    "ds_read_b64_tr_b16 v[210:211], v242 offset:59392\n"
    "v_exp_f32 v136, v136\n"
    "v_add_f32 v247, v247, v134\n"
    "v_mfma_f32_32x32x16_bf16 v[48:63], v[220:223], v[172:175], v[48:63]\n"
    "ds_read_b64_tr_b16 v[212:213], v242 offset:51712\n"
    "ds_read_b64_tr_b16 v[214:215], v242 offset:59904\n"
    "s_cmp_lg_u32 s69, 0\n"
    "s_cbranch_scc1 L_stub2_lp_1\n"
    "L_back2_lp_1:\n"
    "s_waitcnt lgkmcnt(8)\n"
    "v_mfma_f32_32x32x16_bf16 a[0:15], v[192:195], v[68:71], a[0:15]\n"
    "ds_read_b64_tr_b16 v[216:217], v242 offset:52224\n"
    "ds_read_b64_tr_b16 v[218:219], v242 offset:60416\n"
    "v_exp_f32 v137, v137\n"
    "v_add_f32 v248, v248, v135\n"
    "v_mfma_f32_32x32x16_bf16 a[16:31], v[196:199], v[68:71], a[16:31]\n"
    "ds_read_b64_tr_b16 v[220:221], v242 offset:52736\n"
    "ds_read_b64_tr_b16 v[222:223], v242 offset:60928\n"
    "v_exp_f32 v138, v138\n"
    "v_add_f32 v247, v247, v136\n"
    "s_waitcnt lgkmcnt(8)\n"
    "v_mfma_f32_32x32x16_bf16 a[32:47], v[200:203], v[68:71], a[32:47]\n"
    "ds_read_b64_tr_b16 v[192:193], v242 offset:53248\n"
    "ds_read_b64_tr_b16 v[194:195], v242 offset:61440\n"
    "v_exp_f32 v139, v139\n"
    "v_add_f32 v248, v248, v137\n"
    "v_mfma_f32_32x32x16_bf16 a[48:63], v[204:207], v[68:71], a[48:63]\n"
    "ds_read_b64_tr_b16 v[196:197], v242 offset:53760\n"
    "ds_read_b64_tr_b16 v[198:199], v242 offset:61952\n"
    "v_exp_f32 v140, v140\n"
    "v_add_f32 v247, v247, v138\n"
    "s_waitcnt lgkmcnt(8)\n"
    "v_mfma_f32_32x32x16_bf16 a[64:79], v[208:211], v[68:71], a[64:79]\n"
    "ds_read_b64_tr_b16 v[200:201], v242 offset:54272\n"
    "ds_read_b64_tr_b16 v[202:203], v242 offset:62464\n"
    "v_exp_f32 v141, v141\n"
    "v_add_f32 v248, v248, v139\n"
    "v_mfma_f32_32x32x16_bf16 a[80:95], v[212:215], v[68:71], a[80:95]\n"
    "ds_read_b64_tr_b16 v[204:205], v242 offset:54784\n"
    "ds_read_b64_tr_b16 v[206:207], v242 offset:62976\n"
    "v_exp_f32 v142, v142\n"
    "v_add_f32 v247, v247, v140\n"
    "s_waitcnt lgkmcnt(8)\n"
    "v_mfma_f32_32x32x16_bf16 a[96:111], v[216:219], v[68:71], a[96:111]\n"
    "ds_read_b64_tr_b16 v[208:209], v242 offset:55296\n"
    "ds_read_b64_tr_b16 v[210:211], v242 offset:63488\n"
    "v_exp_f32 v143, v143\n"
    "v_add_f32 v248, v248, v141\n"
    "v_add_f32 v247, v247, v142\n"
    "v_mfma_f32_32x32x16_bf16 a[112:127], v[220:223], v[68:71], a[112:127]\n"
    "ds_read_b64_tr_b16 v[212:213], v242 offset:55808\n"
    "ds_read_b64_tr_b16 v[214:215], v242 offset:64000\n"
    "v_add_f32 v248, v248, v143\n"
    "v_cvt_pk_bf16_f32 v160, v128, v129\n"
    "v_cvt_pk_bf16_f32 v161, v130, v131\n"
    "s_waitcnt lgkmcnt(8)\n"
    "v_mfma_f32_32x32x16_bf16 a[128:143], v[192:195], v[68:71], a[128:143]\n"
    "ds_read_b64_tr_b16 v[216:217], v242 offset:56320\n"
    "ds_read_b64_tr_b16 v[218:219], v242 offset:64512\n"
    "v_cvt_pk_bf16_f32 v162, v132, v133\n"
    "v_cvt_pk_bf16_f32 v163, v134, v135\n"
    "v_cvt_pk_bf16_f32 v164, v136, v137\n"
    "v_cvt_pk_bf16_f32 v165, v138, v139\n"
    "v_mfma_f32_32x32x16_bf16 a[144:159], v[196:199], v[68:71], a[144:159]\n"
    "ds_read_b64_tr_b16 v[220:221], v242 offset:56832\n"
    "ds_read_b64_tr_b16 v[222:223], v242 offset:65024\n"
    "v_cvt_pk_bf16_f32 v166, v140, v141\n"
    "v_cvt_pk_bf16_f32 v167, v142, v143\n"
    "s_waitcnt lgkmcnt(8)\n"
    "v_mfma_f32_32x32x16_bf16 a[160:175], v[200:203], v[68:71], a[160:175]\n"
    "s_waitcnt vmcnt(8)\n"
    "s_barrier\n"
    "ds_read_b128 v[192:195], v240 offset:0\n"
    "v_exp_f32 v144, v144\n"
    "v_exp_f32 v145, v145\n"
    "v_mfma_f32_32x32x16_bf16 a[176:191], v[204:207], v[68:71], a[176:191]\n"
    "ds_read_b128 v[196:199], v241 offset:0\n"
    "v_exp_f32 v146, v146\n"
    "v_add_f32 v247, v247, v144\n"
    "s_waitcnt lgkmcnt(6)\n"
    "v_mfma_f32_32x32x16_bf16 a[192:207], v[208:211], v[68:71], a[192:207]\n"
    "ds_read_b128 v[200:203], v240 offset:512\n"
    "v_exp_f32 v147, v147\n"
    "v_add_f32 v248, v248, v145\n"
    "v_exp_f32 v148, v148\n"
    "v_mfma_f32_32x32x16_bf16 a[208:223], v[212:215], v[68:71], a[208:223]\n"
    "ds_read_b128 v[204:207], v241 offset:512\n"
    "v_add_f32 v247, v247, v146\n"
    "v_exp_f32 v149, v149\n"
    "v_add_f32 v248, v248, v147\n"
    "s_waitcnt lgkmcnt(4)\n"
    "v_mfma_f32_32x32x16_bf16 a[224:239], v[216:219], v[68:71], a[224:239]\n"
    "ds_read_b128 v[208:211], v240 offset:1024\n"
    "v_exp_f32 v150, v150\n"
    "v_add_f32 v247, v247, v148\n"
    "v_exp_f32 v151, v151\n"
    "v_mfma_f32_32x32x16_bf16 a[240:255], v[220:223], v[68:71], a[240:255]\n"
    "ds_read_b128 v[212:215], v241 offset:1024\n"
    "s_waitcnt lgkmcnt(4)\n"
    "v_mfma_f32_32x32x16_bf16 v[128:143], v[192:195], v[96:99], v[176:191]\n"
    "ds_read_b128 v[216:219], v240 offset:1536\n"
    "v_add_f32 v248, v248, v149\n"
    "v_exp_f32 v152, v152\n"
    "v_add_f32 v247, v247, v150\n"
    "v_mfma_f32_32x32x16_bf16 v[128:143], v[196:199], v[100:103], v[128:143]\n"
    "ds_read_b128 v[220:223], v241 offset:1536\n"
    "v_exp_f32 v153, v153\n"
    "v_add_f32 v248, v248, v151\n"
    "s_waitcnt lgkmcnt(4)\n"
    "v_mfma_f32_32x32x16_bf16 v[128:143], v[200:203], v[104:107], v[128:143]\n"
    "ds_read_b128 v[192:195], v240 offset:8192\n"
    "v_exp_f32 v154, v154\n"
    "v_add_f32 v247, v247, v152\n"
    "v_exp_f32 v155, v155\n"
    "v_mfma_f32_32x32x16_bf16 v[128:143], v[204:207], v[108:111], v[128:143]\n"
    "ds_read_b128 v[196:199], v241 offset:8192\n"
    "v_add_f32 v248, v248, v153\n"
    "v_exp_f32 v156, v156\n"
    "v_add_f32 v247, v247, v154\n"
    "s_waitcnt lgkmcnt(4)\n"
    "v_mfma_f32_32x32x16_bf16 v[128:143], v[208:211], v[112:115], v[128:143]\n"
    "ds_read_b128 v[200:203], v240 offset:8704\n"
    "v_exp_f32 v157, v157\n"
    "v_add_f32 v248, v248, v155\n"
    "v_exp_f32 v158, v158\n"
    "v_mfma_f32_32x32x16_bf16 v[128:143], v[212:215], v[116:119], v[128:143]\n"
    "ds_read_b128 v[204:207], v241 offset:8704\n"
    "v_add_f32 v247, v247, v156\n"
    "v_exp_f32 v159, v159\n"
    "v_add_f32 v248, v248, v157\n"
    "s_waitcnt lgkmcnt(4)\n"
    "v_mfma_f32_32x32x16_bf16 v[128:143], v[216:219], v[120:123], v[128:143]\n"
    "ds_read_b128 v[208:211], v240 offset:9216\n"
    "v_add_f32 v247, v247, v158\n"
    "v_add_f32 v248, v248, v159\n"
    "v_cvt_pk_bf16_f32 v168, v144, v145\n"
    "v_cvt_pk_bf16_f32 v169, v146, v147\n"
    "v_cvt_pk_bf16_f32 v170, v148, v149\n"
    "v_mfma_f32_32x32x16_bf16 v[128:143], v[220:223], v[124:127], v[128:143]\n"
    "ds_read_b128 v[212:215], v241 offset:9216\n"
    "v_cvt_pk_bf16_f32 v171, v150, v151\n"
    "v_cvt_pk_bf16_f32 v172, v152, v153\n"
    "v_cvt_pk_bf16_f32 v173, v154, v155\n"
    "v_cvt_pk_bf16_f32 v174, v156, v157\n"
    "v_cvt_pk_bf16_f32 v175, v158, v159\n"
    "s_waitcnt lgkmcnt(4)\n"
    "v_mfma_f32_32x32x16_bf16 v[144:159], v[192:195], v[96:99], v[176:191]\n"
    "ds_read_b128 v[216:219], v240 offset:9728\n"
    "s_add_u32 m0, s61, 32768\n"
    "s_add_u32 s66, s62, 32768\n"
    "buffer_load_dwordx4 v245, s[40:43], s66 offen nt lds\n"
    "buffer_load_dwordx4 v245, s[40:43], s66 offen offset:2048 nt lds\n"
    "v_mfma_f32_32x32x16_bf16 v[144:159], v[196:199], v[100:103], v[144:159]\n"
    "ds_read_b128 v[220:223], v241 offset:9728\n"
    "s_add_u32 m0, s61, 33664\n"
    "s_nop 0\n"
    "buffer_load_dwordx4 v245, s[40:43], s66 offen offset:128 nt lds\n"
    "buffer_load_dwordx4 v245, s[40:43], s66 offen offset:2176 nt lds\n"
    "s_waitcnt lgkmcnt(4)\n"
    "v_mfma_f32_32x32x16_bf16 v[144:159], v[200:203], v[104:107], v[144:159]\n"
    "s_waitcnt vmcnt(8)\n"
    "s_barrier\n"
    "ds_read_b64_tr_b16 v[192:193], v242 offset:16384\n"
    "ds_read_b64_tr_b16 v[194:195], v242 offset:18432\n"
    "v_mfma_f32_32x32x16_bf16 v[144:159], v[204:207], v[108:111], v[144:159]\n"
    "ds_read_b64_tr_b16 v[196:197], v242 offset:16896\n"
    "ds_read_b64_tr_b16 v[198:199], v242 offset:18944\n"
    "s_waitcnt lgkmcnt(6)\n"
    "v_mfma_f32_32x32x16_bf16 v[144:159], v[208:211], v[112:115], v[144:159]\n"
    "ds_read_b64_tr_b16 v[200:201], v242 offset:17408\n"
    "ds_read_b64_tr_b16 v[202:203], v242 offset:19456\n"
    "v_max3_f32 v249, v128, v129, v130\n"
    "v_max3_f32 v249, v249, v131, v132\n"
    "v_max3_f32 v249, v249, v133, v134\n"
    "v_max3_f32 v249, v249, v135, v136\n"
    "v_mfma_f32_32x32x16_bf16 v[144:159], v[212:215], v[116:119], v[144:159]\n"
    "ds_read_b64_tr_b16 v[204:205], v242 offset:17920\n"
    "ds_read_b64_tr_b16 v[206:207], v242 offset:19968\n"
    "v_max3_f32 v249, v249, v137, v138\n"
    "v_max3_f32 v249, v249, v139, v140\n"
    "v_max3_f32 v249, v249, v141, v142\n"
    "s_waitcnt lgkmcnt(8)\n"
    "v_mfma_f32_32x32x16_bf16 v[144:159], v[216:219], v[120:123], v[144:159]\n"
    "ds_read_b64_tr_b16 v[208:209], v242 offset:20480\n"
    "ds_read_b64_tr_b16 v[210:211], v242 offset:22528\n"
    "v_max_f32 v249, v249, v143\n"
    "v_mfma_f32_32x32x16_bf16 v[144:159], v[220:223], v[124:127], v[144:159]\n"
    "ds_read_b64_tr_b16 v[212:213], v242 offset:20992\n"
    "ds_read_b64_tr_b16 v[214:215], v242 offset:23040\n"
    "s_waitcnt lgkmcnt(8)\n"
    "v_mfma_f32_32x32x16_bf16 v[0:15], v[192:195], v[160:163], v[0:15]\n"
    "ds_read_b64_tr_b16 v[216:217], v242 offset:21504\n"
    "ds_read_b64_tr_b16 v[218:219], v242 offset:23552\n"
    "v_mfma_f32_32x32x16_bf16 v[16:31], v[196:199], v[160:163], v[16:31]\n"
    "ds_read_b64_tr_b16 v[220:221], v242 offset:22016\n"
    "ds_read_b64_tr_b16 v[222:223], v242 offset:24064\n"
    "s_waitcnt lgkmcnt(8)\n"
    "v_mfma_f32_32x32x16_bf16 v[32:47], v[200:203], v[160:163], v[32:47]\n"
    "ds_read_b64_tr_b16 v[192:193], v242 offset:24576\n"
    "ds_read_b64_tr_b16 v[194:195], v242 offset:26624\n"
    "v_max3_f32 v250, v144, v145, v146\n"
    "v_max3_f32 v250, v250, v147, v148\n"
    "v_max3_f32 v250, v250, v149, v150\n"
    "v_max3_f32 v250, v250, v151, v152\n"
    "v_max3_f32 v250, v250, v153, v154\n"
    "v_mfma_f32_32x32x16_bf16 v[48:63], v[204:207], v[160:163], v[48:63]\n"
    "ds_read_b64_tr_b16 v[196:197], v242 offset:25088\n"
    "ds_read_b64_tr_b16 v[198:199], v242 offset:27136\n"
    "v_max3_f32 v250, v250, v155, v156\n"
    "v_max3_f32 v250, v250, v157, v158\n"
    "v_max_f32 v250, v250, v159\n"
    "v_max_f32 v249, v249, v250\n"
    "s_waitcnt lgkmcnt(8)\n"
    "v_mfma_f32_32x32x16_bf16 v[0:15], v[208:211], v[164:167], v[0:15]\n"
    "ds_read_b64_tr_b16 v[200:201], v242 offset:25600\n"
    "ds_read_b64_tr_b16 v[202:203], v242 offset:27648\n"
    "v_mov_b32 v251, v249\n"
    "s_nop 1\n"
    "v_permlane32_swap_b32 v249, v251\n"
    "v_max_f32 v249, v249, v251\n"
    "v_cmp_lt_f32 vcc, 0x41000000, v249\n"
    "s_nop 0\n"
    "v_mfma_f32_32x32x16_bf16 v[16:31], v[212:215], v[164:167], v[16:31]\n"
    "ds_read_b64_tr_b16 v[204:205], v242 offset:26112\n"
    "ds_read_b64_tr_b16 v[206:207], v242 offset:28160\n"
    "s_cbranch_vccnz L_stub1_lp_2\n"
    "L_back1_lp_2:\n"
    "v_exp_f32 v128, v128\n"
    "s_waitcnt lgkmcnt(8)\n"
    "v_mfma_f32_32x32x16_bf16 v[32:47], v[216:219], v[164:167], v[32:47]\n"
    "ds_read_b64_tr_b16 v[208:209], v242 offset:28672\n"
    "ds_read_b64_tr_b16 v[210:211], v242 offset:30720\n"
    "v_exp_f32 v129, v129\n"
    "v_exp_f32 v130, v130\n"
    "v_mfma_f32_32x32x16_bf16 v[48:63], v[220:223], v[164:167], v[48:63]\n"
    "ds_read_b64_tr_b16 v[212:213], v242 offset:29184\n"
    "ds_read_b64_tr_b16 v[214:215], v242 offset:31232\n"
    "v_add_f32 v247, v247, v128\n"
    "v_exp_f32 v131, v131\n"
    "s_waitcnt lgkmcnt(8)\n"
    "v_mfma_f32_32x32x16_bf16 v[0:15], v[192:195], v[168:171], v[0:15]\n"
    "ds_read_b64_tr_b16 v[216:217], v242 offset:29696\n"
    "ds_read_b64_tr_b16 v[218:219], v242 offset:31744\n"
    "v_add_f32 v248, v248, v129\n"
    "v_exp_f32 v132, v132\n"
    "v_add_f32 v247, v247, v130\n"
    "v_mfma_f32_32x32x16_bf16 v[16:31], v[196:199], v[168:171], v[16:31]\n"
    "ds_read_b64_tr_b16 v[220:221], v242 offset:30208\n"
    "ds_read_b64_tr_b16 v[222:223], v242 offset:32256\n"
    "v_exp_f32 v133, v133\n"
    "v_add_f32 v248, v248, v131\n"
    "s_waitcnt lgkmcnt(8)\n"
    "v_mfma_f32_32x32x16_bf16 v[32:47], v[200:203], v[168:171], v[32:47]\n"
    "s_barrier\n"
    "ds_read_b64_tr_b16 v[192:193], v242 offset:32768\n"
    "ds_read_b64_tr_b16 v[194:195], v242 offset:40960\n"
    "v_exp_f32 v134, v134\n"
    "v_add_f32 v247, v247, v132\n"
    "v_mfma_f32_32x32x16_bf16 v[48:63], v[204:207], v[168:171], v[48:63]\n"
    "ds_read_b64_tr_b16 v[196:197], v242 offset:33280\n"
    "ds_read_b64_tr_b16 v[198:199], v242 offset:41472\n"
    "v_exp_f32 v135, v135\n"
    "v_add_f32 v248, v248, v133\n"
    "s_waitcnt lgkmcnt(8)\n"
    "v_mfma_f32_32x32x16_bf16 v[0:15], v[208:211], v[172:175], v[0:15]\n"
    "ds_read_b64_tr_b16 v[200:201], v242 offset:33792\n"
    "ds_read_b64_tr_b16 v[202:203], v242 offset:41984\n"
    "s_add_u32 m0, s61, 0\n"
    "s_add_u32 s66, s62, 65536\n"
    "buffer_load_dwordx4 v243, s[36:39], s66 offen nt lds\n"
    "buffer_load_dwordx4 v244, s[36:39], s66 offen offset:2048 nt lds\n"
    "v_mfma_f32_32x32x16_bf16 v[16:31], v[212:215], v[172:175], v[16:31]\n"
    "ds_read_b64_tr_b16 v[204:205], v242 offset:34304\n"
    "ds_read_b64_tr_b16 v[206:207], v242 offset:42496\n"
    "s_add_u32 m0, s61, 896\n"
    "s_nop 0\n"
    "buffer_load_dwordx4 v243, s[36:39], s66 offen offset:128 nt lds\n"
    "buffer_load_dwordx4 v244, s[36:39], s66 offen offset:2176 nt lds\n"
    "s_waitcnt lgkmcnt(8)\n"
    "v_mfma_f32_32x32x16_bf16 v[32:47], v[216:219], v[172:175], v[32:47]\n"
    "ds_read_b64_tr_b16 v[208:209], v242 offset:34816\n"
    "ds_read_b64_tr_b16 v[210:211], v242 offset:43008\n"
    "v_exp_f32 v136, v136\n"
    "v_add_f32 v247, v247, v134\n"
    "v_mfma_f32_32x32x16_bf16 v[48:63], v[220:223], v[172:175], v[48:63]\n"
    "ds_read_b64_tr_b16 v[212:213], v242 offset:35328\n"
    "ds_read_b64_tr_b16 v[214:215], v242 offset:43520\n"
    "s_cmp_lg_u32 s69, 0\n"
    "s_cbranch_scc1 L_stub2_lp_2\n"
    "L_back2_lp_2:\n"
    "s_waitcnt lgkmcnt(8)\n"
    "v_mfma_f32_32x32x16_bf16 a[0:15], v[192:195], v[72:75], a[0:15]\n"
    "ds_read_b64_tr_b16 v[216:217], v242 offset:35840\n"
    "ds_read_b64_tr_b16 v[218:219], v242 offset:44032\n"
    "v_exp_f32 v137, v137\n"
    "v_add_f32 v248, v248, v135\n"
    "v_mfma_f32_32x32x16_bf16 a[16:31], v[196:199], v[72:75], a[16:31]\n"
    "ds_read_b64_tr_b16 v[220:221], v242 offset:36352\n"
    "ds_read_b64_tr_b16 v[222:223], v242 offset:44544\n"
    "v_exp_f32 v138, v138\n"
    "v_add_f32 v247, v247, v136\n"
    "s_waitcnt lgkmcnt(8)\n"
    "v_mfma_f32_32x32x16_bf16 a[32:47], v[200:203], v[72:75], a[32:47]\n"
    "ds_read_b64_tr_b16 v[192:193], v242 offset:36864\n"
    "ds_read_b64_tr_b16 v[194:195], v242 offset:45056\n"
    "v_exp_f32 v139, v139\n"
    "v_add_f32 v248, v248, v137\n"
    "v_mfma_f32_32x32x16_bf16 a[48:63], v[204:207], v[72:75], a[48:63]\n"
    "ds_read_b64_tr_b16 v[196:197], v242 offset:37376\n"
    "ds_read_b64_tr_b16 v[198:199], v242 offset:45568\n"
    "v_exp_f32 v140, v140\n"
    "v_add_f32 v247, v247, v138\n"
    "s_waitcnt lgkmcnt(8)\n"
    "v_mfma_f32_32x32x16_bf16 a[64:79], v[208:211], v[72:75], a[64:79]\n"
    "ds_read_b64_tr_b16 v[200:201], v242 offset:37888\n"
    "ds_read_b64_tr_b16 v[202:203], v242 offset:46080\n"
    "v_exp_f32 v141, v141\n"
    "v_add_f32 v248, v248, v139\n"
    "v_mfma_f32_32x32x16_bf16 a[80:95], v[212:215], v[72:75], a[80:95]\n"
    "ds_read_b64_tr_b16 v[204:205], v242 offset:38400\n"
    "ds_read_b64_tr_b16 v[206:207], v242 offset:46592\n"
    "v_exp_f32 v142, v142\n"
    "v_add_f32 v247, v247, v140\n"
    "s_waitcnt lgkmcnt(8)\n"
    "v_mfma_f32_32x32x16_bf16 a[96:111], v[216:219], v[72:75], a[96:111]\n"
    "ds_read_b64_tr_b16 v[208:209], v242 offset:38912\n"
    "ds_read_b64_tr_b16 v[210:211], v242 offset:47104\n"
    "v_exp_f32 v143, v143\n"
    "v_add_f32 v248, v248, v141\n"
    "v_add_f32 v247, v247, v142\n"
    "v_mfma_f32_32x32x16_bf16 a[112:127], v[220:223], v[72:75], a[112:127]\n"
    "ds_read_b64_tr_b16 v[212:213], v242 offset:39424\n"
    "ds_read_b64_tr_b16 v[214:215], v242 offset:47616\n"
    "v_add_f32 v248, v248, v143\n"
    "v_cvt_pk_bf16_f32 v160, v128, v129\n"
    "v_cvt_pk_bf16_f32 v161, v130, v131\n"
    "s_waitcnt lgkmcnt(8)\n"
    "v_mfma_f32_32x32x16_bf16 a[128:143], v[192:195], v[72:75], a[128:143]\n"
    "ds_read_b64_tr_b16 v[216:217], v242 offset:39936\n"
    "ds_read_b64_tr_b16 v[218:219], v242 offset:48128\n"
    "v_cvt_pk_bf16_f32 v162, v132, v133\n"
    "v_cvt_pk_bf16_f32 v163, v134, v135\n"
    "v_cvt_pk_bf16_f32 v164, v136, v137\n"
    "v_cvt_pk_bf16_f32 v165, v138, v139\n"
    "v_mfma_f32_32x32x16_bf16 a[144:159], v[196:199], v[72:75], a[144:159]\n"
    "ds_read_b64_tr_b16 v[220:221], v242 offset:40448\n"
    "ds_read_b64_tr_b16 v[222:223], v242 offset:48640\n"
    "v_cvt_pk_bf16_f32 v166, v140, v141\n"
    "v_cvt_pk_bf16_f32 v167, v142, v143\n"
    "s_waitcnt lgkmcnt(8)\n"
    "v_mfma_f32_32x32x16_bf16 a[160:175], v[200:203], v[72:75], a[160:175]\n"
    "s_waitcnt vmcnt(8)\n"
    "s_barrier\n"
    "ds_read_b128 v[192:195], v240 offset:16384\n"
    "v_exp_f32 v144, v144\n"
    "v_exp_f32 v145, v145\n"
    "v_mfma_f32_32x32x16_bf16 a[176:191], v[204:207], v[72:75], a[176:191]\n"
    "ds_read_b128 v[196:199], v241 offset:16384\n"
    "v_exp_f32 v146, v146\n"
    "v_add_f32 v247, v247, v144\n"
    "s_waitcnt lgkmcnt(6)\n"
    "v_mfma_f32_32x32x16_bf16 a[192:207], v[208:211], v[72:75], a[192:207]\n"
    "ds_read_b128 v[200:203], v240 offset:16896\n"
    "v_exp_f32 v147, v147\n"
    "v_add_f32 v248, v248, v145\n"
    "v_exp_f32 v148, v148\n"
    "v_mfma_f32_32x32x16_bf16 a[208:223], v[212:215], v[72:75], a[208:223]\n"
    "ds_read_b128 v[204:207], v241 offset:16896\n"
    "v_add_f32 v247, v247, v146\n"
    "v_exp_f32 v149, v149\n"
    "v_add_f32 v248, v248, v147\n"
    "s_waitcnt lgkmcnt(4)\n"
    "v_mfma_f32_32x32x16_bf16 a[224:239], v[216:219], v[72:75], a[224:239]\n"
    "ds_read_b128 v[208:211], v240 offset:17408\n"
    "v_exp_f32 v150, v150\n"
    "v_add_f32 v247, v247, v148\n"
    "v_exp_f32 v151, v151\n"
    "v_mfma_f32_32x32x16_bf16 a[240:255], v[220:223], v[72:75], a[240:255]\n"
    "ds_read_b128 v[212:215], v241 offset:17408\n"
    "s_waitcnt lgkmcnt(4)\n"
    "v_mfma_f32_32x32x16_bf16 v[128:143], v[192:195], v[96:99], v[176:191]\n"
    "ds_read_b128 v[216:219], v240 offset:17920\n"
    "v_add_f32 v248, v248, v149\n"
    "v_exp_f32 v152, v152\n"
    "v_add_f32 v247, v247, v150\n"
    "v_mfma_f32_32x32x16_bf16 v[128:143], v[196:199], v[100:103], v[128:143]\n"
    "ds_read_b128 v[220:223], v241 offset:17920\n"
    "v_exp_f32 v153, v153\n"
    "v_add_f32 v248, v248, v151\n"
    "s_waitcnt lgkmcnt(4)\n"
    "v_mfma_f32_32x32x16_bf16 v[128:143], v[200:203], v[104:107], v[128:143]\n"
    "ds_read_b128 v[192:195], v240 offset:24576\n"
    "v_exp_f32 v154, v154\n"
    "v_add_f32 v247, v247, v152\n"
    "v_exp_f32 v155, v155\n"
    "v_mfma_f32_32x32x16_bf16 v[128:143], v[204:207], v[108:111], v[128:143]\n"
    "ds_read_b128 v[196:199], v241 offset:24576\n"
    "v_add_f32 v248, v248, v153\n"
    "v_exp_f32 v156, v156\n"
    "v_add_f32 v247, v247, v154\n"
    "s_waitcnt lgkmcnt(4)\n"
    "v_mfma_f32_32x32x16_bf16 v[128:143], v[208:211], v[112:115], v[128:143]\n"
    "ds_read_b128 v[200:203], v240 offset:25088\n"
    "v_exp_f32 v157, v157\n"
    "v_add_f32 v248, v248, v155\n"
    "v_exp_f32 v158, v158\n"
    "v_mfma_f32_32x32x16_bf16 v[128:143], v[212:215], v[116:119], v[128:143]\n"
    "ds_read_b128 v[204:207], v241 offset:25088\n"
    "v_add_f32 v247, v247, v156\n"
    "v_exp_f32 v159, v159\n"
    "v_add_f32 v248, v248, v157\n"
    "s_waitcnt lgkmcnt(4)\n"
    "v_mfma_f32_32x32x16_bf16 v[128:143], v[216:219], v[120:123], v[128:143]\n"
    "ds_read_b128 v[208:211], v240 offset:25600\n"
    "v_add_f32 v247, v247, v158\n"
    "v_add_f32 v248, v248, v159\n"
    "v_cvt_pk_bf16_f32 v168, v144, v145\n"
    "v_cvt_pk_bf16_f32 v169, v146, v147\n"
    "v_cvt_pk_bf16_f32 v170, v148, v149\n"
    "v_mfma_f32_32x32x16_bf16 v[128:143], v[220:223], v[124:127], v[128:143]\n"
    "ds_read_b128 v[212:215], v241 offset:25600\n"
    "v_cvt_pk_bf16_f32 v171, v150, v151\n"
    "v_cvt_pk_bf16_f32 v172, v152, v153\n"
    "v_cvt_pk_bf16_f32 v173, v154, v155\n"
    "v_cvt_pk_bf16_f32 v174, v156, v157\n"
    "v_cvt_pk_bf16_f32 v175, v158, v159\n"
    "s_waitcnt lgkmcnt(4)\n"
    "v_mfma_f32_32x32x16_bf16 v[144:159], v[192:195], v[96:99], v[176:191]\n"
    "ds_read_b128 v[216:219], v240 offset:26112\n"
    "s_add_u32 m0, s61, 49152\n"
    "s_add_u32 s66, s62, 49152\n"
    "buffer_load_dwordx4 v245, s[40:43], s66 offen nt lds\n"
    "buffer_load_dwordx4 v245, s[40:43], s66 offen offset:2048 nt lds\n"
    "v_mfma_f32_32x32x16_bf16 v[144:159], v[196:199], v[100:103], v[144:159]\n"
    "ds_read_b128 v[220:223], v241 offset:26112\n"
    "s_add_u32 m0, s61, 50048\n"
    "s_nop 0\n"
    "buffer_load_dwordx4 v245, s[40:43], s66 offen offset:128 nt lds\n"
    "buffer_load_dwordx4 v245, s[40:43], s66 offen offset:2176 nt lds\n"
    "s_waitcnt lgkmcnt(4)\n"
    "v_mfma_f32_32x32x16_bf16 v[144:159], v[200:203], v[104:107], v[144:159]\n"
    "s_waitcnt vmcnt(8)\n"
    "s_barrier\n"
    "ds_read_b64_tr_b16 v[192:193], v242 offset:0\n"
    "ds_read_b64_tr_b16 v[194:195], v242 offset:2048\n"
    "v_mfma_f32_32x32x16_bf16 v[144:159], v[204:207], v[108:111], v[144:159]\n"
    "ds_read_b64_tr_b16 v[196:197], v242 offset:512\n"
    "ds_read_b64_tr_b16 v[198:199], v242 offset:2560\n"
    "s_waitcnt lgkmcnt(6)\n"
    "v_mfma_f32_32x32x16_bf16 v[144:159], v[208:211], v[112:115], v[144:159]\n"
    "ds_read_b64_tr_b16 v[200:201], v242 offset:1024\n"
    "ds_read_b64_tr_b16 v[202:203], v242 offset:3072\n"
    "v_max3_f32 v249, v128, v129, v130\n"
    "v_max3_f32 v249, v249, v131, v132\n"
    "v_max3_f32 v249, v249, v133, v134\n"
    "v_max3_f32 v249, v249, v135, v136\n"
    "v_mfma_f32_32x32x16_bf16 v[144:159], v[212:215], v[116:119], v[144:159]\n"
    "ds_read_b64_tr_b16 v[204:205], v242 offset:1536\n"
    "ds_read_b64_tr_b16 v[206:207], v242 offset:3584\n"
    "v_max3_f32 v249, v249, v137, v138\n"
    "v_max3_f32 v249, v249, v139, v140\n"
    "v_max3_f32 v249, v249, v141, v142\n"
    "s_waitcnt lgkmcnt(8)\n"
    "v_mfma_f32_32x32x16_bf16 v[144:159], v[216:219], v[120:123], v[144:159]\n"
    "ds_read_b64_tr_b16 v[208:209], v242 offset:4096\n"
    "ds_read_b64_tr_b16 v[210:211], v242 offset:6144\n"
    "v_max_f32 v249, v249, v143\n"
    "v_mfma_f32_32x32x16_bf16 v[144:159], v[220:223], v[124:127], v[144:159]\n"
    "ds_read_b64_tr_b16 v[212:213], v242 offset:4608\n"
    "ds_read_b64_tr_b16 v[214:215], v242 offset:6656\n"
    "s_waitcnt lgkmcnt(8)\n"
    "v_mfma_f32_32x32x16_bf16 v[0:15], v[192:195], v[160:163], v[0:15]\n"
    "ds_read_b64_tr_b16 v[216:217], v242 offset:5120\n"
    "ds_read_b64_tr_b16 v[218:219], v242 offset:7168\n"
    "v_mfma_f32_32x32x16_bf16 v[16:31], v[196:199], v[160:163], v[16:31]\n"
    "ds_read_b64_tr_b16 v[220:221], v242 offset:5632\n"
    "ds_read_b64_tr_b16 v[222:223], v242 offset:7680\n"
    "s_waitcnt lgkmcnt(8)\n"
    "v_mfma_f32_32x32x16_bf16 v[32:47], v[200:203], v[160:163], v[32:47]\n"
    "ds_read_b64_tr_b16 v[192:193], v242 offset:8192\n"
    "ds_read_b64_tr_b16 v[194:195], v242 offset:10240\n"
    "v_max3_f32 v250, v144, v145, v146\n"
    "v_max3_f32 v250, v250, v147, v148\n"
    "v_max3_f32 v250, v250, v149, v150\n"
    "v_max3_f32 v250, v250, v151, v152\n"
    "v_max3_f32 v250, v250, v153, v154\n"
    "v_mfma_f32_32x32x16_bf16 v[48:63], v[204:207], v[160:163], v[48:63]\n"
    "ds_read_b64_tr_b16 v[196:197], v242 offset:8704\n"
    "ds_read_b64_tr_b16 v[198:199], v242 offset:10752\n"
    "v_max3_f32 v250, v250, v155, v156\n"
    "v_max3_f32 v250, v250, v157, v158\n"
    "v_max_f32 v250, v250, v159\n"
    "v_max_f32 v249, v249, v250\n"
    "s_waitcnt lgkmcnt(8)\n"
    "v_mfma_f32_32x32x16_bf16 v[0:15], v[208:211], v[164:167], v[0:15]\n"
    "ds_read_b64_tr_b16 v[200:201], v242 offset:9216\n"
    "ds_read_b64_tr_b16 v[202:203], v242 offset:11264\n"
    "v_mov_b32 v251, v249\n"
    "s_nop 1\n"
    "v_permlane32_swap_b32 v249, v251\n"
    "v_max_f32 v249, v249, v251\n"
    "v_cmp_lt_f32 vcc, 0x41000000, v249\n"
    "s_nop 0\n"
    "v_mfma_f32_32x32x16_bf16 v[16:31], v[212:215], v[164:167], v[16:31]\n"
    "ds_read_b64_tr_b16 v[204:205], v242 offset:9728\n"
    "ds_read_b64_tr_b16 v[206:207], v242 offset:11776\n"
    "s_cbranch_vccnz L_stub1_lp_3\n"
    "L_back1_lp_3:\n"
    "v_exp_f32 v128, v128\n"
    "s_waitcnt lgkmcnt(8)\n"
    "v_mfma_f32_32x32x16_bf16 v[32:47], v[216:219], v[164:167], v[32:47]\n"
    "ds_read_b64_tr_b16 v[208:209], v242 offset:12288\n"
    "ds_read_b64_tr_b16 v[210:211], v242 offset:14336\n"
    "v_exp_f32 v129, v129\n"
    "v_exp_f32 v130, v130\n"
    "v_mfma_f32_32x32x16_bf16 v[48:63], v[220:223], v[164:167], v[48:63]\n"
    "ds_read_b64_tr_b16 v[212:213], v242 offset:12800\n"
    "ds_read_b64_tr_b16 v[214:215], v242 offset:14848\n"
    "v_add_f32 v247, v247, v128\n"
    "v_exp_f32 v131, v131\n"
    "s_waitcnt lgkmcnt(8)\n"
    "v_mfma_f32_32x32x16_bf16 v[0:15], v[192:195], v[168:171], v[0:15]\n"
    "ds_read_b64_tr_b16 v[216:217], v242 offset:13312\n"
    "ds_read_b64_tr_b16 v[218:219], v242 offset:15360\n"
    "v_add_f32 v248, v248, v129\n"
    "v_exp_f32 v132, v132\n"
    "v_add_f32 v247, v247, v130\n"
    "v_mfma_f32_32x32x16_bf16 v[16:31], v[196:199], v[168:171], v[16:31]\n"
    "ds_read_b64_tr_b16 v[220:221], v242 offset:13824\n"
    "ds_read_b64_tr_b16 v[222:223], v242 offset:15872\n"
    "v_exp_f32 v133, v133\n"
    "v_add_f32 v248, v248, v131\n"
    "s_waitcnt lgkmcnt(8)\n"
    "v_mfma_f32_32x32x16_bf16 v[32:47], v[200:203], v[168:171], v[32:47]\n"
    "s_barrier\n"
    "ds_read_b64_tr_b16 v[192:193], v242 offset:49152\n"
    "ds_read_b64_tr_b16 v[194:195], v242 offset:57344\n"
    "v_exp_f32 v134, v134\n"
    "v_add_f32 v247, v247, v132\n"
    "v_mfma_f32_32x32x16_bf16 v[48:63], v[204:207], v[168:171], v[48:63]\n"
    "ds_read_b64_tr_b16 v[196:197], v242 offset:49664\n"
    "ds_read_b64_tr_b16 v[198:199], v242 offset:57856\n"
    "v_exp_f32 v135, v135\n"
    "v_add_f32 v248, v248, v133\n"
    "s_waitcnt lgkmcnt(8)\n"
    "v_mfma_f32_32x32x16_bf16 v[0:15], v[208:211], v[172:175], v[0:15]\n"
    "ds_read_b64_tr_b16 v[200:201], v242 offset:50176\n"
    "ds_read_b64_tr_b16 v[202:203], v242 offset:58368\n"
    "s_add_u32 m0, s61, 16384\n"
    "s_add_u32 s66, s62, 81920\n"
    "buffer_load_dwordx4 v243, s[36:39], s66 offen nt lds\n"
    "buffer_load_dwordx4 v244, s[36:39], s66 offen offset:2048 nt lds\n"
    "v_mfma_f32_32x32x16_bf16 v[16:31], v[212:215], v[172:175], v[16:31]\n"
    "ds_read_b64_tr_b16 v[204:205], v242 offset:50688\n"
    "ds_read_b64_tr_b16 v[206:207], v242 offset:58880\n"
    "s_add_u32 m0, s61, 17280\n"
    "s_nop 0\n"
    "buffer_load_dwordx4 v243, s[36:39], s66 offen offset:128 nt lds\n"
    "buffer_load_dwordx4 v244, s[36:39], s66 offen offset:2176 nt lds\n"
    "s_waitcnt lgkmcnt(8)\n"
    "v_mfma_f32_32x32x16_bf16 v[32:47], v[216:219], v[172:175], v[32:47]\n"
    "ds_read_b64_tr_b16 v[208:209], v242 offset:51200\n"
    "ds_read_b64_tr_b16 v[210:211], v242 offset:59392\n"
    "v_exp_f32 v136, v136\n"
    "v_add_f32 v247, v247, v134\n"
    "v_mfma_f32_32x32x16_bf16 v[48:63], v[220:223], v[172:175], v[48:63]\n"
    "ds_read_b64_tr_b16 v[212:213], v242 offset:51712\n"
    "ds_read_b64_tr_b16 v[214:215], v242 offset:59904\n"
    "s_cmp_lg_u32 s69, 0\n"
    "s_cbranch_scc1 L_stub2_lp_3\n"
    "L_back2_lp_3:\n"
    "s_waitcnt lgkmcnt(8)\n"
    "v_mfma_f32_32x32x16_bf16 a[0:15], v[192:195], v[76:79], a[0:15]\n"
    "ds_read_b64_tr_b16 v[216:217], v242 offset:52224\n"
    "ds_read_b64_tr_b16 v[218:219], v242 offset:60416\n"
    "v_exp_f32 v137, v137\n"
    "v_add_f32 v248, v248, v135\n"
    "v_mfma_f32_32x32x16_bf16 a[16:31], v[196:199], v[76:79], a[16:31]\n"
    "ds_read_b64_tr_b16 v[220:221], v242 offset:52736\n"
    "ds_read_b64_tr_b16 v[222:223], v242 offset:60928\n"
    "v_exp_f32 v138, v138\n"
    "v_add_f32 v247, v247, v136\n"
    "s_waitcnt lgkmcnt(8)\n"
    "v_mfma_f32_32x32x16_bf16 a[32:47], v[200:203], v[76:79], a[32:47]\n"
    "ds_read_b64_tr_b16 v[192:193], v242 offset:53248\n"
    "ds_read_b64_tr_b16 v[194:195], v242 offset:61440\n"
    "v_exp_f32 v139, v139\n"
    "v_add_f32 v248, v248, v137\n"
    "v_mfma_f32_32x32x16_bf16 a[48:63], v[204:207], v[76:79], a[48:63]\n"
    "ds_read_b64_tr_b16 v[196:197], v242 offset:53760\n"
    "ds_read_b64_tr_b16 v[198:199], v242 offset:61952\n"
    "v_exp_f32 v140, v140\n"
    "v_add_f32 v247, v247, v138\n"
    "s_waitcnt lgkmcnt(8)\n"
    "v_mfma_f32_32x32x16_bf16 a[64:79], v[208:211], v[76:79], a[64:79]\n"
    "ds_read_b64_tr_b16 v[200:201], v242 offset:54272\n"
    "ds_read_b64_tr_b16 v[202:203], v242 offset:62464\n"
    "v_exp_f32 v141, v141\n"
    "v_add_f32 v248, v248, v139\n"
    "v_mfma_f32_32x32x16_bf16 a[80:95], v[212:215], v[76:79], a[80:95]\n"
    "ds_read_b64_tr_b16 v[204:205], v242 offset:54784\n"
    "ds_read_b64_tr_b16 v[206:207], v242 offset:62976\n"
    "v_exp_f32 v142, v142\n"
    "v_add_f32 v247, v247, v140\n"
    "s_waitcnt lgkmcnt(8)\n"
    "v_mfma_f32_32x32x16_bf16 a[96:111], v[216:219], v[76:79], a[96:111]\n"
    "ds_read_b64_tr_b16 v[208:209], v242 offset:55296\n"
    "ds_read_b64_tr_b16 v[210:211], v242 offset:63488\n"
    "v_exp_f32 v143, v143\n"
    "v_add_f32 v248, v248, v141\n"
    "v_add_f32 v247, v247, v142\n"
    "v_mfma_f32_32x32x16_bf16 a[112:127], v[220:223], v[76:79], a[112:127]\n"
    "ds_read_b64_tr_b16 v[212:213], v242 offset:55808\n"
    "ds_read_b64_tr_b16 v[214:215], v242 offset:64000\n"
    "v_add_f32 v248, v248, v143\n"
    "v_cvt_pk_bf16_f32 v160, v128, v129\n"
    "v_cvt_pk_bf16_f32 v161, v130, v131\n"
    "s_waitcnt lgkmcnt(8)\n"
    "v_mfma_f32_32x32x16_bf16 a[128:143], v[192:195], v[76:79], a[128:143]\n"
    "ds_read_b64_tr_b16 v[216:217], v242 offset:56320\n"
    "ds_read_b64_tr_b16 v[218:219], v242 offset:64512\n"
    "v_cvt_pk_bf16_f32 v162, v132, v133\n"
    "v_cvt_pk_bf16_f32 v163, v134, v135\n"
    "v_cvt_pk_bf16_f32 v164, v136, v137\n"
    "v_cvt_pk_bf16_f32 v165, v138, v139\n"
    "v_mfma_f32_32x32x16_bf16 a[144:159], v[196:199], v[76:79], a[144:159]\n"
    "ds_read_b64_tr_b16 v[220:221], v242 offset:56832\n"
    "ds_read_b64_tr_b16 v[222:223], v242 offset:65024\n"
    "v_cvt_pk_bf16_f32 v166, v140, v141\n"
    "v_cvt_pk_bf16_f32 v167, v142, v143\n"
    "s_waitcnt lgkmcnt(8)\n"
    "v_mfma_f32_32x32x16_bf16 a[160:175], v[200:203], v[76:79], a[160:175]\n"
    "s_waitcnt vmcnt(8)\n"
    "s_barrier\n"
    "ds_read_b128 v[192:195], v240 offset:0\n"
    "v_exp_f32 v144, v144\n"
    "v_exp_f32 v145, v145\n"
    "v_mfma_f32_32x32x16_bf16 a[176:191], v[204:207], v[76:79], a[176:191]\n"
    "ds_read_b128 v[196:199], v241 offset:0\n"
    "v_exp_f32 v146, v146\n"
    "v_add_f32 v247, v247, v144\n"
    "s_waitcnt lgkmcnt(6)\n"
    "v_mfma_f32_32x32x16_bf16 a[192:207], v[208:211], v[76:79], a[192:207]\n"
    "ds_read_b128 v[200:203], v240 offset:512\n"
    "v_exp_f32 v147, v147\n"
    "v_add_f32 v248, v248, v145\n"
    "v_exp_f32 v148, v148\n"
    "v_mfma_f32_32x32x16_bf16 a[208:223], v[212:215], v[76:79], a[208:223]\n"
    "ds_read_b128 v[204:207], v241 offset:512\n"
    "v_add_f32 v247, v247, v146\n"
    "v_exp_f32 v149, v149\n"
    "v_add_f32 v248, v248, v147\n"
    "s_waitcnt lgkmcnt(4)\n"
    "v_mfma_f32_32x32x16_bf16 a[224:239], v[216:219], v[76:79], a[224:239]\n"
    "ds_read_b128 v[208:211], v240 offset:1024\n"
    "v_exp_f32 v150, v150\n"
    "v_add_f32 v247, v247, v148\n"
    "v_exp_f32 v151, v151\n"
    "v_mfma_f32_32x32x16_bf16 a[240:255], v[220:223], v[76:79], a[240:255]\n"
    "ds_read_b128 v[212:215], v241 offset:1024\n"
    "s_waitcnt lgkmcnt(4)\n"
    "v_mfma_f32_32x32x16_bf16 v[128:143], v[192:195], v[96:99], v[176:191]\n"
    "ds_read_b128 v[216:219], v240 offset:1536\n"
    "v_add_f32 v248, v248, v149\n"
    "v_exp_f32 v152, v152\n"
    "v_add_f32 v247, v247, v150\n"
    "v_mfma_f32_32x32x16_bf16 v[128:143], v[196:199], v[100:103], v[128:143]\n"
    "ds_read_b128 v[220:223], v241 offset:1536\n"
    "v_exp_f32 v153, v153\n"
    "v_add_f32 v248, v248, v151\n"
    "s_waitcnt lgkmcnt(4)\n"
    "v_mfma_f32_32x32x16_bf16 v[128:143], v[200:203], v[104:107], v[128:143]\n"
    "ds_read_b128 v[192:195], v240 offset:8192\n"
    "v_exp_f32 v154, v154\n"
    "v_add_f32 v247, v247, v152\n"
    "v_exp_f32 v155, v155\n"
    "v_mfma_f32_32x32x16_bf16 v[128:143], v[204:207], v[108:111], v[128:143]\n"
    "ds_read_b128 v[196:199], v241 offset:8192\n"
    "v_add_f32 v248, v248, v153\n"
    "v_exp_f32 v156, v156\n"
    "v_add_f32 v247, v247, v154\n"
    "s_waitcnt lgkmcnt(4)\n"
    "v_mfma_f32_32x32x16_bf16 v[128:143], v[208:211], v[112:115], v[128:143]\n"
    "ds_read_b128 v[200:203], v240 offset:8704\n"
    "v_exp_f32 v157, v157\n"
    "v_add_f32 v248, v248, v155\n"
    "v_exp_f32 v158, v158\n"
    "v_mfma_f32_32x32x16_bf16 v[128:143], v[212:215], v[116:119], v[128:143]\n"
    "ds_read_b128 v[204:207], v241 offset:8704\n"
    "v_add_f32 v247, v247, v156\n"
    "v_exp_f32 v159, v159\n"
    "v_add_f32 v248, v248, v157\n"
    "s_waitcnt lgkmcnt(4)\n"
    "v_mfma_f32_32x32x16_bf16 v[128:143], v[216:219], v[120:123], v[128:143]\n"
    "ds_read_b128 v[208:211], v240 offset:9216\n"
    "v_add_f32 v247, v247, v158\n"
    "v_add_f32 v248, v248, v159\n"
    "v_cvt_pk_bf16_f32 v168, v144, v145\n"
    "v_cvt_pk_bf16_f32 v169, v146, v147\n"
    "v_cvt_pk_bf16_f32 v170, v148, v149\n"
    "v_mfma_f32_32x32x16_bf16 v[128:143], v[220:223], v[124:127], v[128:143]\n"
    "ds_read_b128 v[212:215], v241 offset:9216\n"
    "v_cvt_pk_bf16_f32 v171, v150, v151\n"
    "v_cvt_pk_bf16_f32 v172, v152, v153\n"
    "v_cvt_pk_bf16_f32 v173, v154, v155\n"
    "v_cvt_pk_bf16_f32 v174, v156, v157\n"
    "v_cvt_pk_bf16_f32 v175, v158, v159\n"
    "s_waitcnt lgkmcnt(4)\n"
    "v_mfma_f32_32x32x16_bf16 v[144:159], v[192:195], v[96:99], v[176:191]\n"
    "ds_read_b128 v[216:219], v240 offset:9728\n"
    "s_add_u32 m0, s61, 32768\n"
    "s_add_u32 s66, s62, 65536\n"
    "buffer_load_dwordx4 v245, s[40:43], s66 offen nt lds\n"
    "buffer_load_dwordx4 v245, s[40:43], s66 offen offset:2048 nt lds\n"
    "v_mfma_f32_32x32x16_bf16 v[144:159], v[196:199], v[100:103], v[144:159]\n"
    "ds_read_b128 v[220:223], v241 offset:9728\n"
    "s_add_u32 m0, s61, 33664\n"
    "s_nop 0\n"
    "buffer_load_dwordx4 v245, s[40:43], s66 offen offset:128 nt lds\n"
    "buffer_load_dwordx4 v245, s[40:43], s66 offen offset:2176 nt lds\n"
    "s_waitcnt lgkmcnt(4)\n"
    "v_mfma_f32_32x32x16_bf16 v[144:159], v[200:203], v[104:107], v[144:159]\n"
    "s_waitcnt vmcnt(8)\n"
    "s_barrier\n"
    "ds_read_b64_tr_b16 v[192:193], v242 offset:16384\n"
    "ds_read_b64_tr_b16 v[194:195], v242 offset:18432\n"
    "v_mfma_f32_32x32x16_bf16 v[144:159], v[204:207], v[108:111], v[144:159]\n"
    "ds_read_b64_tr_b16 v[196:197], v242 offset:16896\n"
    "ds_read_b64_tr_b16 v[198:199], v242 offset:18944\n"
    "s_waitcnt lgkmcnt(6)\n"
    "v_mfma_f32_32x32x16_bf16 v[144:159], v[208:211], v[112:115], v[144:159]\n"
    "ds_read_b64_tr_b16 v[200:201], v242 offset:17408\n"
    "ds_read_b64_tr_b16 v[202:203], v242 offset:19456\n"
    "v_max3_f32 v249, v128, v129, v130\n"
    "v_max3_f32 v249, v249, v131, v132\n"
    "v_max3_f32 v249, v249, v133, v134\n"
    "v_max3_f32 v249, v249, v135, v136\n"
    "v_mfma_f32_32x32x16_bf16 v[144:159], v[212:215], v[116:119], v[144:159]\n"
    "ds_read_b64_tr_b16 v[204:205], v242 offset:17920\n"
    "ds_read_b64_tr_b16 v[206:207], v242 offset:19968\n"
    "v_max3_f32 v249, v249, v137, v138\n"
    "v_max3_f32 v249, v249, v139, v140\n"
    "v_max3_f32 v249, v249, v141, v142\n"
    "s_waitcnt lgkmcnt(8)\n"
    "v_mfma_f32_32x32x16_bf16 v[144:159], v[216:219], v[120:123], v[144:159]\n"
    "ds_read_b64_tr_b16 v[208:209], v242 offset:20480\n"
    "ds_read_b64_tr_b16 v[210:211], v242 offset:22528\n"
    "v_max_f32 v249, v249, v143\n"
    "v_mfma_f32_32x32x16_bf16 v[144:159], v[220:223], v[124:127], v[144:159]\n"
    "ds_read_b64_tr_b16 v[212:213], v242 offset:20992\n"
    "ds_read_b64_tr_b16 v[214:215], v242 offset:23040\n"
    "s_waitcnt lgkmcnt(8)\n"
    "v_mfma_f32_32x32x16_bf16 v[0:15], v[192:195], v[160:163], v[0:15]\n"
    "ds_read_b64_tr_b16 v[216:217], v242 offset:21504\n"
    "ds_read_b64_tr_b16 v[218:219], v242 offset:23552\n"
    "v_mfma_f32_32x32x16_bf16 v[16:31], v[196:199], v[160:163], v[16:31]\n"
    "ds_read_b64_tr_b16 v[220:221], v242 offset:22016\n"
    "ds_read_b64_tr_b16 v[222:223], v242 offset:24064\n"
    "s_waitcnt lgkmcnt(8)\n"
    "v_mfma_f32_32x32x16_bf16 v[32:47], v[200:203], v[160:163], v[32:47]\n"
    "ds_read_b64_tr_b16 v[192:193], v242 offset:24576\n"
    "ds_read_b64_tr_b16 v[194:195], v242 offset:26624\n"
    "v_max3_f32 v250, v144, v145, v146\n"
    "v_max3_f32 v250, v250, v147, v148\n"
    "v_max3_f32 v250, v250, v149, v150\n"
    "v_max3_f32 v250, v250, v151, v152\n"
    "v_max3_f32 v250, v250, v153, v154\n"
    "v_mfma_f32_32x32x16_bf16 v[48:63], v[204:207], v[160:163], v[48:63]\n"
    "ds_read_b64_tr_b16 v[196:197], v242 offset:25088\n"
    "ds_read_b64_tr_b16 v[198:199], v242 offset:27136\n"
    "v_max3_f32 v250, v250, v155, v156\n"
    "v_max3_f32 v250, v250, v157, v158\n"
    "v_max_f32 v250, v250, v159\n"
    "v_max_f32 v249, v249, v250\n"
    "s_waitcnt lgkmcnt(8)\n"
    "v_mfma_f32_32x32x16_bf16 v[0:15], v[208:211], v[164:167], v[0:15]\n"
    "ds_read_b64_tr_b16 v[200:201], v242 offset:25600\n"
    "ds_read_b64_tr_b16 v[202:203], v242 offset:27648\n"
    "v_mov_b32 v251, v249\n"
    "s_nop 1\n"
    "v_permlane32_swap_b32 v249, v251\n"
    "v_max_f32 v249, v249, v251\n"
    "v_cmp_lt_f32 vcc, 0x41000000, v249\n"
    "s_nop 0\n"
    "v_mfma_f32_32x32x16_bf16 v[16:31], v[212:215], v[164:167], v[16:31]\n"
    "ds_read_b64_tr_b16 v[204:205], v242 offset:26112\n"
    "ds_read_b64_tr_b16 v[206:207], v242 offset:28160\n"
    "s_cbranch_vccnz L_stub1_lp_4\n"
    "L_back1_lp_4:\n"
    "v_exp_f32 v128, v128\n"
    "s_waitcnt lgkmcnt(8)\n"
    "v_mfma_f32_32x32x16_bf16 v[32:47], v[216:219], v[164:167], v[32:47]\n"
    "ds_read_b64_tr_b16 v[208:209], v242 offset:28672\n"
    "ds_read_b64_tr_b16 v[210:211], v242 offset:30720\n"
    "v_exp_f32 v129, v129\n"
    "v_exp_f32 v130, v130\n"
    "v_mfma_f32_32x32x16_bf16 v[48:63], v[220:223], v[164:167], v[48:63]\n"
    "ds_read_b64_tr_b16 v[212:213], v242 offset:29184\n"
    "ds_read_b64_tr_b16 v[214:215], v242 offset:31232\n"
    "v_add_f32 v247, v247, v128\n"
    "v_exp_f32 v131, v131\n"
    "s_waitcnt lgkmcnt(8)\n"
    "v_mfma_f32_32x32x16_bf16 v[0:15], v[192:195], v[168:171], v[0:15]\n"
    "ds_read_b64_tr_b16 v[216:217], v242 offset:29696\n"
    "ds_read_b64_tr_b16 v[218:219], v242 offset:31744\n"
    "v_add_f32 v248, v248, v129\n"
    "v_exp_f32 v132, v132\n"
    "v_add_f32 v247, v247, v130\n"
    "v_mfma_f32_32x32x16_bf16 v[16:31], v[196:199], v[168:171], v[16:31]\n"
    "ds_read_b64_tr_b16 v[220:221], v242 offset:30208\n"
    "ds_read_b64_tr_b16 v[222:223], v242 offset:32256\n"
    "v_exp_f32 v133, v133\n"
    "v_add_f32 v248, v248, v131\n"
    "s_waitcnt lgkmcnt(8)\n"
    "v_mfma_f32_32x32x16_bf16 v[32:47], v[200:203], v[168:171], v[32:47]\n"
    "s_barrier\n"
    "ds_read_b64_tr_b16 v[192:193], v242 offset:32768\n"
    "ds_read_b64_tr_b16 v[194:195], v242 offset:40960\n"
    "v_exp_f32 v134, v134\n"
    "v_add_f32 v247, v247, v132\n"
    "v_mfma_f32_32x32x16_bf16 v[48:63], v[204:207], v[168:171], v[48:63]\n"
    "ds_read_b64_tr_b16 v[196:197], v242 offset:33280\n"
    "ds_read_b64_tr_b16 v[198:199], v242 offset:41472\n"
    "v_exp_f32 v135, v135\n"
    "v_add_f32 v248, v248, v133\n"
    "s_waitcnt lgkmcnt(8)\n"
    "v_mfma_f32_32x32x16_bf16 v[0:15], v[208:211], v[172:175], v[0:15]\n"
    "ds_read_b64_tr_b16 v[200:201], v242 offset:33792\n"
    "ds_read_b64_tr_b16 v[202:203], v242 offset:41984\n"
    "s_add_u32 m0, s61, 0\n"
    "s_add_u32 s66, s62, 98304\n"
    "buffer_load_dwordx4 v243, s[36:39], s66 offen nt lds\n"
    "buffer_load_dwordx4 v244, s[36:39], s66 offen offset:2048 nt lds\n"
    "v_mfma_f32_32x32x16_bf16 v[16:31], v[212:215], v[172:175], v[16:31]\n"
    "ds_read_b64_tr_b16 v[204:205], v242 offset:34304\n"
    "ds_read_b64_tr_b16 v[206:207], v242 offset:42496\n"
    "s_add_u32 m0, s61, 896\n"
    "s_nop 0\n"
    "buffer_load_dwordx4 v243, s[36:39], s66 offen offset:128 nt lds\n"
    "buffer_load_dwordx4 v244, s[36:39], s66 offen offset:2176 nt lds\n"
    "s_waitcnt lgkmcnt(8)\n"
    "v_mfma_f32_32x32x16_bf16 v[32:47], v[216:219], v[172:175], v[32:47]\n"
    "ds_read_b64_tr_b16 v[208:209], v242 offset:34816\n"
    "ds_read_b64_tr_b16 v[210:211], v242 offset:43008\n"
    "v_exp_f32 v136, v136\n"
    "v_add_f32 v247, v247, v134\n"
    "v_mfma_f32_32x32x16_bf16 v[48:63], v[220:223], v[172:175], v[48:63]\n"
    "ds_read_b64_tr_b16 v[212:213], v242 offset:35328\n"
    "ds_read_b64_tr_b16 v[214:215], v242 offset:43520\n"
    "s_cmp_lg_u32 s69, 0\n"
    "s_cbranch_scc1 L_stub2_lp_4\n"
    "L_back2_lp_4:\n"
    "s_waitcnt lgkmcnt(8)\n"
    "v_mfma_f32_32x32x16_bf16 a[0:15], v[192:195], v[80:83], a[0:15]\n"
    "ds_read_b64_tr_b16 v[216:217], v242 offset:35840\n"
    "ds_read_b64_tr_b16 v[218:219], v242 offset:44032\n"
    "v_exp_f32 v137, v137\n"
    "v_add_f32 v248, v248, v135\n"
    "v_mfma_f32_32x32x16_bf16 a[16:31], v[196:199], v[80:83], a[16:31]\n"
    "ds_read_b64_tr_b16 v[220:221], v242 offset:36352\n"
    "ds_read_b64_tr_b16 v[222:223], v242 offset:44544\n"
    "v_exp_f32 v138, v138\n"
    "v_add_f32 v247, v247, v136\n"
    "s_waitcnt lgkmcnt(8)\n"
    "v_mfma_f32_32x32x16_bf16 a[32:47], v[200:203], v[80:83], a[32:47]\n"
    "ds_read_b64_tr_b16 v[192:193], v242 offset:36864\n"
    "ds_read_b64_tr_b16 v[194:195], v242 offset:45056\n"
    "v_exp_f32 v139, v139\n"
    "v_add_f32 v248, v248, v137\n"
    "v_mfma_f32_32x32x16_bf16 a[48:63], v[204:207], v[80:83], a[48:63]\n"
    "ds_read_b64_tr_b16 v[196:197], v242 offset:37376\n"
    "ds_read_b64_tr_b16 v[198:199], v242 offset:45568\n"
    "v_exp_f32 v140, v140\n"
    "v_add_f32 v247, v247, v138\n"
    "s_waitcnt lgkmcnt(8)\n"
    "v_mfma_f32_32x32x16_bf16 a[64:79], v[208:211], v[80:83], a[64:79]\n"
    "ds_read_b64_tr_b16 v[200:201], v242 offset:37888\n"
    "ds_read_b64_tr_b16 v[202:203], v242 offset:46080\n"
    "v_exp_f32 v141, v141\n"
    "v_add_f32 v248, v248, v139\n"
    "v_mfma_f32_32x32x16_bf16 a[80:95], v[212:215], v[80:83], a[80:95]\n"
    "ds_read_b64_tr_b16 v[204:205], v242 offset:38400\n"
    "ds_read_b64_tr_b16 v[206:207], v242 offset:46592\n"
    "v_exp_f32 v142, v142\n"
    "v_add_f32 v247, v247, v140\n"
    "s_waitcnt lgkmcnt(8)\n"
    "v_mfma_f32_32x32x16_bf16 a[96:111], v[216:219], v[80:83], a[96:111]\n"
    "ds_read_b64_tr_b16 v[208:209], v242 offset:38912\n"
    "ds_read_b64_tr_b16 v[210:211], v242 offset:47104\n"
    "v_exp_f32 v143, v143\n"
    "v_add_f32 v248, v248, v141\n"
    "v_add_f32 v247, v247, v142\n"
    "v_mfma_f32_32x32x16_bf16 a[112:127], v[220:223], v[80:83], a[112:127]\n"
    "ds_read_b64_tr_b16 v[212:213], v242 offset:39424\n"
    "ds_read_b64_tr_b16 v[214:215], v242 offset:47616\n"
    "v_add_f32 v248, v248, v143\n"
    "v_cvt_pk_bf16_f32 v160, v128, v129\n"
    "v_cvt_pk_bf16_f32 v161, v130, v131\n"
    "s_waitcnt lgkmcnt(8)\n"
    "v_mfma_f32_32x32x16_bf16 a[128:143], v[192:195], v[80:83], a[128:143]\n"
    "ds_read_b64_tr_b16 v[216:217], v242 offset:39936\n"
    "ds_read_b64_tr_b16 v[218:219], v242 offset:48128\n"
    "v_cvt_pk_bf16_f32 v162, v132, v133\n"
    "v_cvt_pk_bf16_f32 v163, v134, v135\n"
    "v_cvt_pk_bf16_f32 v164, v136, v137\n"
    "v_cvt_pk_bf16_f32 v165, v138, v139\n"
    "v_mfma_f32_32x32x16_bf16 a[144:159], v[196:199], v[80:83], a[144:159]\n"
    "ds_read_b64_tr_b16 v[220:221], v242 offset:40448\n"
    "ds_read_b64_tr_b16 v[222:223], v242 offset:48640\n"
    "v_cvt_pk_bf16_f32 v166, v140, v141\n"
    "v_cvt_pk_bf16_f32 v167, v142, v143\n"
    "s_waitcnt lgkmcnt(8)\n"
    "v_mfma_f32_32x32x16_bf16 a[160:175], v[200:203], v[80:83], a[160:175]\n"
    "s_waitcnt vmcnt(8)\n"
    "s_barrier\n"
    "ds_read_b128 v[192:195], v240 offset:16384\n"
    "v_exp_f32 v144, v144\n"
    "v_exp_f32 v145, v145\n"
    "v_mfma_f32_32x32x16_bf16 a[176:191], v[204:207], v[80:83], a[176:191]\n"
    "ds_read_b128 v[196:199], v241 offset:16384\n"
    "v_exp_f32 v146, v146\n"
    "v_add_f32 v247, v247, v144\n"
    "s_waitcnt lgkmcnt(6)\n"
    "v_mfma_f32_32x32x16_bf16 a[192:207], v[208:211], v[80:83], a[192:207]\n"
    "ds_read_b128 v[200:203], v240 offset:16896\n"
    "v_exp_f32 v147, v147\n"
    "v_add_f32 v248, v248, v145\n"
    "v_exp_f32 v148, v148\n"
    "v_mfma_f32_32x32x16_bf16 a[208:223], v[212:215], v[80:83], a[208:223]\n"
    "ds_read_b128 v[204:207], v241 offset:16896\n"
    "v_add_f32 v247, v247, v146\n"
    "v_exp_f32 v149, v149\n"
    "v_add_f32 v248, v248, v147\n"
    "s_waitcnt lgkmcnt(4)\n"
    "v_mfma_f32_32x32x16_bf16 a[224:239], v[216:219], v[80:83], a[224:239]\n"
    "ds_read_b128 v[208:211], v240 offset:17408\n"
    "v_exp_f32 v150, v150\n"
    "v_add_f32 v247, v247, v148\n"
    "v_exp_f32 v151, v151\n"
    "v_mfma_f32_32x32x16_bf16 a[240:255], v[220:223], v[80:83], a[240:255]\n"
    "ds_read_b128 v[212:215], v241 offset:17408\n"
    "s_waitcnt lgkmcnt(4)\n"
    "v_mfma_f32_32x32x16_bf16 v[128:143], v[192:195], v[96:99], v[176:191]\n"
    "ds_read_b128 v[216:219], v240 offset:17920\n"
    "v_add_f32 v248, v248, v149\n"
    "v_exp_f32 v152, v152\n"
    "v_add_f32 v247, v247, v150\n"
    "v_mfma_f32_32x32x16_bf16 v[128:143], v[196:199], v[100:103], v[128:143]\n"
    "ds_read_b128 v[220:223], v241 offset:17920\n"
    "v_exp_f32 v153, v153\n"
    "v_add_f32 v248, v248, v151\n"
    "s_waitcnt lgkmcnt(4)\n"
    "v_mfma_f32_32x32x16_bf16 v[128:143], v[200:203], v[104:107], v[128:143]\n"
    "ds_read_b128 v[192:195], v240 offset:24576\n"
    "v_exp_f32 v154, v154\n"
    "v_add_f32 v247, v247, v152\n"
    "v_exp_f32 v155, v155\n"
    "v_mfma_f32_32x32x16_bf16 v[128:143], v[204:207], v[108:111], v[128:143]\n"
    "ds_read_b128 v[196:199], v241 offset:24576\n"
    "v_add_f32 v248, v248, v153\n"
    "v_exp_f32 v156, v156\n"
    "v_add_f32 v247, v247, v154\n"
    "s_waitcnt lgkmcnt(4)\n"
    "v_mfma_f32_32x32x16_bf16 v[128:143], v[208:211], v[112:115], v[128:143]\n"
    "ds_read_b128 v[200:203], v240 offset:25088\n"
    "v_exp_f32 v157, v157\n"
    "v_add_f32 v248, v248, v155\n"
    "v_exp_f32 v158, v158\n"
    "v_mfma_f32_32x32x16_bf16 v[128:143], v[212:215], v[116:119], v[128:143]\n"
    "ds_read_b128 v[204:207], v241 offset:25088\n"
    "v_add_f32 v247, v247, v156\n"
    "v_exp_f32 v159, v159\n"
    "v_add_f32 v248, v248, v157\n"
    "s_waitcnt lgkmcnt(4)\n"
    "v_mfma_f32_32x32x16_bf16 v[128:143], v[216:219], v[120:123], v[128:143]\n"
    "ds_read_b128 v[208:211], v240 offset:25600\n"
    "v_add_f32 v247, v247, v158\n"
    "v_add_f32 v248, v248, v159\n"
    "v_cvt_pk_bf16_f32 v168, v144, v145\n"
    "v_cvt_pk_bf16_f32 v169, v146, v147\n"
    "v_cvt_pk_bf16_f32 v170, v148, v149\n"
    "v_mfma_f32_32x32x16_bf16 v[128:143], v[220:223], v[124:127], v[128:143]\n"
    "ds_read_b128 v[212:215], v241 offset:25600\n"
    "v_cvt_pk_bf16_f32 v171, v150, v151\n"
    "v_cvt_pk_bf16_f32 v172, v152, v153\n"
    "v_cvt_pk_bf16_f32 v173, v154, v155\n"
    "v_cvt_pk_bf16_f32 v174, v156, v157\n"
    "v_cvt_pk_bf16_f32 v175, v158, v159\n"
    "s_waitcnt lgkmcnt(4)\n"
    "v_mfma_f32_32x32x16_bf16 v[144:159], v[192:195], v[96:99], v[176:191]\n"
    "ds_read_b128 v[216:219], v240 offset:26112\n"
    "s_add_u32 m0, s61, 49152\n"
    "s_add_u32 s66, s62, 81920\n"
    "buffer_load_dwordx4 v245, s[40:43], s66 offen nt lds\n"
    "buffer_load_dwordx4 v245, s[40:43], s66 offen offset:2048 nt lds\n"
    "v_mfma_f32_32x32x16_bf16 v[144:159], v[196:199], v[100:103], v[144:159]\n"
    "ds_read_b128 v[220:223], v241 offset:26112\n"
    "s_add_u32 m0, s61, 50048\n"
    "s_nop 0\n"
    "buffer_load_dwordx4 v245, s[40:43], s66 offen offset:128 nt lds\n"
    "buffer_load_dwordx4 v245, s[40:43], s66 offen offset:2176 nt lds\n"
    "s_waitcnt lgkmcnt(4)\n"
    "v_mfma_f32_32x32x16_bf16 v[144:159], v[200:203], v[104:107], v[144:159]\n"
    "s_waitcnt vmcnt(8)\n"
    "s_barrier\n"
    "ds_read_b64_tr_b16 v[192:193], v242 offset:0\n"
    "ds_read_b64_tr_b16 v[194:195], v242 offset:2048\n"
    "v_mfma_f32_32x32x16_bf16 v[144:159], v[204:207], v[108:111], v[144:159]\n"
    "ds_read_b64_tr_b16 v[196:197], v242 offset:512\n"
    "ds_read_b64_tr_b16 v[198:199], v242 offset:2560\n"
    "s_waitcnt lgkmcnt(6)\n"
    "v_mfma_f32_32x32x16_bf16 v[144:159], v[208:211], v[112:115], v[144:159]\n"
    "ds_read_b64_tr_b16 v[200:201], v242 offset:1024\n"
    "ds_read_b64_tr_b16 v[202:203], v242 offset:3072\n"
    "v_max3_f32 v249, v128, v129, v130\n"
    "v_max3_f32 v249, v249, v131, v132\n"
    "v_max3_f32 v249, v249, v133, v134\n"
    "v_max3_f32 v249, v249, v135, v136\n"
    "v_mfma_f32_32x32x16_bf16 v[144:159], v[212:215], v[116:119], v[144:159]\n"
    "ds_read_b64_tr_b16 v[204:205], v242 offset:1536\n"
    "ds_read_b64_tr_b16 v[206:207], v242 offset:3584\n"
    "v_max3_f32 v249, v249, v137, v138\n"
    "v_max3_f32 v249, v249, v139, v140\n"
    "v_max3_f32 v249, v249, v141, v142\n"
    "s_waitcnt lgkmcnt(8)\n"
    "v_mfma_f32_32x32x16_bf16 v[144:159], v[216:219], v[120:123], v[144:159]\n"
    "ds_read_b64_tr_b16 v[208:209], v242 offset:4096\n"
    "ds_read_b64_tr_b16 v[210:211], v242 offset:6144\n"
    "v_max_f32 v249, v249, v143\n"
    "v_mfma_f32_32x32x16_bf16 v[144:159], v[220:223], v[124:127], v[144:159]\n"
    "ds_read_b64_tr_b16 v[212:213], v242 offset:4608\n"
    "ds_read_b64_tr_b16 v[214:215], v242 offset:6656\n"
    "s_waitcnt lgkmcnt(8)\n"
    "v_mfma_f32_32x32x16_bf16 v[0:15], v[192:195], v[160:163], v[0:15]\n"
    "ds_read_b64_tr_b16 v[216:217], v242 offset:5120\n"
    "ds_read_b64_tr_b16 v[218:219], v242 offset:7168\n"
    "v_mfma_f32_32x32x16_bf16 v[16:31], v[196:199], v[160:163], v[16:31]\n"
    "ds_read_b64_tr_b16 v[220:221], v242 offset:5632\n"
    "ds_read_b64_tr_b16 v[222:223], v242 offset:7680\n"
    "s_waitcnt lgkmcnt(8)\n"
    "v_mfma_f32_32x32x16_bf16 v[32:47], v[200:203], v[160:163], v[32:47]\n"
    "ds_read_b64_tr_b16 v[192:193], v242 offset:8192\n"
    "ds_read_b64_tr_b16 v[194:195], v242 offset:10240\n"
    "v_max3_f32 v250, v144, v145, v146\n"
    "v_max3_f32 v250, v250, v147, v148\n"
    "v_max3_f32 v250, v250, v149, v150\n"
    "v_max3_f32 v250, v250, v151, v152\n"
    "v_max3_f32 v250, v250, v153, v154\n"
    "v_mfma_f32_32x32x16_bf16 v[48:63], v[204:207], v[160:163], v[48:63]\n"
    "ds_read_b64_tr_b16 v[196:197], v242 offset:8704\n"
    "ds_read_b64_tr_b16 v[198:199], v242 offset:10752\n"
    "v_max3_f32 v250, v250, v155, v156\n"
    "v_max3_f32 v250, v250, v157, v158\n"
    "v_max_f32 v250, v250, v159\n"
    "v_max_f32 v249, v249, v250\n"
    "s_waitcnt lgkmcnt(8)\n"
    "v_mfma_f32_32x32x16_bf16 v[0:15], v[208:211], v[164:167], v[0:15]\n"
    "ds_read_b64_tr_b16 v[200:201], v242 offset:9216\n"
    "ds_read_b64_tr_b16 v[202:203], v242 offset:11264\n"
    "v_mov_b32 v251, v249\n"
    "s_nop 1\n"
    "v_permlane32_swap_b32 v249, v251\n"
    "v_max_f32 v249, v249, v251\n"
    "v_cmp_lt_f32 vcc, 0x41000000, v249\n"
    "s_nop 0\n"
    "v_mfma_f32_32x32x16_bf16 v[16:31], v[212:215], v[164:167], v[16:31]\n"
    "ds_read_b64_tr_b16 v[204:205], v242 offset:9728\n"
    "ds_read_b64_tr_b16 v[206:207], v242 offset:11776\n"
    "s_cbranch_vccnz L_stub1_lp_5\n"
    "L_back1_lp_5:\n"
    "v_exp_f32 v128, v128\n"
    "s_waitcnt lgkmcnt(8)\n"
    "v_mfma_f32_32x32x16_bf16 v[32:47], v[216:219], v[164:167], v[32:47]\n"
    "ds_read_b64_tr_b16 v[208:209], v242 offset:12288\n"
    "ds_read_b64_tr_b16 v[210:211], v242 offset:14336\n"
    "v_exp_f32 v129, v129\n"
    "v_exp_f32 v130, v130\n"
    "v_mfma_f32_32x32x16_bf16 v[48:63], v[220:223], v[164:167], v[48:63]\n"
    "ds_read_b64_tr_b16 v[212:213], v242 offset:12800\n"
    "ds_read_b64_tr_b16 v[214:215], v242 offset:14848\n"
    "v_add_f32 v247, v247, v128\n"
    "v_exp_f32 v131, v131\n"
    "s_waitcnt lgkmcnt(8)\n"
    "v_mfma_f32_32x32x16_bf16 v[0:15], v[192:195], v[168:171], v[0:15]\n"
    "ds_read_b64_tr_b16 v[216:217], v242 offset:13312\n"
    "ds_read_b64_tr_b16 v[218:219], v242 offset:15360\n"
    "v_add_f32 v248, v248, v129\n"
    "v_exp_f32 v132, v132\n"
    "v_add_f32 v247, v247, v130\n"
    "v_mfma_f32_32x32x16_bf16 v[16:31], v[196:199], v[168:171], v[16:31]\n"
    "ds_read_b64_tr_b16 v[220:221], v242 offset:13824\n"
    "ds_read_b64_tr_b16 v[222:223], v242 offset:15872\n"
    "v_exp_f32 v133, v133\n"
    "v_add_f32 v248, v248, v131\n"
    "s_waitcnt lgkmcnt(8)\n"
    "v_mfma_f32_32x32x16_bf16 v[32:47], v[200:203], v[168:171], v[32:47]\n"
    "s_barrier\n"
    "ds_read_b64_tr_b16 v[192:193], v242 offset:49152\n"
    "ds_read_b64_tr_b16 v[194:195], v242 offset:57344\n"
    "v_exp_f32 v134, v134\n"
    "v_add_f32 v247, v247, v132\n"
    "v_mfma_f32_32x32x16_bf16 v[48:63], v[204:207], v[168:171], v[48:63]\n"
    "ds_read_b64_tr_b16 v[196:197], v242 offset:49664\n"
    "ds_read_b64_tr_b16 v[198:199], v242 offset:57856\n"
    "v_exp_f32 v135, v135\n"
    "v_add_f32 v248, v248, v133\n"
    "s_waitcnt lgkmcnt(8)\n"
    "v_mfma_f32_32x32x16_bf16 v[0:15], v[208:211], v[172:175], v[0:15]\n"
    "ds_read_b64_tr_b16 v[200:201], v242 offset:50176\n"
    "ds_read_b64_tr_b16 v[202:203], v242 offset:58368\n"
    "s_add_u32 m0, s61, 16384\n"
    "s_add_u32 s66, s62, 114688\n"
    "buffer_load_dwordx4 v243, s[36:39], s66 offen nt lds\n"
    "buffer_load_dwordx4 v244, s[36:39], s66 offen offset:2048 nt lds\n"
    "v_mfma_f32_32x32x16_bf16 v[16:31], v[212:215], v[172:175], v[16:31]\n"
    "ds_read_b64_tr_b16 v[204:205], v242 offset:50688\n"
    "ds_read_b64_tr_b16 v[206:207], v242 offset:58880\n"
    "s_add_u32 m0, s61, 17280\n"
    "s_nop 0\n"
    "buffer_load_dwordx4 v243, s[36:39], s66 offen offset:128 nt lds\n"
    "buffer_load_dwordx4 v244, s[36:39], s66 offen offset:2176 nt lds\n"
    "s_waitcnt lgkmcnt(8)\n"
    "v_mfma_f32_32x32x16_bf16 v[32:47], v[216:219], v[172:175], v[32:47]\n"
    "ds_read_b64_tr_b16 v[208:209], v242 offset:51200\n"
    "ds_read_b64_tr_b16 v[210:211], v242 offset:59392\n"
    "v_exp_f32 v136, v136\n"
    "v_add_f32 v247, v247, v134\n"
    "v_mfma_f32_32x32x16_bf16 v[48:63], v[220:223], v[172:175], v[48:63]\n"
    "ds_read_b64_tr_b16 v[212:213], v242 offset:51712\n"
    "ds_read_b64_tr_b16 v[214:215], v242 offset:59904\n"
    "s_cmp_lg_u32 s69, 0\n"
    "s_cbranch_scc1 L_stub2_lp_5\n"
    "L_back2_lp_5:\n"
    "s_waitcnt lgkmcnt(8)\n"
    "v_mfma_f32_32x32x16_bf16 a[0:15], v[192:195], v[84:87], a[0:15]\n"
    "ds_read_b64_tr_b16 v[216:217], v242 offset:52224\n"
    "ds_read_b64_tr_b16 v[218:219], v242 offset:60416\n"
    "v_exp_f32 v137, v137\n"
    "v_add_f32 v248, v248, v135\n"
    "v_mfma_f32_32x32x16_bf16 a[16:31], v[196:199], v[84:87], a[16:31]\n"
    "ds_read_b64_tr_b16 v[220:221], v242 offset:52736\n"
    "ds_read_b64_tr_b16 v[222:223], v242 offset:60928\n"
    "v_exp_f32 v138, v138\n"
    "v_add_f32 v247, v247, v136\n"
    "s_waitcnt lgkmcnt(8)\n"
    "v_mfma_f32_32x32x16_bf16 a[32:47], v[200:203], v[84:87], a[32:47]\n"
    "ds_read_b64_tr_b16 v[192:193], v242 offset:53248\n"
    "ds_read_b64_tr_b16 v[194:195], v242 offset:61440\n"
    "v_exp_f32 v139, v139\n"
    "v_add_f32 v248, v248, v137\n"
    "v_mfma_f32_32x32x16_bf16 a[48:63], v[204:207], v[84:87], a[48:63]\n"
    "ds_read_b64_tr_b16 v[196:197], v242 offset:53760\n"
    "ds_read_b64_tr_b16 v[198:199], v242 offset:61952\n"
    "v_exp_f32 v140, v140\n"
    "v_add_f32 v247, v247, v138\n"
    "s_waitcnt lgkmcnt(8)\n"
    "v_mfma_f32_32x32x16_bf16 a[64:79], v[208:211], v[84:87], a[64:79]\n"
    "ds_read_b64_tr_b16 v[200:201], v242 offset:54272\n"
    "ds_read_b64_tr_b16 v[202:203], v242 offset:62464\n"
    "v_exp_f32 v141, v141\n"
    "v_add_f32 v248, v248, v139\n"
    "v_mfma_f32_32x32x16_bf16 a[80:95], v[212:215], v[84:87], a[80:95]\n"
    "ds_read_b64_tr_b16 v[204:205], v242 offset:54784\n"
    "ds_read_b64_tr_b16 v[206:207], v242 offset:62976\n"
    "v_exp_f32 v142, v142\n"
    "v_add_f32 v247, v247, v140\n"
    "s_waitcnt lgkmcnt(8)\n"
    "v_mfma_f32_32x32x16_bf16 a[96:111], v[216:219], v[84:87], a[96:111]\n"
    "ds_read_b64_tr_b16 v[208:209], v242 offset:55296\n"
    "ds_read_b64_tr_b16 v[210:211], v242 offset:63488\n"
    "v_exp_f32 v143, v143\n"
    "v_add_f32 v248, v248, v141\n"
    "v_add_f32 v247, v247, v142\n"
    "v_mfma_f32_32x32x16_bf16 a[112:127], v[220:223], v[84:87], a[112:127]\n"
    "ds_read_b64_tr_b16 v[212:213], v242 offset:55808\n"
    "ds_read_b64_tr_b16 v[214:215], v242 offset:64000\n"
    "v_add_f32 v248, v248, v143\n"
    "v_cvt_pk_bf16_f32 v160, v128, v129\n"
    "v_cvt_pk_bf16_f32 v161, v130, v131\n"
    "s_waitcnt lgkmcnt(8)\n"
    "v_mfma_f32_32x32x16_bf16 a[128:143], v[192:195], v[84:87], a[128:143]\n"
    "ds_read_b64_tr_b16 v[216:217], v242 offset:56320\n"
    "ds_read_b64_tr_b16 v[218:219], v242 offset:64512\n"
    "v_cvt_pk_bf16_f32 v162, v132, v133\n"
    "v_cvt_pk_bf16_f32 v163, v134, v135\n"
    "v_cvt_pk_bf16_f32 v164, v136, v137\n"
    "v_cvt_pk_bf16_f32 v165, v138, v139\n"
    "v_mfma_f32_32x32x16_bf16 a[144:159], v[196:199], v[84:87], a[144:159]\n"
    "ds_read_b64_tr_b16 v[220:221], v242 offset:56832\n"
    "ds_read_b64_tr_b16 v[222:223], v242 offset:65024\n"
    "v_cvt_pk_bf16_f32 v166, v140, v141\n"
    "v_cvt_pk_bf16_f32 v167, v142, v143\n"
    "s_waitcnt lgkmcnt(8)\n"
    "v_mfma_f32_32x32x16_bf16 a[160:175], v[200:203], v[84:87], a[160:175]\n"
    "s_waitcnt vmcnt(8)\n"
    "s_barrier\n"
    "ds_read_b128 v[192:195], v240 offset:0\n"
    "v_exp_f32 v144, v144\n"
    "v_exp_f32 v145, v145\n"
    "v_mfma_f32_32x32x16_bf16 a[176:191], v[204:207], v[84:87], a[176:191]\n"
    "ds_read_b128 v[196:199], v241 offset:0\n"
    "v_exp_f32 v146, v146\n"
    "v_add_f32 v247, v247, v144\n"
    "s_waitcnt lgkmcnt(6)\n"
    "v_mfma_f32_32x32x16_bf16 a[192:207], v[208:211], v[84:87], a[192:207]\n"
    "ds_read_b128 v[200:203], v240 offset:512\n"
    "v_exp_f32 v147, v147\n"
    "v_add_f32 v248, v248, v145\n"
    "v_exp_f32 v148, v148\n"
    "v_mfma_f32_32x32x16_bf16 a[208:223], v[212:215], v[84:87], a[208:223]\n"
    "ds_read_b128 v[204:207], v241 offset:512\n"
    "v_add_f32 v247, v247, v146\n"
    "v_exp_f32 v149, v149\n"
    "v_add_f32 v248, v248, v147\n"
    "s_waitcnt lgkmcnt(4)\n"
    "v_mfma_f32_32x32x16_bf16 a[224:239], v[216:219], v[84:87], a[224:239]\n"
    "ds_read_b128 v[208:211], v240 offset:1024\n"
    "v_exp_f32 v150, v150\n"
    "v_add_f32 v247, v247, v148\n"
    "v_exp_f32 v151, v151\n"
    "v_mfma_f32_32x32x16_bf16 a[240:255], v[220:223], v[84:87], a[240:255]\n"
    "ds_read_b128 v[212:215], v241 offset:1024\n"
    "s_waitcnt lgkmcnt(4)\n"
    "v_mfma_f32_32x32x16_bf16 v[128:143], v[192:195], v[96:99], v[176:191]\n"
    "ds_read_b128 v[216:219], v240 offset:1536\n"
    "v_add_f32 v248, v248, v149\n"
    "v_exp_f32 v152, v152\n"
    "v_add_f32 v247, v247, v150\n"
    "v_mfma_f32_32x32x16_bf16 v[128:143], v[196:199], v[100:103], v[128:143]\n"
    "ds_read_b128 v[220:223], v241 offset:1536\n"
    "v_exp_f32 v153, v153\n"
    "v_add_f32 v248, v248, v151\n"
    "s_waitcnt lgkmcnt(4)\n"
    "v_mfma_f32_32x32x16_bf16 v[128:143], v[200:203], v[104:107], v[128:143]\n"
    "ds_read_b128 v[192:195], v240 offset:8192\n"
    "v_exp_f32 v154, v154\n"
    "v_add_f32 v247, v247, v152\n"
    "v_exp_f32 v155, v155\n"
    "v_mfma_f32_32x32x16_bf16 v[128:143], v[204:207], v[108:111], v[128:143]\n"
    "ds_read_b128 v[196:199], v241 offset:8192\n"
    "v_add_f32 v248, v248, v153\n"
    "v_exp_f32 v156, v156\n"
    "v_add_f32 v247, v247, v154\n"
    "s_waitcnt lgkmcnt(4)\n"
    "v_mfma_f32_32x32x16_bf16 v[128:143], v[208:211], v[112:115], v[128:143]\n"
    "ds_read_b128 v[200:203], v240 offset:8704\n"
    "v_exp_f32 v157, v157\n"
    "v_add_f32 v248, v248, v155\n"
    "v_exp_f32 v158, v158\n"
    "v_mfma_f32_32x32x16_bf16 v[128:143], v[212:215], v[116:119], v[128:143]\n"
    "ds_read_b128 v[204:207], v241 offset:8704\n"
    "v_add_f32 v247, v247, v156\n"
    "v_exp_f32 v159, v159\n"
    "v_add_f32 v248, v248, v157\n"
    "s_waitcnt lgkmcnt(4)\n"
    "v_mfma_f32_32x32x16_bf16 v[128:143], v[216:219], v[120:123], v[128:143]\n"
    "ds_read_b128 v[208:211], v240 offset:9216\n"
    "v_add_f32 v247, v247, v158\n"
    "v_add_f32 v248, v248, v159\n"
    "v_cvt_pk_bf16_f32 v168, v144, v145\n"
    "v_cvt_pk_bf16_f32 v169, v146, v147\n"
    "v_cvt_pk_bf16_f32 v170, v148, v149\n"
    "v_mfma_f32_32x32x16_bf16 v[128:143], v[220:223], v[124:127], v[128:143]\n"
    "ds_read_b128 v[212:215], v241 offset:9216\n"
    "v_cvt_pk_bf16_f32 v171, v150, v151\n"
    "v_cvt_pk_bf16_f32 v172, v152, v153\n"
    "v_cvt_pk_bf16_f32 v173, v154, v155\n"
    "v_cvt_pk_bf16_f32 v174, v156, v157\n"
    "v_cvt_pk_bf16_f32 v175, v158, v159\n"
    "s_waitcnt lgkmcnt(4)\n"
    "v_mfma_f32_32x32x16_bf16 v[144:159], v[192:195], v[96:99], v[176:191]\n"
    "ds_read_b128 v[216:219], v240 offset:9728\n"
    "s_add_u32 m0, s61, 32768\n"
    "s_add_u32 s66, s62, 98304\n"
    "buffer_load_dwordx4 v245, s[40:43], s66 offen nt lds\n"
    "buffer_load_dwordx4 v245, s[40:43], s66 offen offset:2048 nt lds\n"
    "v_mfma_f32_32x32x16_bf16 v[144:159], v[196:199], v[100:103], v[144:159]\n"
    "ds_read_b128 v[220:223], v241 offset:9728\n"
    "s_add_u32 m0, s61, 33664\n"
    "s_nop 0\n"
    "buffer_load_dwordx4 v245, s[40:43], s66 offen offset:128 nt lds\n"
    "buffer_load_dwordx4 v245, s[40:43], s66 offen offset:2176 nt lds\n"
    "s_waitcnt lgkmcnt(4)\n"
    "v_mfma_f32_32x32x16_bf16 v[144:159], v[200:203], v[104:107], v[144:159]\n"
    "s_waitcnt vmcnt(8)\n"
    "s_barrier\n"
    "ds_read_b64_tr_b16 v[192:193], v242 offset:16384\n"
    "ds_read_b64_tr_b16 v[194:195], v242 offset:18432\n"
    "v_mfma_f32_32x32x16_bf16 v[144:159], v[204:207], v[108:111], v[144:159]\n"
    "ds_read_b64_tr_b16 v[196:197], v242 offset:16896\n"
    "ds_read_b64_tr_b16 v[198:199], v242 offset:18944\n"
    "s_waitcnt lgkmcnt(6)\n"
    "v_mfma_f32_32x32x16_bf16 v[144:159], v[208:211], v[112:115], v[144:159]\n"
    "ds_read_b64_tr_b16 v[200:201], v242 offset:17408\n"
    "ds_read_b64_tr_b16 v[202:203], v242 offset:19456\n"
    "v_max3_f32 v249, v128, v129, v130\n"
    "v_max3_f32 v249, v249, v131, v132\n"
    "v_max3_f32 v249, v249, v133, v134\n"
    "v_max3_f32 v249, v249, v135, v136\n"
    "v_mfma_f32_32x32x16_bf16 v[144:159], v[212:215], v[116:119], v[144:159]\n"
    "ds_read_b64_tr_b16 v[204:205], v242 offset:17920\n"
    "ds_read_b64_tr_b16 v[206:207], v242 offset:19968\n"
    "v_max3_f32 v249, v249, v137, v138\n"
    "v_max3_f32 v249, v249, v139, v140\n"
    "v_max3_f32 v249, v249, v141, v142\n"
    "s_waitcnt lgkmcnt(8)\n"
    "v_mfma_f32_32x32x16_bf16 v[144:159], v[216:219], v[120:123], v[144:159]\n"
    "ds_read_b64_tr_b16 v[208:209], v242 offset:20480\n"
    "ds_read_b64_tr_b16 v[210:211], v242 offset:22528\n"
    "v_max_f32 v249, v249, v143\n"
    "v_mfma_f32_32x32x16_bf16 v[144:159], v[220:223], v[124:127], v[144:159]\n"
    "ds_read_b64_tr_b16 v[212:213], v242 offset:20992\n"
    "ds_read_b64_tr_b16 v[214:215], v242 offset:23040\n"
    "s_waitcnt lgkmcnt(8)\n"
    "v_mfma_f32_32x32x16_bf16 v[0:15], v[192:195], v[160:163], v[0:15]\n"
    "ds_read_b64_tr_b16 v[216:217], v242 offset:21504\n"
    "ds_read_b64_tr_b16 v[218:219], v242 offset:23552\n"
    "v_mfma_f32_32x32x16_bf16 v[16:31], v[196:199], v[160:163], v[16:31]\n"
    "ds_read_b64_tr_b16 v[220:221], v242 offset:22016\n"
    "ds_read_b64_tr_b16 v[222:223], v242 offset:24064\n"
    "s_waitcnt lgkmcnt(8)\n"
    "v_mfma_f32_32x32x16_bf16 v[32:47], v[200:203], v[160:163], v[32:47]\n"
    "ds_read_b64_tr_b16 v[192:193], v242 offset:24576\n"
    "ds_read_b64_tr_b16 v[194:195], v242 offset:26624\n"
    "v_max3_f32 v250, v144, v145, v146\n"
    "v_max3_f32 v250, v250, v147, v148\n"
    "v_max3_f32 v250, v250, v149, v150\n"
    "v_max3_f32 v250, v250, v151, v152\n"
    "v_max3_f32 v250, v250, v153, v154\n"
    "v_mfma_f32_32x32x16_bf16 v[48:63], v[204:207], v[160:163], v[48:63]\n"
    "ds_read_b64_tr_b16 v[196:197], v242 offset:25088\n"
    "ds_read_b64_tr_b16 v[198:199], v242 offset:27136\n"
    "v_max3_f32 v250, v250, v155, v156\n"
    "v_max3_f32 v250, v250, v157, v158\n"
    "v_max_f32 v250, v250, v159\n"
    "v_max_f32 v249, v249, v250\n"
    "s_waitcnt lgkmcnt(8)\n"
    "v_mfma_f32_32x32x16_bf16 v[0:15], v[208:211], v[164:167], v[0:15]\n"
    "ds_read_b64_tr_b16 v[200:201], v242 offset:25600\n"
    "ds_read_b64_tr_b16 v[202:203], v242 offset:27648\n"
    "v_mov_b32 v251, v249\n"
    "s_nop 1\n"
    "v_permlane32_swap_b32 v249, v251\n"
    "v_max_f32 v249, v249, v251\n"
    "v_cmp_lt_f32 vcc, 0x41000000, v249\n"
    "s_nop 0\n"
    "v_mfma_f32_32x32x16_bf16 v[16:31], v[212:215], v[164:167], v[16:31]\n"
    "ds_read_b64_tr_b16 v[204:205], v242 offset:26112\n"
    "ds_read_b64_tr_b16 v[206:207], v242 offset:28160\n"
    "s_cbranch_vccnz L_stub1_lp_6\n"
    "L_back1_lp_6:\n"
    "v_exp_f32 v128, v128\n"
    "s_waitcnt lgkmcnt(8)\n"
    "v_mfma_f32_32x32x16_bf16 v[32:47], v[216:219], v[164:167], v[32:47]\n"
    "ds_read_b64_tr_b16 v[208:209], v242 offset:28672\n"
    "ds_read_b64_tr_b16 v[210:211], v242 offset:30720\n"
    "v_exp_f32 v129, v129\n"
    "v_exp_f32 v130, v130\n"
    "v_mfma_f32_32x32x16_bf16 v[48:63], v[220:223], v[164:167], v[48:63]\n"
    "ds_read_b64_tr_b16 v[212:213], v242 offset:29184\n"
    "ds_read_b64_tr_b16 v[214:215], v242 offset:31232\n"
    "v_add_f32 v247, v247, v128\n"
    "v_exp_f32 v131, v131\n"
    "s_waitcnt lgkmcnt(8)\n"
    "v_mfma_f32_32x32x16_bf16 v[0:15], v[192:195], v[168:171], v[0:15]\n"
    "ds_read_b64_tr_b16 v[216:217], v242 offset:29696\n"
    "ds_read_b64_tr_b16 v[218:219], v242 offset:31744\n"
    "v_add_f32 v248, v248, v129\n"
    "v_exp_f32 v132, v132\n"
    "v_add_f32 v247, v247, v130\n"
    "v_mfma_f32_32x32x16_bf16 v[16:31], v[196:199], v[168:171], v[16:31]\n"
    "ds_read_b64_tr_b16 v[220:221], v242 offset:30208\n"
    "ds_read_b64_tr_b16 v[222:223], v242 offset:32256\n"
    "v_exp_f32 v133, v133\n"
    "v_add_f32 v248, v248, v131\n"
    "s_waitcnt lgkmcnt(8)\n"
    "v_mfma_f32_32x32x16_bf16 v[32:47], v[200:203], v[168:171], v[32:47]\n"
    "s_barrier\n"
    "ds_read_b64_tr_b16 v[192:193], v242 offset:32768\n"
    "ds_read_b64_tr_b16 v[194:195], v242 offset:40960\n"
    "v_exp_f32 v134, v134\n"
    "v_add_f32 v247, v247, v132\n"
    "v_mfma_f32_32x32x16_bf16 v[48:63], v[204:207], v[168:171], v[48:63]\n"
    "ds_read_b64_tr_b16 v[196:197], v242 offset:33280\n"
    "ds_read_b64_tr_b16 v[198:199], v242 offset:41472\n"
    "v_exp_f32 v135, v135\n"
    "v_add_f32 v248, v248, v133\n"
    "s_waitcnt lgkmcnt(8)\n"
    "v_mfma_f32_32x32x16_bf16 v[0:15], v[208:211], v[172:175], v[0:15]\n"
    "ds_read_b64_tr_b16 v[200:201], v242 offset:33792\n"
    "ds_read_b64_tr_b16 v[202:203], v242 offset:41984\n"
    "s_add_u32 m0, s61, 0\n"
    "s_add_u32 s66, s63, 0\n"
    "buffer_load_dwordx4 v243, s[36:39], s66 offen nt lds\n"
    "buffer_load_dwordx4 v244, s[36:39], s66 offen offset:2048 nt lds\n"
    "v_mfma_f32_32x32x16_bf16 v[16:31], v[212:215], v[172:175], v[16:31]\n"
    "ds_read_b64_tr_b16 v[204:205], v242 offset:34304\n"
    "ds_read_b64_tr_b16 v[206:207], v242 offset:42496\n"
    "s_add_u32 m0, s61, 896\n"
    "s_nop 0\n"
    "buffer_load_dwordx4 v243, s[36:39], s66 offen offset:128 nt lds\n"
    "buffer_load_dwordx4 v244, s[36:39], s66 offen offset:2176 nt lds\n"
    "s_waitcnt lgkmcnt(8)\n"
    "v_mfma_f32_32x32x16_bf16 v[32:47], v[216:219], v[172:175], v[32:47]\n"
    "ds_read_b64_tr_b16 v[208:209], v242 offset:34816\n"
    "ds_read_b64_tr_b16 v[210:211], v242 offset:43008\n"
    "v_exp_f32 v136, v136\n"
    "v_add_f32 v247, v247, v134\n"
    "v_mfma_f32_32x32x16_bf16 v[48:63], v[220:223], v[172:175], v[48:63]\n"
    "ds_read_b64_tr_b16 v[212:213], v242 offset:35328\n"
    "ds_read_b64_tr_b16 v[214:215], v242 offset:43520\n"
    "s_cmp_lg_u32 s69, 0\n"
    "s_cbranch_scc1 L_stub2_lp_6\n"
    "L_back2_lp_6:\n"
    "s_waitcnt lgkmcnt(8)\n"
    "v_mfma_f32_32x32x16_bf16 a[0:15], v[192:195], v[88:91], a[0:15]\n"
    "ds_read_b64_tr_b16 v[216:217], v242 offset:35840\n"
    "ds_read_b64_tr_b16 v[218:219], v242 offset:44032\n"
    "v_exp_f32 v137, v137\n"
    "v_add_f32 v248, v248, v135\n"
    "v_mfma_f32_32x32x16_bf16 a[16:31], v[196:199], v[88:91], a[16:31]\n"
    "ds_read_b64_tr_b16 v[220:221], v242 offset:36352\n"
    "ds_read_b64_tr_b16 v[222:223], v242 offset:44544\n"
    "v_exp_f32 v138, v138\n"
    "v_add_f32 v247, v247, v136\n"
    "s_waitcnt lgkmcnt(8)\n"
    "v_mfma_f32_32x32x16_bf16 a[32:47], v[200:203], v[88:91], a[32:47]\n"
    "ds_read_b64_tr_b16 v[192:193], v242 offset:36864\n"
    "ds_read_b64_tr_b16 v[194:195], v242 offset:45056\n"
    "v_exp_f32 v139, v139\n"
    "v_add_f32 v248, v248, v137\n"
    "v_mfma_f32_32x32x16_bf16 a[48:63], v[204:207], v[88:91], a[48:63]\n"
    "ds_read_b64_tr_b16 v[196:197], v242 offset:37376\n"
    "ds_read_b64_tr_b16 v[198:199], v242 offset:45568\n"
    "v_exp_f32 v140, v140\n"
    "v_add_f32 v247, v247, v138\n"
    "s_waitcnt lgkmcnt(8)\n"
    "v_mfma_f32_32x32x16_bf16 a[64:79], v[208:211], v[88:91], a[64:79]\n"
    "ds_read_b64_tr_b16 v[200:201], v242 offset:37888\n"
    "ds_read_b64_tr_b16 v[202:203], v242 offset:46080\n"
    "v_exp_f32 v141, v141\n"
    "v_add_f32 v248, v248, v139\n"
    "v_mfma_f32_32x32x16_bf16 a[80:95], v[212:215], v[88:91], a[80:95]\n"
    "ds_read_b64_tr_b16 v[204:205], v242 offset:38400\n"
    "ds_read_b64_tr_b16 v[206:207], v242 offset:46592\n"
    "v_exp_f32 v142, v142\n"
    "v_add_f32 v247, v247, v140\n"
    "s_waitcnt lgkmcnt(8)\n"
    "v_mfma_f32_32x32x16_bf16 a[96:111], v[216:219], v[88:91], a[96:111]\n"
    "ds_read_b64_tr_b16 v[208:209], v242 offset:38912\n"
    "ds_read_b64_tr_b16 v[210:211], v242 offset:47104\n"
    "v_exp_f32 v143, v143\n"
    "v_add_f32 v248, v248, v141\n"
    "v_add_f32 v247, v247, v142\n"
    "v_mfma_f32_32x32x16_bf16 a[112:127], v[220:223], v[88:91], a[112:127]\n"
    "ds_read_b64_tr_b16 v[212:213], v242 offset:39424\n"
    "ds_read_b64_tr_b16 v[214:215], v242 offset:47616\n"
    "v_add_f32 v248, v248, v143\n"
    "v_cvt_pk_bf16_f32 v160, v128, v129\n"
    "v_cvt_pk_bf16_f32 v161, v130, v131\n"
    "s_waitcnt lgkmcnt(8)\n"
    "v_mfma_f32_32x32x16_bf16 a[128:143], v[192:195], v[88:91], a[128:143]\n"
    "ds_read_b64_tr_b16 v[216:217], v242 offset:39936\n"
    "ds_read_b64_tr_b16 v[218:219], v242 offset:48128\n"
    "v_cvt_pk_bf16_f32 v162, v132, v133\n"
    "v_cvt_pk_bf16_f32 v163, v134, v135\n"
    "v_cvt_pk_bf16_f32 v164, v136, v137\n"
    "v_cvt_pk_bf16_f32 v165, v138, v139\n"
    "v_mfma_f32_32x32x16_bf16 a[144:159], v[196:199], v[88:91], a[144:159]\n"
    "ds_read_b64_tr_b16 v[220:221], v242 offset:40448\n"
    "ds_read_b64_tr_b16 v[222:223], v242 offset:48640\n"
    "v_cvt_pk_bf16_f32 v166, v140, v141\n"
    "v_cvt_pk_bf16_f32 v167, v142, v143\n"
    "s_waitcnt lgkmcnt(8)\n"
    "v_mfma_f32_32x32x16_bf16 a[160:175], v[200:203], v[88:91], a[160:175]\n"
    "s_waitcnt vmcnt(8)\n"
    "s_barrier\n"
    "ds_read_b128 v[192:195], v240 offset:16384\n"
    "v_exp_f32 v144, v144\n"
    "v_exp_f32 v145, v145\n"
    "v_mfma_f32_32x32x16_bf16 a[176:191], v[204:207], v[88:91], a[176:191]\n"
    "ds_read_b128 v[196:199], v241 offset:16384\n"
    "v_exp_f32 v146, v146\n"
    "v_add_f32 v247, v247, v144\n"
    "s_waitcnt lgkmcnt(6)\n"
    "v_mfma_f32_32x32x16_bf16 a[192:207], v[208:211], v[88:91], a[192:207]\n"
    "ds_read_b128 v[200:203], v240 offset:16896\n"
    "v_exp_f32 v147, v147\n"
    "v_add_f32 v248, v248, v145\n"
    "v_exp_f32 v148, v148\n"
    "v_mfma_f32_32x32x16_bf16 a[208:223], v[212:215], v[88:91], a[208:223]\n"
    "ds_read_b128 v[204:207], v241 offset:16896\n"
    "v_add_f32 v247, v247, v146\n"
    "v_exp_f32 v149, v149\n"
    "v_add_f32 v248, v248, v147\n"
    "s_waitcnt lgkmcnt(4)\n"
    "v_mfma_f32_32x32x16_bf16 a[224:239], v[216:219], v[88:91], a[224:239]\n"
    "ds_read_b128 v[208:211], v240 offset:17408\n"
    "v_exp_f32 v150, v150\n"
    "v_add_f32 v247, v247, v148\n"
    "v_exp_f32 v151, v151\n"
    "v_mfma_f32_32x32x16_bf16 a[240:255], v[220:223], v[88:91], a[240:255]\n"
    "ds_read_b128 v[212:215], v241 offset:17408\n"
    "s_waitcnt lgkmcnt(4)\n"
    "v_mfma_f32_32x32x16_bf16 v[128:143], v[192:195], v[96:99], v[176:191]\n"
    "ds_read_b128 v[216:219], v240 offset:17920\n"
    "v_add_f32 v248, v248, v149\n"
    "v_exp_f32 v152, v152\n"
    "v_add_f32 v247, v247, v150\n"
    "v_mfma_f32_32x32x16_bf16 v[128:143], v[196:199], v[100:103], v[128:143]\n"
    "ds_read_b128 v[220:223], v241 offset:17920\n"
    "v_exp_f32 v153, v153\n"
    "v_add_f32 v248, v248, v151\n"
    "s_waitcnt lgkmcnt(4)\n"
    "v_mfma_f32_32x32x16_bf16 v[128:143], v[200:203], v[104:107], v[128:143]\n"
    "ds_read_b128 v[192:195], v240 offset:24576\n"
    "v_exp_f32 v154, v154\n"
    "v_add_f32 v247, v247, v152\n"
    "v_exp_f32 v155, v155\n"
    "v_mfma_f32_32x32x16_bf16 v[128:143], v[204:207], v[108:111], v[128:143]\n"
    "ds_read_b128 v[196:199], v241 offset:24576\n"
    "v_add_f32 v248, v248, v153\n"
    "v_exp_f32 v156, v156\n"
    "v_add_f32 v247, v247, v154\n"
    "s_waitcnt lgkmcnt(4)\n"
    "v_mfma_f32_32x32x16_bf16 v[128:143], v[208:211], v[112:115], v[128:143]\n"
    "ds_read_b128 v[200:203], v240 offset:25088\n"
    "v_exp_f32 v157, v157\n"
    "v_add_f32 v248, v248, v155\n"
    "v_exp_f32 v158, v158\n"
    "v_mfma_f32_32x32x16_bf16 v[128:143], v[212:215], v[116:119], v[128:143]\n"
    "ds_read_b128 v[204:207], v241 offset:25088\n"
    "v_add_f32 v247, v247, v156\n"
    "v_exp_f32 v159, v159\n"
    "v_add_f32 v248, v248, v157\n"
    "s_waitcnt lgkmcnt(4)\n"
    "v_mfma_f32_32x32x16_bf16 v[128:143], v[216:219], v[120:123], v[128:143]\n"
    "ds_read_b128 v[208:211], v240 offset:25600\n"
    "v_add_f32 v247, v247, v158\n"
    "v_add_f32 v248, v248, v159\n"
    "v_cvt_pk_bf16_f32 v168, v144, v145\n"
    "v_cvt_pk_bf16_f32 v169, v146, v147\n"
    "v_cvt_pk_bf16_f32 v170, v148, v149\n"
    "v_mfma_f32_32x32x16_bf16 v[128:143], v[220:223], v[124:127], v[128:143]\n"
    "ds_read_b128 v[212:215], v241 offset:25600\n"
    "v_cvt_pk_bf16_f32 v171, v150, v151\n"
    "v_cvt_pk_bf16_f32 v172, v152, v153\n"
    "v_cvt_pk_bf16_f32 v173, v154, v155\n"
    "v_cvt_pk_bf16_f32 v174, v156, v157\n"
    "v_cvt_pk_bf16_f32 v175, v158, v159\n"
    "s_waitcnt lgkmcnt(4)\n"
    "v_mfma_f32_32x32x16_bf16 v[144:159], v[192:195], v[96:99], v[176:191]\n"
    "ds_read_b128 v[216:219], v240 offset:26112\n"
    "s_add_u32 m0, s61, 49152\n"
    "s_add_u32 s66, s62, 114688\n"
    "buffer_load_dwordx4 v245, s[40:43], s66 offen nt lds\n"
    "buffer_load_dwordx4 v245, s[40:43], s66 offen offset:2048 nt lds\n"
    "v_mfma_f32_32x32x16_bf16 v[144:159], v[196:199], v[100:103], v[144:159]\n"
    "ds_read_b128 v[220:223], v241 offset:26112\n"
    "s_add_u32 m0, s61, 50048\n"
    "s_nop 0\n"
    "buffer_load_dwordx4 v245, s[40:43], s66 offen offset:128 nt lds\n"
    "buffer_load_dwordx4 v245, s[40:43], s66 offen offset:2176 nt lds\n"
    "s_waitcnt lgkmcnt(4)\n"
    "v_mfma_f32_32x32x16_bf16 v[144:159], v[200:203], v[104:107], v[144:159]\n"
    "s_waitcnt vmcnt(8)\n"
    "s_barrier\n"
    "ds_read_b64_tr_b16 v[192:193], v242 offset:0\n"
    "ds_read_b64_tr_b16 v[194:195], v242 offset:2048\n"
    "v_mfma_f32_32x32x16_bf16 v[144:159], v[204:207], v[108:111], v[144:159]\n"
    "ds_read_b64_tr_b16 v[196:197], v242 offset:512\n"
    "ds_read_b64_tr_b16 v[198:199], v242 offset:2560\n"
    "s_waitcnt lgkmcnt(6)\n"
    "v_mfma_f32_32x32x16_bf16 v[144:159], v[208:211], v[112:115], v[144:159]\n"
    "ds_read_b64_tr_b16 v[200:201], v242 offset:1024\n"
    "ds_read_b64_tr_b16 v[202:203], v242 offset:3072\n"
    "v_max3_f32 v249, v128, v129, v130\n"
    "v_max3_f32 v249, v249, v131, v132\n"
    "v_max3_f32 v249, v249, v133, v134\n"
    "v_max3_f32 v249, v249, v135, v136\n"
    "v_mfma_f32_32x32x16_bf16 v[144:159], v[212:215], v[116:119], v[144:159]\n"
    "ds_read_b64_tr_b16 v[204:205], v242 offset:1536\n"
    "ds_read_b64_tr_b16 v[206:207], v242 offset:3584\n"
    "v_max3_f32 v249, v249, v137, v138\n"
    "v_max3_f32 v249, v249, v139, v140\n"
    "v_max3_f32 v249, v249, v141, v142\n"
    "s_waitcnt lgkmcnt(8)\n"
    "v_mfma_f32_32x32x16_bf16 v[144:159], v[216:219], v[120:123], v[144:159]\n"
    "ds_read_b64_tr_b16 v[208:209], v242 offset:4096\n"
    "ds_read_b64_tr_b16 v[210:211], v242 offset:6144\n"
    "v_max_f32 v249, v249, v143\n"
    "v_mfma_f32_32x32x16_bf16 v[144:159], v[220:223], v[124:127], v[144:159]\n"
    "ds_read_b64_tr_b16 v[212:213], v242 offset:4608\n"
    "ds_read_b64_tr_b16 v[214:215], v242 offset:6656\n"
    "s_waitcnt lgkmcnt(8)\n"
    "v_mfma_f32_32x32x16_bf16 v[0:15], v[192:195], v[160:163], v[0:15]\n"
    "ds_read_b64_tr_b16 v[216:217], v242 offset:5120\n"
    "ds_read_b64_tr_b16 v[218:219], v242 offset:7168\n"
    "v_mfma_f32_32x32x16_bf16 v[16:31], v[196:199], v[160:163], v[16:31]\n"
    "ds_read_b64_tr_b16 v[220:221], v242 offset:5632\n"
    "ds_read_b64_tr_b16 v[222:223], v242 offset:7680\n"
    "s_waitcnt lgkmcnt(8)\n"
    "v_mfma_f32_32x32x16_bf16 v[32:47], v[200:203], v[160:163], v[32:47]\n"
    "ds_read_b64_tr_b16 v[192:193], v242 offset:8192\n"
    "ds_read_b64_tr_b16 v[194:195], v242 offset:10240\n"
    "v_max3_f32 v250, v144, v145, v146\n"
    "v_max3_f32 v250, v250, v147, v148\n"
    "v_max3_f32 v250, v250, v149, v150\n"
    "v_max3_f32 v250, v250, v151, v152\n"
    "v_max3_f32 v250, v250, v153, v154\n"
    "v_mfma_f32_32x32x16_bf16 v[48:63], v[204:207], v[160:163], v[48:63]\n"
    "ds_read_b64_tr_b16 v[196:197], v242 offset:8704\n"
    "ds_read_b64_tr_b16 v[198:199], v242 offset:10752\n"
    "v_max3_f32 v250, v250, v155, v156\n"
    "v_max3_f32 v250, v250, v157, v158\n"
    "v_max_f32 v250, v250, v159\n"
    "v_max_f32 v249, v249, v250\n"
    "s_waitcnt lgkmcnt(8)\n"
    "v_mfma_f32_32x32x16_bf16 v[0:15], v[208:211], v[164:167], v[0:15]\n"
    "ds_read_b64_tr_b16 v[200:201], v242 offset:9216\n"
    "ds_read_b64_tr_b16 v[202:203], v242 offset:11264\n"
    "v_mov_b32 v251, v249\n"
    "s_nop 1\n"
    "v_permlane32_swap_b32 v249, v251\n"
    "v_max_f32 v249, v249, v251\n"
    "v_cmp_lt_f32 vcc, 0x41000000, v249\n"
    "s_nop 0\n"
    "v_mfma_f32_32x32x16_bf16 v[16:31], v[212:215], v[164:167], v[16:31]\n"
    "ds_read_b64_tr_b16 v[204:205], v242 offset:9728\n"
    "ds_read_b64_tr_b16 v[206:207], v242 offset:11776\n"
    "s_cbranch_vccnz L_stub1_lp_7\n"
    "L_back1_lp_7:\n"
    "v_exp_f32 v128, v128\n"
    "s_waitcnt lgkmcnt(8)\n"
    "v_mfma_f32_32x32x16_bf16 v[32:47], v[216:219], v[164:167], v[32:47]\n"
    "ds_read_b64_tr_b16 v[208:209], v242 offset:12288\n"
    "ds_read_b64_tr_b16 v[210:211], v242 offset:14336\n"
    "v_exp_f32 v129, v129\n"
    "v_exp_f32 v130, v130\n"
    "v_mfma_f32_32x32x16_bf16 v[48:63], v[220:223], v[164:167], v[48:63]\n"
    "ds_read_b64_tr_b16 v[212:213], v242 offset:12800\n"
    "ds_read_b64_tr_b16 v[214:215], v242 offset:14848\n"
    "v_add_f32 v247, v247, v128\n"
    "v_exp_f32 v131, v131\n"
    "s_waitcnt lgkmcnt(8)\n"
    "v_mfma_f32_32x32x16_bf16 v[0:15], v[192:195], v[168:171], v[0:15]\n"
    "ds_read_b64_tr_b16 v[216:217], v242 offset:13312\n"
    "ds_read_b64_tr_b16 v[218:219], v242 offset:15360\n"
    "v_add_f32 v248, v248, v129\n"
    "v_exp_f32 v132, v132\n"
    "v_add_f32 v247, v247, v130\n"
    "v_mfma_f32_32x32x16_bf16 v[16:31], v[196:199], v[168:171], v[16:31]\n"
    "ds_read_b64_tr_b16 v[220:221], v242 offset:13824\n"
    "ds_read_b64_tr_b16 v[222:223], v242 offset:15872\n"
    "v_exp_f32 v133, v133\n"
    "v_add_f32 v248, v248, v131\n"
    "s_waitcnt lgkmcnt(8)\n"
    "v_mfma_f32_32x32x16_bf16 v[32:47], v[200:203], v[168:171], v[32:47]\n"
    "s_barrier\n"
    "ds_read_b64_tr_b16 v[192:193], v242 offset:49152\n"
    "ds_read_b64_tr_b16 v[194:195], v242 offset:57344\n"
    "v_exp_f32 v134, v134\n"
    "v_add_f32 v247, v247, v132\n"
    "v_mfma_f32_32x32x16_bf16 v[48:63], v[204:207], v[168:171], v[48:63]\n"
    "ds_read_b64_tr_b16 v[196:197], v242 offset:49664\n"
    "ds_read_b64_tr_b16 v[198:199], v242 offset:57856\n"
    "v_exp_f32 v135, v135\n"
    "v_add_f32 v248, v248, v133\n"
    "s_waitcnt lgkmcnt(8)\n"
    "v_mfma_f32_32x32x16_bf16 v[0:15], v[208:211], v[172:175], v[0:15]\n"
    "ds_read_b64_tr_b16 v[200:201], v242 offset:50176\n"
    "ds_read_b64_tr_b16 v[202:203], v242 offset:58368\n"
    "s_add_u32 m0, s61, 16384\n"
    "s_add_u32 s66, s63, 16384\n"
    "buffer_load_dwordx4 v243, s[36:39], s66 offen nt lds\n"
    "buffer_load_dwordx4 v244, s[36:39], s66 offen offset:2048 nt lds\n"
    "v_mfma_f32_32x32x16_bf16 v[16:31], v[212:215], v[172:175], v[16:31]\n"
    "ds_read_b64_tr_b16 v[204:205], v242 offset:50688\n"
    "ds_read_b64_tr_b16 v[206:207], v242 offset:58880\n"
    "s_add_u32 m0, s61, 17280\n"
    "s_nop 0\n"
    "buffer_load_dwordx4 v243, s[36:39], s66 offen offset:128 nt lds\n"
    "buffer_load_dwordx4 v244, s[36:39], s66 offen offset:2176 nt lds\n"
    "s_waitcnt lgkmcnt(8)\n"
    "v_mfma_f32_32x32x16_bf16 v[32:47], v[216:219], v[172:175], v[32:47]\n"
    "ds_read_b64_tr_b16 v[208:209], v242 offset:51200\n"
    "ds_read_b64_tr_b16 v[210:211], v242 offset:59392\n"
    "v_exp_f32 v136, v136\n"
    "v_add_f32 v247, v247, v134\n"
    "v_mfma_f32_32x32x16_bf16 v[48:63], v[220:223], v[172:175], v[48:63]\n"
    "ds_read_b64_tr_b16 v[212:213], v242 offset:51712\n"
    "ds_read_b64_tr_b16 v[214:215], v242 offset:59904\n"
    "s_cmp_lg_u32 s69, 0\n"
    "s_cbranch_scc1 L_stub2_lp_7\n"
    "L_back2_lp_7:\n"
    "s_waitcnt lgkmcnt(8)\n"
    "v_mfma_f32_32x32x16_bf16 a[0:15], v[192:195], v[92:95], a[0:15]\n"
    "ds_read_b64_tr_b16 v[216:217], v242 offset:52224\n"
    "ds_read_b64_tr_b16 v[218:219], v242 offset:60416\n"
    "v_exp_f32 v137, v137\n"
    "v_add_f32 v248, v248, v135\n"
    "v_mfma_f32_32x32x16_bf16 a[16:31], v[196:199], v[92:95], a[16:31]\n"
    "ds_read_b64_tr_b16 v[220:221], v242 offset:52736\n"
    "ds_read_b64_tr_b16 v[222:223], v242 offset:60928\n"
    "v_exp_f32 v138, v138\n"
    "v_add_f32 v247, v247, v136\n"
    "s_waitcnt lgkmcnt(8)\n"
    "v_mfma_f32_32x32x16_bf16 a[32:47], v[200:203], v[92:95], a[32:47]\n"
    "ds_read_b64_tr_b16 v[192:193], v242 offset:53248\n"
    "ds_read_b64_tr_b16 v[194:195], v242 offset:61440\n"
    "v_exp_f32 v139, v139\n"
    "v_add_f32 v248, v248, v137\n"
    "v_mfma_f32_32x32x16_bf16 a[48:63], v[204:207], v[92:95], a[48:63]\n"
    "ds_read_b64_tr_b16 v[196:197], v242 offset:53760\n"
    "ds_read_b64_tr_b16 v[198:199], v242 offset:61952\n"
    "v_exp_f32 v140, v140\n"
    "v_add_f32 v247, v247, v138\n"
    "s_waitcnt lgkmcnt(8)\n"
    "v_mfma_f32_32x32x16_bf16 a[64:79], v[208:211], v[92:95], a[64:79]\n"
    "ds_read_b64_tr_b16 v[200:201], v242 offset:54272\n"
    "ds_read_b64_tr_b16 v[202:203], v242 offset:62464\n"
    "v_exp_f32 v141, v141\n"
    "v_add_f32 v248, v248, v139\n"
    "v_mfma_f32_32x32x16_bf16 a[80:95], v[212:215], v[92:95], a[80:95]\n"
    "ds_read_b64_tr_b16 v[204:205], v242 offset:54784\n"
    "ds_read_b64_tr_b16 v[206:207], v242 offset:62976\n"
    "v_exp_f32 v142, v142\n"
    "v_add_f32 v247, v247, v140\n"
    "s_waitcnt lgkmcnt(8)\n"
    "v_mfma_f32_32x32x16_bf16 a[96:111], v[216:219], v[92:95], a[96:111]\n"
    "ds_read_b64_tr_b16 v[208:209], v242 offset:55296\n"
    "ds_read_b64_tr_b16 v[210:211], v242 offset:63488\n"
    "v_exp_f32 v143, v143\n"
    "v_add_f32 v248, v248, v141\n"
    "v_add_f32 v247, v247, v142\n"
    "v_mfma_f32_32x32x16_bf16 a[112:127], v[220:223], v[92:95], a[112:127]\n"
    "ds_read_b64_tr_b16 v[212:213], v242 offset:55808\n"
    "ds_read_b64_tr_b16 v[214:215], v242 offset:64000\n"
    "v_add_f32 v248, v248, v143\n"
    "v_cvt_pk_bf16_f32 v160, v128, v129\n"
    "v_cvt_pk_bf16_f32 v161, v130, v131\n"
    "s_waitcnt lgkmcnt(8)\n"
    "v_mfma_f32_32x32x16_bf16 a[128:143], v[192:195], v[92:95], a[128:143]\n"
    "ds_read_b64_tr_b16 v[216:217], v242 offset:56320\n"
    "ds_read_b64_tr_b16 v[218:219], v242 offset:64512\n"
    "v_cvt_pk_bf16_f32 v162, v132, v133\n"
    "v_cvt_pk_bf16_f32 v163, v134, v135\n"
    "v_cvt_pk_bf16_f32 v164, v136, v137\n"
    "v_cvt_pk_bf16_f32 v165, v138, v139\n"
    "v_mfma_f32_32x32x16_bf16 a[144:159], v[196:199], v[92:95], a[144:159]\n"
    "ds_read_b64_tr_b16 v[220:221], v242 offset:56832\n"
    "ds_read_b64_tr_b16 v[222:223], v242 offset:65024\n"
    "v_cvt_pk_bf16_f32 v166, v140, v141\n"
    "v_cvt_pk_bf16_f32 v167, v142, v143\n"
    "s_waitcnt lgkmcnt(8)\n"
    "v_mfma_f32_32x32x16_bf16 a[160:175], v[200:203], v[92:95], a[160:175]\n"
    "s_waitcnt vmcnt(8)\n"
    "s_barrier\n"
    "ds_read_b128 v[192:195], v240 offset:0\n"
    "ds_read_b128 v[96:99], v254 offset:0\n"
    "v_exp_f32 v144, v144\n"
    "v_mfma_f32_32x32x16_bf16 a[176:191], v[204:207], v[92:95], a[176:191]\n"
    "ds_read_b128 v[196:199], v241 offset:0\n"
    "ds_read_b128 v[100:103], v254 offset:1024\n"
    "v_exp_f32 v145, v145\n"
    "s_waitcnt lgkmcnt(8)\n"
    "v_mfma_f32_32x32x16_bf16 a[192:207], v[208:211], v[92:95], a[192:207]\n"
    "ds_read_b128 v[200:203], v240 offset:512\n"
    "ds_read_b128 v[104:107], v254 offset:2048\n"
    "v_exp_f32 v146, v146\n"
    "v_add_f32 v247, v247, v144\n"
    "v_mfma_f32_32x32x16_bf16 a[208:223], v[212:215], v[92:95], a[208:223]\n"
    "ds_read_b128 v[204:207], v241 offset:512\n"
    "ds_read_b128 v[108:111], v254 offset:3072\n"
    "v_exp_f32 v147, v147\n"
    "v_add_f32 v248, v248, v145\n"
    "s_waitcnt lgkmcnt(8)\n"
    "v_mfma_f32_32x32x16_bf16 a[224:239], v[216:219], v[92:95], a[224:239]\n"
    "ds_read_b128 v[208:211], v240 offset:1024\n"
    "ds_read_b128 v[112:115], v254 offset:4096\n"
    "v_exp_f32 v148, v148\n"
    "v_add_f32 v247, v247, v146\n"
    "v_mfma_f32_32x32x16_bf16 a[240:255], v[220:223], v[92:95], a[240:255]\n"
    "ds_read_b128 v[212:215], v241 offset:1024\n"
    "ds_read_b128 v[116:119], v254 offset:5120\n"
    "ds_read_b128 v[120:123], v254 offset:6144\n"
    "ds_read_b128 v[124:127], v254 offset:7168\n"
    "v_exp_f32 v149, v149\n"
    "v_add_f32 v248, v248, v147\n"
    "v_exp_f32 v150, v150\n"
    "v_add_f32 v247, v247, v148\n"
    "v_exp_f32 v151, v151\n"
    "s_mov_b32 s64, s65\n"
    "s_mov_b32 s62, s63\n"
    "s_add_u32 s63, s63, 131072\n"
    "s_add_u32 s65, s65, 131072\n"
    "s_sub_u32 s68, s68, 1\n"
    "s_cmp_eq_u32 s68, 1\n"
    "s_cselect_b32 s63, 268435456, s63\n"
    "s_cselect_b32 s65, 268435456, s65\n"
    "s_cmp_lg_u32 s68, 0\n"
    "s_cbranch_scc1 L_att_loop\n"
    "v_add_f32 v248, v248, v149\n"
    "v_exp_f32 v152, v152\n"
    "v_add_f32 v247, v247, v150\n"
    "v_exp_f32 v153, v153\n"
    "v_add_f32 v248, v248, v151\n"
    "v_exp_f32 v154, v154\n"
    "v_add_f32 v247, v247, v152\n"
    "v_exp_f32 v155, v155\n"
    "v_add_f32 v248, v248, v153\n"
    "v_exp_f32 v156, v156\n"
    "v_add_f32 v247, v247, v154\n"
    "v_exp_f32 v157, v157\n"
    "v_add_f32 v248, v248, v155\n"
    "v_exp_f32 v158, v158\n"
    "v_add_f32 v247, v247, v156\n"
    "v_exp_f32 v159, v159\n"
    "v_add_f32 v248, v248, v157\n"
    "v_add_f32 v247, v247, v158\n"
    "v_add_f32 v248, v248, v159\n"
    "v_cvt_pk_bf16_f32 v168, v144, v145\n"
    "v_cvt_pk_bf16_f32 v169, v146, v147\n"
    "v_cvt_pk_bf16_f32 v170, v148, v149\n"
    "v_cvt_pk_bf16_f32 v171, v150, v151\n"
    "v_cvt_pk_bf16_f32 v172, v152, v153\n"
    "v_cvt_pk_bf16_f32 v173, v154, v155\n"
    "v_cvt_pk_bf16_f32 v174, v156, v157\n"
    "v_cvt_pk_bf16_f32 v175, v158, v159\n"
    "v_add_f32 v253, v247, v248\n"
    "v_mov_b32 v251, v253\n"
    "s_nop 1\n"
    "v_permlane32_swap_b32 v253, v251\n"
    "v_add_f32 v253, v253, v251\n"
    "v_rcp_f32 v253, v253\n"
    "s_add_u32 s79, s78, 3\n"
    "s_lshl_b32 s79, s79, 17\n"
    "s_lshl_b32 s67, s60, 15\n"
    "s_add_u32 s79, s79, s67\n"
    "v_mbcnt_lo_u32_b32 v254, -1, 0\n"
    "v_mbcnt_hi_u32_b32 v254, -1, v254\n"
    "v_lshlrev_b32 v254, 4, v254\n"
    "v_add_u32 v176, 65536, v242\n"
    "s_waitcnt vmcnt(4)\n"
    "s_barrier\n"
    "s_add_u32 m0, s61, 32768\n"
    "s_add_u32 s66, s79, 512\n"
    "buffer_load_dwordx4 v246, s[48:51], s66 offen lds\n"
    "s_add_u32 m0, s61, 33792\n"
    "s_add_u32 s66, s79, 8704\n"
    "buffer_load_dwordx4 v246, s[48:51], s66 offen lds\n"
    "s_add_u32 m0, s61, 34816\n"
    "s_add_u32 s66, s79, 16896\n"
    "buffer_load_dwordx4 v246, s[48:51], s66 offen lds\n"
    "s_add_u32 m0, s61, 35840\n"
    "s_add_u32 s66, s79, 25088\n"
    "buffer_load_dwordx4 v246, s[48:51], s66 offen lds\n"
    "s_add_u32 m0, s61, 65536\n"
    "s_add_u32 s66, s79, 640\n"
    "buffer_load_dwordx4 v246, s[48:51], s66 offen lds\n"
    "s_add_u32 m0, s61, 66560\n"
    "s_add_u32 s66, s79, 8832\n"
    "buffer_load_dwordx4 v246, s[48:51], s66 offen lds\n"
    "s_add_u32 m0, s61, 67584\n"
    "s_add_u32 s66, s79, 17024\n"
    "buffer_load_dwordx4 v246, s[48:51], s66 offen lds\n"
    "s_add_u32 m0, s61, 68608\n"
    "s_add_u32 s66, s79, 25216\n"
    "buffer_load_dwordx4 v246, s[48:51], s66 offen lds\n"
    "s_add_u32 m0, s61, 81920\n"
    "s_add_u32 s66, s79, 768\n"
    "buffer_load_dwordx4 v246, s[48:51], s66 offen lds\n"
    "s_add_u32 m0, s61, 82944\n"
    "s_add_u32 s66, s79, 8960\n"
    "buffer_load_dwordx4 v246, s[48:51], s66 offen lds\n"
    "s_add_u32 m0, s61, 83968\n"
    "s_add_u32 s66, s79, 17152\n"
    "buffer_load_dwordx4 v246, s[48:51], s66 offen lds\n"
    "s_add_u32 m0, s61, 84992\n"
    "s_add_u32 s66, s79, 25344\n"
    "buffer_load_dwordx4 v246, s[48:51], s66 offen lds\n"
    "ds_read_b64_tr_b16 v[216:217], v242 offset:16384\n"
    "s_waitcnt lgkmcnt(14)\n"
    "ds_read_b64_tr_b16 v[218:219], v242 offset:18432\n"
    "s_waitcnt lgkmcnt(14)\n"
    "ds_read_b64_tr_b16 v[220:221], v242 offset:20480\n"
    "s_waitcnt lgkmcnt(14)\n"
    "ds_read_b64_tr_b16 v[222:223], v242 offset:22528\n"
    "s_waitcnt lgkmcnt(14)\n"
    "ds_read_b64_tr_b16 v[192:193], v242 offset:24576\n"
    "s_waitcnt lgkmcnt(14)\n"
    "ds_read_b64_tr_b16 v[194:195], v242 offset:26624\n"
    "s_waitcnt lgkmcnt(14)\n"
    "ds_read_b64_tr_b16 v[196:197], v242 offset:28672\n"
    "s_waitcnt lgkmcnt(14)\n"
    "ds_read_b64_tr_b16 v[198:199], v242 offset:30720\n"
    "s_waitcnt lgkmcnt(14)\n"
    "ds_read_b64_tr_b16 v[200:201], v242 offset:16896\n"
    "s_waitcnt lgkmcnt(14)\n"
    "ds_read_b64_tr_b16 v[202:203], v242 offset:18944\n"
    "s_waitcnt lgkmcnt(14)\n"
    "ds_read_b64_tr_b16 v[204:205], v242 offset:20992\n"
    "s_waitcnt lgkmcnt(14)\n"
    "ds_read_b64_tr_b16 v[206:207], v242 offset:23040\n"
    "s_waitcnt lgkmcnt(8)\n"
    "v_mfma_f32_32x32x16_bf16 v[0:15], v[216:219], v[160:163], v[0:15]\n"
    "ds_read_b64_tr_b16 v[208:209], v242 offset:25088\n"
    "ds_read_b64_tr_b16 v[210:211], v242 offset:27136\n"
    "v_mfma_f32_32x32x16_bf16 v[0:15], v[220:223], v[164:167], v[0:15]\n"
    "ds_read_b64_tr_b16 v[212:213], v242 offset:29184\n"
    "ds_read_b64_tr_b16 v[214:215], v242 offset:31232\n"
    "s_waitcnt lgkmcnt(8)\n"
    "v_mfma_f32_32x32x16_bf16 v[0:15], v[192:195], v[168:171], v[0:15]\n"
    "ds_read_b64_tr_b16 v[216:217], v242 offset:17408\n"
    "ds_read_b64_tr_b16 v[218:219], v242 offset:19456\n"
    "v_mfma_f32_32x32x16_bf16 v[0:15], v[196:199], v[172:175], v[0:15]\n"
    "ds_read_b64_tr_b16 v[220:221], v242 offset:21504\n"
    "ds_read_b64_tr_b16 v[222:223], v242 offset:23552\n"
    "s_waitcnt lgkmcnt(8)\n"
    "v_mfma_f32_32x32x16_bf16 v[16:31], v[200:203], v[160:163], v[16:31]\n"
    "ds_read_b64_tr_b16 v[192:193], v242 offset:25600\n"
    "ds_read_b64_tr_b16 v[194:195], v242 offset:27648\n"
    "v_mfma_f32_32x32x16_bf16 v[16:31], v[204:207], v[164:167], v[16:31]\n"
    "ds_read_b64_tr_b16 v[196:197], v242 offset:29696\n"
    "ds_read_b64_tr_b16 v[198:199], v242 offset:31744\n"
    "s_waitcnt lgkmcnt(8)\n"
    "v_mfma_f32_32x32x16_bf16 v[16:31], v[208:211], v[168:171], v[16:31]\n"
    "ds_read_b64_tr_b16 v[200:201], v242 offset:17920\n"
    "ds_read_b64_tr_b16 v[202:203], v242 offset:19968\n"
    "v_mfma_f32_32x32x16_bf16 v[16:31], v[212:215], v[172:175], v[16:31]\n"
    "ds_read_b64_tr_b16 v[204:205], v242 offset:22016\n"
    "ds_read_b64_tr_b16 v[206:207], v242 offset:24064\n"
    "v_mul_f32 v0, v0, v253\n"
    "v_mul_f32 v1, v1, v253\n"
    "v_cvt_pk_bf16_f32 v64, v0, v1\n"
    "v_mul_f32 v2, v2, v253\n"
    "v_mul_f32 v3, v3, v253\n"
    "s_waitcnt lgkmcnt(8)\n"
    "v_mfma_f32_32x32x16_bf16 v[32:47], v[216:219], v[160:163], v[32:47]\n"
    "ds_read_b64_tr_b16 v[208:209], v242 offset:26112\n"
    "ds_read_b64_tr_b16 v[210:211], v242 offset:28160\n"
    "v_cvt_pk_bf16_f32 v65, v2, v3\n"
    "v_mul_f32 v4, v4, v253\n"
    "v_mul_f32 v5, v5, v253\n"
    "v_cvt_pk_bf16_f32 v66, v4, v5\n"
    "v_mul_f32 v6, v6, v253\n"
    "v_mul_f32 v7, v7, v253\n"
    "v_mfma_f32_32x32x16_bf16 v[32:47], v[220:223], v[164:167], v[32:47]\n"
    "ds_read_b64_tr_b16 v[212:213], v242 offset:30208\n"
    "ds_read_b64_tr_b16 v[214:215], v242 offset:32256\n"
    "v_cvt_pk_bf16_f32 v67, v6, v7\n"
    "v_mul_f32 v8, v8, v253\n"
    "v_mul_f32 v9, v9, v253\n"
    "v_cvt_pk_bf16_f32 v68, v8, v9\n"
    "v_mul_f32 v10, v10, v253\n"
    "s_waitcnt lgkmcnt(8)\n"
    "v_mfma_f32_32x32x16_bf16 v[32:47], v[192:195], v[168:171], v[32:47]\n"
    "s_barrier\n"
    "ds_read_b64_tr_b16 v[216:217], v176 offset:0\n"
    "ds_read_b64_tr_b16 v[218:219], v176 offset:1024\n"
    "v_mul_f32 v11, v11, v253\n"
    "v_cvt_pk_bf16_f32 v69, v10, v11\n"
    "v_mul_f32 v12, v12, v253\n"
    "v_mul_f32 v13, v13, v253\n"
    "v_cvt_pk_bf16_f32 v70, v12, v13\n"
    "v_mfma_f32_32x32x16_bf16 v[32:47], v[196:199], v[172:175], v[32:47]\n"
    "ds_read_b64_tr_b16 v[220:221], v176 offset:512\n"
    "ds_read_b64_tr_b16 v[222:223], v176 offset:1536\n"
    "v_mul_f32 v14, v14, v253\n"
    "v_mul_f32 v15, v15, v253\n"
    "v_cvt_pk_bf16_f32 v71, v14, v15\n"
    "v_mul_f32 v16, v16, v253\n"
    "v_mul_f32 v17, v17, v253\n"
    "v_cvt_pk_bf16_f32 v72, v16, v17\n"
    "s_waitcnt lgkmcnt(8)\n"
    "v_mfma_f32_32x32x16_bf16 v[48:63], v[200:203], v[160:163], v[48:63]\n"
    "ds_read_b64_tr_b16 v[192:193], v176 offset:2048\n"
    "ds_read_b64_tr_b16 v[194:195], v176 offset:3072\n"
    "v_mul_f32 v18, v18, v253\n"
    "v_mul_f32 v19, v19, v253\n"
    "v_cvt_pk_bf16_f32 v73, v18, v19\n"
    "v_mul_f32 v20, v20, v253\n"
    "v_mul_f32 v21, v21, v253\n"
    "v_cvt_pk_bf16_f32 v74, v20, v21\n"
    "v_mul_f32 v22, v22, v253\n"
    "v_mfma_f32_32x32x16_bf16 v[48:63], v[204:207], v[164:167], v[48:63]\n"
    "ds_read_b64_tr_b16 v[196:197], v176 offset:2560\n"
    "ds_read_b64_tr_b16 v[198:199], v176 offset:3584\n"
    "v_mul_f32 v23, v23, v253\n"
    "v_cvt_pk_bf16_f32 v75, v22, v23\n"
    "v_mul_f32 v24, v24, v253\n"
    "v_mul_f32 v25, v25, v253\n"
    "v_cvt_pk_bf16_f32 v76, v24, v25\n"
    "v_mul_f32 v26, v26, v253\n"
    "s_waitcnt lgkmcnt(8)\n"
    "v_mfma_f32_32x32x16_bf16 v[48:63], v[208:211], v[168:171], v[48:63]\n"
    "ds_read_b64_tr_b16 v[200:201], v176 offset:4096\n"
    "ds_read_b64_tr_b16 v[202:203], v176 offset:5120\n"
    "v_mul_f32 v27, v27, v253\n"
    "v_cvt_pk_bf16_f32 v77, v26, v27\n"
    "v_mul_f32 v28, v28, v253\n"
    "v_mul_f32 v29, v29, v253\n"
    "v_cvt_pk_bf16_f32 v78, v28, v29\n"
    "v_mul_f32 v30, v30, v253\n"
    "v_mul_f32 v31, v31, v253\n"
    "v_mfma_f32_32x32x16_bf16 v[48:63], v[212:215], v[172:175], v[48:63]\n"
    "ds_read_b64_tr_b16 v[204:205], v176 offset:4608\n"
    "ds_read_b64_tr_b16 v[206:207], v176 offset:5632\n"
    "v_cvt_pk_bf16_f32 v79, v30, v31\n"
    "v_mul_f32 v32, v32, v253\n"
    "v_mul_f32 v33, v33, v253\n"
    "v_cvt_pk_bf16_f32 v80, v32, v33\n"
    "v_mul_f32 v34, v34, v253\n"
    "v_mul_f32 v35, v35, v253\n"
    "s_waitcnt lgkmcnt(8)\n"
    "v_mfma_f32_32x32x16_bf16 a[0:15], v[216:219], v[64:67], a[0:15]\n"
    "ds_read_b64_tr_b16 v[208:209], v176 offset:6144\n"
    "ds_read_b64_tr_b16 v[210:211], v176 offset:7168\n"
    "v_cvt_pk_bf16_f32 v81, v34, v35\n"
    "v_mul_f32 v36, v36, v253\n"
    "v_mul_f32 v37, v37, v253\n"
    "v_cvt_pk_bf16_f32 v82, v36, v37\n"
    "v_mul_f32 v38, v38, v253\n"
    "v_mul_f32 v39, v39, v253\n"
    "v_cvt_pk_bf16_f32 v83, v38, v39\n"
    "v_mfma_f32_32x32x16_bf16 a[16:31], v[220:223], v[64:67], a[16:31]\n"
    "ds_read_b64_tr_b16 v[212:213], v176 offset:6656\n"
    "ds_read_b64_tr_b16 v[214:215], v176 offset:7680\n"
    "v_mul_f32 v40, v40, v253\n"
    "v_mul_f32 v41, v41, v253\n"
    "v_cvt_pk_bf16_f32 v84, v40, v41\n"
    "v_mul_f32 v42, v42, v253\n"
    "v_mul_f32 v43, v43, v253\n"
    "v_cvt_pk_bf16_f32 v85, v42, v43\n"
    "s_waitcnt lgkmcnt(8)\n"
    "v_mfma_f32_32x32x16_bf16 a[0:15], v[192:195], v[68:71], a[0:15]\n"
    "ds_read_b64_tr_b16 v[216:217], v176 offset:8192\n"
    "ds_read_b64_tr_b16 v[218:219], v176 offset:9216\n"
    "v_mul_f32 v44, v44, v253\n"
    "v_mul_f32 v45, v45, v253\n"
    "v_cvt_pk_bf16_f32 v86, v44, v45\n"
    "v_mul_f32 v46, v46, v253\n"
    "v_mul_f32 v47, v47, v253\n"
    "v_cvt_pk_bf16_f32 v87, v46, v47\n"
    "v_mfma_f32_32x32x16_bf16 a[16:31], v[196:199], v[68:71], a[16:31]\n"
    "ds_read_b64_tr_b16 v[220:221], v176 offset:8704\n"
    "ds_read_b64_tr_b16 v[222:223], v176 offset:9728\n"
    "v_mul_f32 v48, v48, v253\n"
    "v_mul_f32 v49, v49, v253\n"
    "v_cvt_pk_bf16_f32 v88, v48, v49\n"
    "v_mul_f32 v50, v50, v253\n"
    "v_mul_f32 v51, v51, v253\n"
    "v_cvt_pk_bf16_f32 v89, v50, v51\n"
    "s_waitcnt lgkmcnt(8)\n"
    "v_mfma_f32_32x32x16_bf16 a[0:15], v[200:203], v[72:75], a[0:15]\n"
    "ds_read_b64_tr_b16 v[192:193], v176 offset:10240\n"
    "ds_read_b64_tr_b16 v[194:195], v176 offset:11264\n"
    "v_mul_f32 v52, v52, v253\n"
    "v_mul_f32 v53, v53, v253\n"
    "v_cvt_pk_bf16_f32 v90, v52, v53\n"
    "v_mul_f32 v54, v54, v253\n"
    "v_mul_f32 v55, v55, v253\n"
    "v_cvt_pk_bf16_f32 v91, v54, v55\n"
    "v_mul_f32 v56, v56, v253\n"
    "v_mfma_f32_32x32x16_bf16 a[16:31], v[204:207], v[72:75], a[16:31]\n"
    "ds_read_b64_tr_b16 v[196:197], v176 offset:10752\n"
    "ds_read_b64_tr_b16 v[198:199], v176 offset:11776\n"
    "v_mul_f32 v57, v57, v253\n"
    "v_cvt_pk_bf16_f32 v92, v56, v57\n"
    "v_mul_f32 v58, v58, v253\n"
    "v_mul_f32 v59, v59, v253\n"
    "v_cvt_pk_bf16_f32 v93, v58, v59\n"
    "v_mul_f32 v60, v60, v253\n"
    "s_waitcnt lgkmcnt(8)\n"
    "v_mfma_f32_32x32x16_bf16 a[0:15], v[208:211], v[76:79], a[0:15]\n"
    "ds_read_b64_tr_b16 v[200:201], v176 offset:12288\n"
    "ds_read_b64_tr_b16 v[202:203], v176 offset:13312\n"
    "v_mul_f32 v61, v61, v253\n"
    "v_cvt_pk_bf16_f32 v94, v60, v61\n"
    "v_mul_f32 v62, v62, v253\n"
    "v_mul_f32 v63, v63, v253\n"
    "v_cvt_pk_bf16_f32 v95, v62, v63\n"
    "v_mfma_f32_32x32x16_bf16 a[16:31], v[212:215], v[76:79], a[16:31]\n"
    "ds_read_b64_tr_b16 v[204:205], v176 offset:12800\n"
    "ds_read_b64_tr_b16 v[206:207], v176 offset:13824\n"
    "s_waitcnt lgkmcnt(8)\n"
    "v_mfma_f32_32x32x16_bf16 a[0:15], v[216:219], v[80:83], a[0:15]\n"
    "ds_read_b64_tr_b16 v[208:209], v176 offset:14336\n"
    "ds_read_b64_tr_b16 v[210:211], v176 offset:15360\n"
    "v_mfma_f32_32x32x16_bf16 a[16:31], v[220:223], v[80:83], a[16:31]\n"
    "ds_read_b64_tr_b16 v[212:213], v176 offset:14848\n"
    "ds_read_b64_tr_b16 v[214:215], v176 offset:15872\n"
    "s_waitcnt lgkmcnt(8)\n"
    "v_mfma_f32_32x32x16_bf16 a[0:15], v[192:195], v[84:87], a[0:15]\n"
    "s_barrier\n"
    "ds_read_b64_tr_b16 v[216:217], v176 offset:16384\n"
    "ds_read_b64_tr_b16 v[218:219], v176 offset:17408\n"
    "v_mfma_f32_32x32x16_bf16 a[16:31], v[196:199], v[84:87], a[16:31]\n"
    "ds_read_b64_tr_b16 v[220:221], v176 offset:16896\n"
    "ds_read_b64_tr_b16 v[222:223], v176 offset:17920\n"
    "s_add_u32 m0, s61, 49152\n"
    "s_add_u32 s66, s79, 896\n"
    "buffer_load_dwordx4 v246, s[48:51], s66 offen lds\n"
    "s_waitcnt lgkmcnt(8)\n"
    "v_mfma_f32_32x32x16_bf16 a[0:15], v[200:203], v[88:91], a[0:15]\n"
    "ds_read_b64_tr_b16 v[192:193], v176 offset:18432\n"
    "ds_read_b64_tr_b16 v[194:195], v176 offset:19456\n"
    "s_add_u32 m0, s61, 50176\n"
    "s_add_u32 s66, s79, 9088\n"
    "buffer_load_dwordx4 v246, s[48:51], s66 offen lds\n"
    "s_add_u32 m0, s61, 51200\n"
    "s_add_u32 s66, s79, 17280\n"
    "buffer_load_dwordx4 v246, s[48:51], s66 offen lds\n"
    "v_mfma_f32_32x32x16_bf16 a[16:31], v[204:207], v[88:91], a[16:31]\n"
    "ds_read_b64_tr_b16 v[196:197], v176 offset:18944\n"
    "ds_read_b64_tr_b16 v[198:199], v176 offset:19968\n"
    "s_add_u32 m0, s61, 52224\n"
    "s_add_u32 s66, s79, 25472\n"
    "buffer_load_dwordx4 v246, s[48:51], s66 offen lds\n"
    "s_waitcnt lgkmcnt(8)\n"
    "v_mfma_f32_32x32x16_bf16 a[0:15], v[208:211], v[92:95], a[0:15]\n"
    "ds_read_b64_tr_b16 v[200:201], v176 offset:20480\n"
    "ds_read_b64_tr_b16 v[202:203], v176 offset:21504\n"
    "v_mfma_f32_32x32x16_bf16 a[16:31], v[212:215], v[92:95], a[16:31]\n"
    "ds_read_b64_tr_b16 v[204:205], v176 offset:20992\n"
    "ds_read_b64_tr_b16 v[206:207], v176 offset:22016\n"
    "s_waitcnt lgkmcnt(8)\n"
    "v_mfma_f32_32x32x16_bf16 a[32:47], v[216:219], v[64:67], a[32:47]\n"
    "ds_read_b64_tr_b16 v[208:209], v176 offset:22528\n"
    "ds_read_b64_tr_b16 v[210:211], v176 offset:23552\n"
    "v_mfma_f32_32x32x16_bf16 a[48:63], v[220:223], v[64:67], a[48:63]\n"
    "ds_read_b64_tr_b16 v[212:213], v176 offset:23040\n"
    "ds_read_b64_tr_b16 v[214:215], v176 offset:24064\n"
    "s_waitcnt lgkmcnt(8)\n"
    "v_mfma_f32_32x32x16_bf16 a[32:47], v[192:195], v[68:71], a[32:47]\n"
    "ds_read_b64_tr_b16 v[216:217], v176 offset:24576\n"
    "ds_read_b64_tr_b16 v[218:219], v176 offset:25600\n"
    "v_mfma_f32_32x32x16_bf16 a[48:63], v[196:199], v[68:71], a[48:63]\n"
    "ds_read_b64_tr_b16 v[220:221], v176 offset:25088\n"
    "ds_read_b64_tr_b16 v[222:223], v176 offset:26112\n"
    "v_accvgpr_read_b32 v224, a0\n"
    "v_accvgpr_read_b32 v225, a1\n"
    "v_accvgpr_read_b32 v226, a2\n"
    "v_accvgpr_read_b32 v227, a3\n"
    "s_waitcnt lgkmcnt(8)\n"
    "v_mfma_f32_32x32x16_bf16 a[32:47], v[200:203], v[72:75], a[32:47]\n"
    "ds_read_b64_tr_b16 v[192:193], v176 offset:26624\n"
    "ds_read_b64_tr_b16 v[194:195], v176 offset:27648\n"
    "v_accvgpr_read_b32 v228, a4\n"
    "v_accvgpr_read_b32 v229, a5\n"
    "v_accvgpr_read_b32 v230, a6\n"
    "v_accvgpr_read_b32 v231, a7\n"
    "v_accvgpr_read_b32 v232, a8\n"
    "v_mfma_f32_32x32x16_bf16 a[48:63], v[204:207], v[72:75], a[48:63]\n"
    "ds_read_b64_tr_b16 v[196:197], v176 offset:27136\n"
    "ds_read_b64_tr_b16 v[198:199], v176 offset:28160\n"
    "v_accvgpr_read_b32 v233, a9\n"
    "v_accvgpr_read_b32 v234, a10\n"
    "v_accvgpr_read_b32 v235, a11\n"
    "v_accvgpr_read_b32 v236, a12\n"
    "s_waitcnt lgkmcnt(8)\n"
    "v_mfma_f32_32x32x16_bf16 a[32:47], v[208:211], v[76:79], a[32:47]\n"
    "ds_read_b64_tr_b16 v[200:201], v176 offset:28672\n"
    "ds_read_b64_tr_b16 v[202:203], v176 offset:29696\n"
    "v_accvgpr_read_b32 v237, a13\n"
    "v_accvgpr_read_b32 v238, a14\n"
    "v_accvgpr_read_b32 v239, a15\n"
    "v_cvt_pk_bf16_f32 v224, v224, v225\n"
    "v_cvt_pk_bf16_f32 v225, v226, v227\n"
    "v_mfma_f32_32x32x16_bf16 a[48:63], v[212:215], v[76:79], a[48:63]\n"
    "ds_read_b64_tr_b16 v[204:205], v176 offset:29184\n"
    "ds_read_b64_tr_b16 v[206:207], v176 offset:30208\n"
    "v_cvt_pk_bf16_f32 v226, v228, v229\n"
    "v_cvt_pk_bf16_f32 v227, v230, v231\n"
    "v_cvt_pk_bf16_f32 v228, v232, v233\n"
    "v_cvt_pk_bf16_f32 v229, v234, v235\n"
    "s_waitcnt lgkmcnt(8)\n"
    "v_mfma_f32_32x32x16_bf16 a[32:47], v[216:219], v[80:83], a[32:47]\n"
    "ds_read_b64_tr_b16 v[208:209], v176 offset:30720\n"
    "ds_read_b64_tr_b16 v[210:211], v176 offset:31744\n"
    "v_cvt_pk_bf16_f32 v230, v236, v237\n"
    "v_cvt_pk_bf16_f32 v231, v238, v239\n"
    "s_add_u32 s67, s77, 0\n"
    "buffer_store_dwordx4 v[224:227], v254, s[56:59], s67 offen sc1\n"
    "buffer_store_dwordx4 v[228:231], v254, s[56:59], s67 offen offset:1024 sc1\n"
    "v_mfma_f32_32x32x16_bf16 a[48:63], v[220:223], v[80:83], a[48:63]\n"
    "ds_read_b64_tr_b16 v[212:213], v176 offset:31232\n"
    "ds_read_b64_tr_b16 v[214:215], v176 offset:32256\n"
    "v_accvgpr_read_b32 v224, a16\n"
    "v_accvgpr_read_b32 v225, a17\n"
    "v_accvgpr_read_b32 v226, a18\n"
    "v_accvgpr_read_b32 v227, a19\n"
    "s_waitcnt lgkmcnt(8)\n"
    "v_mfma_f32_32x32x16_bf16 a[32:47], v[192:195], v[84:87], a[32:47]\n"
    "s_barrier\n"
    "ds_read_b64_tr_b16 v[216:217], v176 offset:32768\n"
    "ds_read_b64_tr_b16 v[218:219], v176 offset:33792\n"
    "v_accvgpr_read_b32 v228, a20\n"
    "v_accvgpr_read_b32 v229, a21\n"
    "v_accvgpr_read_b32 v230, a22\n"
    "v_accvgpr_read_b32 v231, a23\n"
    "v_mfma_f32_32x32x16_bf16 a[48:63], v[196:199], v[84:87], a[48:63]\n"
    "ds_read_b64_tr_b16 v[220:221], v176 offset:33280\n"
    "ds_read_b64_tr_b16 v[222:223], v176 offset:34304\n"
    "v_accvgpr_read_b32 v232, a24\n"
    "v_accvgpr_read_b32 v233, a25\n"
    "v_accvgpr_read_b32 v234, a26\n"
    "v_accvgpr_read_b32 v235, a27\n"
    "s_waitcnt lgkmcnt(8)\n"
    "v_mfma_f32_32x32x16_bf16 a[32:47], v[200:203], v[88:91], a[32:47]\n"
    "ds_read_b64_tr_b16 v[192:193], v176 offset:34816\n"
    "ds_read_b64_tr_b16 v[194:195], v176 offset:35840\n"
    "v_accvgpr_read_b32 v236, a28\n"
    "v_accvgpr_read_b32 v237, a29\n"
    "v_accvgpr_read_b32 v238, a30\n"
    "v_accvgpr_read_b32 v239, a31\n"
    "v_cvt_pk_bf16_f32 v224, v224, v225\n"
    "v_mfma_f32_32x32x16_bf16 a[48:63], v[204:207], v[88:91], a[48:63]\n"
    "ds_read_b64_tr_b16 v[196:197], v176 offset:35328\n"
    "ds_read_b64_tr_b16 v[198:199], v176 offset:36352\n"
    "v_cvt_pk_bf16_f32 v225, v226, v227\n"
    "v_cvt_pk_bf16_f32 v226, v228, v229\n"
    "v_cvt_pk_bf16_f32 v227, v230, v231\n"
    "v_cvt_pk_bf16_f32 v228, v232, v233\n"
    "s_waitcnt lgkmcnt(8)\n"
    "v_mfma_f32_32x32x16_bf16 a[32:47], v[208:211], v[92:95], a[32:47]\n"
    "ds_read_b64_tr_b16 v[200:201], v176 offset:36864\n"
    "ds_read_b64_tr_b16 v[202:203], v176 offset:37888\n"
    "v_cvt_pk_bf16_f32 v229, v234, v235\n"
    "v_cvt_pk_bf16_f32 v230, v236, v237\n"
    "v_cvt_pk_bf16_f32 v231, v238, v239\n"
    "s_add_u32 s67, s77, 2048\n"
    "buffer_store_dwordx4 v[224:227], v254, s[56:59], s67 offen sc1\n"
    "v_mfma_f32_32x32x16_bf16 a[48:63], v[212:215], v[92:95], a[48:63]\n"
    "ds_read_b64_tr_b16 v[204:205], v176 offset:37376\n"
    "ds_read_b64_tr_b16 v[206:207], v176 offset:38400\n"
    "buffer_store_dwordx4 v[228:231], v254, s[56:59], s67 offen offset:1024 sc1\n"
    "s_waitcnt lgkmcnt(8)\n"
    "v_mfma_f32_32x32x16_bf16 a[64:79], v[216:219], v[64:67], a[64:79]\n"
    "ds_read_b64_tr_b16 v[208:209], v176 offset:38912\n"
    "ds_read_b64_tr_b16 v[210:211], v176 offset:39936\n"
    "v_mfma_f32_32x32x16_bf16 a[80:95], v[220:223], v[64:67], a[80:95]\n"
    "ds_read_b64_tr_b16 v[212:213], v176 offset:39424\n"
    "ds_read_b64_tr_b16 v[214:215], v176 offset:40448\n"
    "s_waitcnt lgkmcnt(8)\n"
    "v_mfma_f32_32x32x16_bf16 a[64:79], v[192:195], v[68:71], a[64:79]\n"
    "ds_read_b64_tr_b16 v[216:217], v176 offset:40960\n"
    "ds_read_b64_tr_b16 v[218:219], v176 offset:41984\n"
    "v_mfma_f32_32x32x16_bf16 a[80:95], v[196:199], v[68:71], a[80:95]\n"
    "ds_read_b64_tr_b16 v[220:221], v176 offset:41472\n"
    "ds_read_b64_tr_b16 v[222:223], v176 offset:42496\n"
    "v_accvgpr_read_b32 v224, a32\n"
    "v_accvgpr_read_b32 v225, a33\n"
    "v_accvgpr_read_b32 v226, a34\n"
    "v_accvgpr_read_b32 v227, a35\n"
    "s_waitcnt lgkmcnt(8)\n"
    "v_mfma_f32_32x32x16_bf16 a[64:79], v[200:203], v[72:75], a[64:79]\n"
    "ds_read_b64_tr_b16 v[192:193], v176 offset:43008\n"
    "ds_read_b64_tr_b16 v[194:195], v176 offset:44032\n"
    "v_accvgpr_read_b32 v228, a36\n"
    "v_accvgpr_read_b32 v229, a37\n"
    "v_accvgpr_read_b32 v230, a38\n"
    "v_accvgpr_read_b32 v231, a39\n"
    "v_accvgpr_read_b32 v232, a40\n"
    "v_mfma_f32_32x32x16_bf16 a[80:95], v[204:207], v[72:75], a[80:95]\n"
    "ds_read_b64_tr_b16 v[196:197], v176 offset:43520\n"
    "ds_read_b64_tr_b16 v[198:199], v176 offset:44544\n"
    "v_accvgpr_read_b32 v233, a41\n"
    "v_accvgpr_read_b32 v234, a42\n"
    "v_accvgpr_read_b32 v235, a43\n"
    "v_accvgpr_read_b32 v236, a44\n"
    "s_waitcnt lgkmcnt(8)\n"
    "v_mfma_f32_32x32x16_bf16 a[64:79], v[208:211], v[76:79], a[64:79]\n"
    "ds_read_b64_tr_b16 v[200:201], v176 offset:45056\n"
    "ds_read_b64_tr_b16 v[202:203], v176 offset:46080\n"
    "v_accvgpr_read_b32 v237, a45\n"
    "v_accvgpr_read_b32 v238, a46\n"
    "v_accvgpr_read_b32 v239, a47\n"
    "v_cvt_pk_bf16_f32 v224, v224, v225\n"
    "v_cvt_pk_bf16_f32 v225, v226, v227\n"
    "v_mfma_f32_32x32x16_bf16 a[80:95], v[212:215], v[76:79], a[80:95]\n"
    "ds_read_b64_tr_b16 v[204:205], v176 offset:45568\n"
    "ds_read_b64_tr_b16 v[206:207], v176 offset:46592\n"
    "v_cvt_pk_bf16_f32 v226, v228, v229\n"
    "v_cvt_pk_bf16_f32 v227, v230, v231\n"
    "v_cvt_pk_bf16_f32 v228, v232, v233\n"
    "v_cvt_pk_bf16_f32 v229, v234, v235\n"
    "s_waitcnt lgkmcnt(8)\n"
    "v_mfma_f32_32x32x16_bf16 a[64:79], v[216:219], v[80:83], a[64:79]\n"
    "ds_read_b64_tr_b16 v[208:209], v176 offset:47104\n"
    "ds_read_b64_tr_b16 v[210:211], v176 offset:48128\n"
    "v_cvt_pk_bf16_f32 v230, v236, v237\n"
    "v_cvt_pk_bf16_f32 v231, v238, v239\n"
    "s_add_u32 s67, s77, 4096\n"
    "buffer_store_dwordx4 v[224:227], v254, s[56:59], s67 offen sc1\n"
    "buffer_store_dwordx4 v[228:231], v254, s[56:59], s67 offen offset:1024 sc1\n"
    "v_mfma_f32_32x32x16_bf16 a[80:95], v[220:223], v[80:83], a[80:95]\n"
    "ds_read_b64_tr_b16 v[212:213], v176 offset:47616\n"
    "ds_read_b64_tr_b16 v[214:215], v176 offset:48640\n"
    "v_accvgpr_read_b32 v224, a48\n"
    "v_accvgpr_read_b32 v225, a49\n"
    "v_accvgpr_read_b32 v226, a50\n"
    "v_accvgpr_read_b32 v227, a51\n"
    "s_waitcnt lgkmcnt(8)\n"
    "v_mfma_f32_32x32x16_bf16 a[64:79], v[192:195], v[84:87], a[64:79]\n"
    "s_barrier\n"
    "ds_read_b64_tr_b16 v[216:217], v176 offset:49152\n"
    "ds_read_b64_tr_b16 v[218:219], v176 offset:50176\n"
    "v_accvgpr_read_b32 v228, a52\n"
    "v_accvgpr_read_b32 v229, a53\n"
    "v_accvgpr_read_b32 v230, a54\n"
    "v_accvgpr_read_b32 v231, a55\n"
    "v_mfma_f32_32x32x16_bf16 a[80:95], v[196:199], v[84:87], a[80:95]\n"
    "ds_read_b64_tr_b16 v[220:221], v176 offset:49664\n"
    "ds_read_b64_tr_b16 v[222:223], v176 offset:50688\n"
    "v_accvgpr_read_b32 v232, a56\n"
    "v_accvgpr_read_b32 v233, a57\n"
    "v_accvgpr_read_b32 v234, a58\n"
    "v_accvgpr_read_b32 v235, a59\n"
    "s_waitcnt lgkmcnt(8)\n"
    "v_mfma_f32_32x32x16_bf16 a[64:79], v[200:203], v[88:91], a[64:79]\n"
    "ds_read_b64_tr_b16 v[192:193], v176 offset:51200\n"
    "ds_read_b64_tr_b16 v[194:195], v176 offset:52224\n"
    "v_accvgpr_read_b32 v236, a60\n"
    "v_accvgpr_read_b32 v237, a61\n"
    "v_accvgpr_read_b32 v238, a62\n"
    "v_accvgpr_read_b32 v239, a63\n"
    "v_cvt_pk_bf16_f32 v224, v224, v225\n"
    "v_mfma_f32_32x32x16_bf16 a[80:95], v[204:207], v[88:91], a[80:95]\n"
    "ds_read_b64_tr_b16 v[196:197], v176 offset:51712\n"
    "ds_read_b64_tr_b16 v[198:199], v176 offset:52736\n"
    "v_cvt_pk_bf16_f32 v225, v226, v227\n"
    "v_cvt_pk_bf16_f32 v226, v228, v229\n"
    "v_cvt_pk_bf16_f32 v227, v230, v231\n"
    "v_cvt_pk_bf16_f32 v228, v232, v233\n"
    "s_waitcnt lgkmcnt(8)\n"
    "v_mfma_f32_32x32x16_bf16 a[64:79], v[208:211], v[92:95], a[64:79]\n"
    "ds_read_b64_tr_b16 v[200:201], v176 offset:53248\n"
    "ds_read_b64_tr_b16 v[202:203], v176 offset:54272\n"
    "v_cvt_pk_bf16_f32 v229, v234, v235\n"
    "v_cvt_pk_bf16_f32 v230, v236, v237\n"
    "v_cvt_pk_bf16_f32 v231, v238, v239\n"
    "s_add_u32 s67, s77, 6144\n"
    "buffer_store_dwordx4 v[224:227], v254, s[56:59], s67 offen sc1\n"
    "v_mfma_f32_32x32x16_bf16 a[80:95], v[212:215], v[92:95], a[80:95]\n"
    "ds_read_b64_tr_b16 v[204:205], v176 offset:53760\n"
    "ds_read_b64_tr_b16 v[206:207], v176 offset:54784\n"
    "buffer_store_dwordx4 v[228:231], v254, s[56:59], s67 offen offset:1024 sc1\n"
    "s_waitcnt lgkmcnt(8)\n"
    "v_mfma_f32_32x32x16_bf16 a[96:111], v[216:219], v[64:67], a[96:111]\n"
    "ds_read_b64_tr_b16 v[208:209], v176 offset:55296\n"
    "ds_read_b64_tr_b16 v[210:211], v176 offset:56320\n"
    "v_mfma_f32_32x32x16_bf16 a[112:127], v[220:223], v[64:67], a[112:127]\n"
    "ds_read_b64_tr_b16 v[212:213], v176 offset:55808\n"
    "ds_read_b64_tr_b16 v[214:215], v176 offset:56832\n"
    "s_waitcnt lgkmcnt(8)\n"
    "v_mfma_f32_32x32x16_bf16 a[96:111], v[192:195], v[68:71], a[96:111]\n"
    "ds_read_b64_tr_b16 v[216:217], v176 offset:57344\n"
    "ds_read_b64_tr_b16 v[218:219], v176 offset:58368\n"
    "v_mfma_f32_32x32x16_bf16 a[112:127], v[196:199], v[68:71], a[112:127]\n"
    "ds_read_b64_tr_b16 v[220:221], v176 offset:57856\n"
    "ds_read_b64_tr_b16 v[222:223], v176 offset:58880\n"
    "v_accvgpr_read_b32 v224, a64\n"
    "v_accvgpr_read_b32 v225, a65\n"
    "v_accvgpr_read_b32 v226, a66\n"
    "v_accvgpr_read_b32 v227, a67\n"
    "s_waitcnt lgkmcnt(8)\n"
    "v_mfma_f32_32x32x16_bf16 a[96:111], v[200:203], v[72:75], a[96:111]\n"
    "ds_read_b64_tr_b16 v[192:193], v176 offset:59392\n"
    "ds_read_b64_tr_b16 v[194:195], v176 offset:60416\n"
    "v_accvgpr_read_b32 v228, a68\n"
    "v_accvgpr_read_b32 v229, a69\n"
    "v_accvgpr_read_b32 v230, a70\n"
    "v_accvgpr_read_b32 v231, a71\n"
    "v_accvgpr_read_b32 v232, a72\n"
    "v_mfma_f32_32x32x16_bf16 a[112:127], v[204:207], v[72:75], a[112:127]\n"
    "ds_read_b64_tr_b16 v[196:197], v176 offset:59904\n"
    "ds_read_b64_tr_b16 v[198:199], v176 offset:60928\n"
    "v_accvgpr_read_b32 v233, a73\n"
    "v_accvgpr_read_b32 v234, a74\n"
    "v_accvgpr_read_b32 v235, a75\n"
    "v_accvgpr_read_b32 v236, a76\n"
    "s_waitcnt lgkmcnt(8)\n"
    "v_mfma_f32_32x32x16_bf16 a[96:111], v[208:211], v[76:79], a[96:111]\n"
    "ds_read_b64_tr_b16 v[200:201], v176 offset:61440\n"
    "ds_read_b64_tr_b16 v[202:203], v176 offset:62464\n"
    "v_accvgpr_read_b32 v237, a77\n"
    "v_accvgpr_read_b32 v238, a78\n"
    "v_accvgpr_read_b32 v239, a79\n"
    "v_cvt_pk_bf16_f32 v224, v224, v225\n"
    "v_cvt_pk_bf16_f32 v225, v226, v227\n"
    "v_mfma_f32_32x32x16_bf16 a[112:127], v[212:215], v[76:79], a[112:127]\n"
    "ds_read_b64_tr_b16 v[204:205], v176 offset:61952\n"
    "ds_read_b64_tr_b16 v[206:207], v176 offset:62976\n"
    "v_cvt_pk_bf16_f32 v226, v228, v229\n"
    "v_cvt_pk_bf16_f32 v227, v230, v231\n"
    "v_cvt_pk_bf16_f32 v228, v232, v233\n"
    "v_cvt_pk_bf16_f32 v229, v234, v235\n"
    "s_waitcnt lgkmcnt(8)\n"
    "v_mfma_f32_32x32x16_bf16 a[96:111], v[216:219], v[80:83], a[96:111]\n"
    "ds_read_b64_tr_b16 v[208:209], v176 offset:63488\n"
    "ds_read_b64_tr_b16 v[210:211], v176 offset:64512\n"
    "v_cvt_pk_bf16_f32 v230, v236, v237\n"
    "v_cvt_pk_bf16_f32 v231, v238, v239\n"
    "s_add_u32 s67, s77, 8192\n"
    "buffer_store_dwordx4 v[224:227], v254, s[56:59], s67 offen sc1\n"
    "buffer_store_dwordx4 v[228:231], v254, s[56:59], s67 offen offset:1024 sc1\n"
    "v_mfma_f32_32x32x16_bf16 a[112:127], v[220:223], v[80:83], a[112:127]\n"
    "ds_read_b64_tr_b16 v[212:213], v176 offset:64000\n"
    "ds_read_b64_tr_b16 v[214:215], v176 offset:65024\n"
    "v_accvgpr_read_b32 v224, a80\n"
    "v_accvgpr_read_b32 v225, a81\n"
    "v_accvgpr_read_b32 v226, a82\n"
    "v_accvgpr_read_b32 v227, a83\n"
    "s_waitcnt lgkmcnt(8)\n"
    "v_mfma_f32_32x32x16_bf16 a[96:111], v[192:195], v[84:87], a[96:111]\n"
    "s_waitcnt vmcnt(22)\n"
    "s_barrier\n"
    "ds_read_b64_tr_b16 v[216:217], v242 offset:0\n"
    "ds_read_b64_tr_b16 v[218:219], v242 offset:1024\n"
    "v_accvgpr_read_b32 v228, a84\n"
    "v_accvgpr_read_b32 v229, a85\n"
    "v_accvgpr_read_b32 v230, a86\n"
    "v_accvgpr_read_b32 v231, a87\n"
    "v_mfma_f32_32x32x16_bf16 a[112:127], v[196:199], v[84:87], a[112:127]\n"
    "ds_read_b64_tr_b16 v[220:221], v242 offset:512\n"
    "ds_read_b64_tr_b16 v[222:223], v242 offset:1536\n"
    "v_accvgpr_read_b32 v232, a88\n"
    "v_accvgpr_read_b32 v233, a89\n"
    "v_accvgpr_read_b32 v234, a90\n"
    "v_accvgpr_read_b32 v235, a91\n"
    "s_waitcnt lgkmcnt(8)\n"
    "v_mfma_f32_32x32x16_bf16 a[96:111], v[200:203], v[88:91], a[96:111]\n"
    "ds_read_b64_tr_b16 v[192:193], v242 offset:2048\n"
    "ds_read_b64_tr_b16 v[194:195], v242 offset:3072\n"
    "v_accvgpr_read_b32 v236, a92\n"
    "v_accvgpr_read_b32 v237, a93\n"
    "v_accvgpr_read_b32 v238, a94\n"
    "v_accvgpr_read_b32 v239, a95\n"
    "v_cvt_pk_bf16_f32 v224, v224, v225\n"
    "v_mfma_f32_32x32x16_bf16 a[112:127], v[204:207], v[88:91], a[112:127]\n"
    "ds_read_b64_tr_b16 v[196:197], v242 offset:2560\n"
    "ds_read_b64_tr_b16 v[198:199], v242 offset:3584\n"
    "v_cvt_pk_bf16_f32 v225, v226, v227\n"
    "v_cvt_pk_bf16_f32 v226, v228, v229\n"
    "v_cvt_pk_bf16_f32 v227, v230, v231\n"
    "v_cvt_pk_bf16_f32 v228, v232, v233\n"
    "s_waitcnt lgkmcnt(8)\n"
    "v_mfma_f32_32x32x16_bf16 a[96:111], v[208:211], v[92:95], a[96:111]\n"
    "ds_read_b64_tr_b16 v[200:201], v242 offset:4096\n"
    "ds_read_b64_tr_b16 v[202:203], v242 offset:5120\n"
    "v_cvt_pk_bf16_f32 v229, v234, v235\n"
    "v_cvt_pk_bf16_f32 v230, v236, v237\n"
    "v_cvt_pk_bf16_f32 v231, v238, v239\n"
    "s_add_u32 s67, s77, 10240\n"
    "buffer_store_dwordx4 v[224:227], v254, s[56:59], s67 offen sc1\n"
    "v_mfma_f32_32x32x16_bf16 a[112:127], v[212:215], v[92:95], a[112:127]\n"
    "ds_read_b64_tr_b16 v[204:205], v242 offset:4608\n"
    "ds_read_b64_tr_b16 v[206:207], v242 offset:5632\n"
    "buffer_store_dwordx4 v[228:231], v254, s[56:59], s67 offen offset:1024 sc1\n"
    "s_waitcnt lgkmcnt(8)\n"
    "v_mfma_f32_32x32x16_bf16 a[128:143], v[216:219], v[64:67], a[128:143]\n"
    "ds_read_b64_tr_b16 v[208:209], v242 offset:6144\n"
    "ds_read_b64_tr_b16 v[210:211], v242 offset:7168\n"
    "v_mfma_f32_32x32x16_bf16 a[144:159], v[220:223], v[64:67], a[144:159]\n"
    "ds_read_b64_tr_b16 v[212:213], v242 offset:6656\n"
    "ds_read_b64_tr_b16 v[214:215], v242 offset:7680\n"
    "s_waitcnt lgkmcnt(8)\n"
    "v_mfma_f32_32x32x16_bf16 a[128:143], v[192:195], v[68:71], a[128:143]\n"
    "ds_read_b64_tr_b16 v[216:217], v242 offset:8192\n"
    "ds_read_b64_tr_b16 v[218:219], v242 offset:9216\n"
    "v_mfma_f32_32x32x16_bf16 a[144:159], v[196:199], v[68:71], a[144:159]\n"
    "ds_read_b64_tr_b16 v[220:221], v242 offset:8704\n"
    "ds_read_b64_tr_b16 v[222:223], v242 offset:9728\n"
    "v_accvgpr_read_b32 v224, a96\n"
    "v_accvgpr_read_b32 v225, a97\n"
    "v_accvgpr_read_b32 v226, a98\n"
    "v_accvgpr_read_b32 v227, a99\n"
    "s_waitcnt lgkmcnt(8)\n"
    "v_mfma_f32_32x32x16_bf16 a[128:143], v[200:203], v[72:75], a[128:143]\n"
    "ds_read_b64_tr_b16 v[192:193], v242 offset:10240\n"
    "ds_read_b64_tr_b16 v[194:195], v242 offset:11264\n"
    "v_accvgpr_read_b32 v228, a100\n"
    "v_accvgpr_read_b32 v229, a101\n"
    "v_accvgpr_read_b32 v230, a102\n"
    "v_accvgpr_read_b32 v231, a103\n"
    "v_accvgpr_read_b32 v232, a104\n"
    "v_mfma_f32_32x32x16_bf16 a[144:159], v[204:207], v[72:75], a[144:159]\n"
    "ds_read_b64_tr_b16 v[196:197], v242 offset:10752\n"
    "ds_read_b64_tr_b16 v[198:199], v242 offset:11776\n"
    "v_accvgpr_read_b32 v233, a105\n"
    "v_accvgpr_read_b32 v234, a106\n"
    "v_accvgpr_read_b32 v235, a107\n"
    "v_accvgpr_read_b32 v236, a108\n"
    "s_waitcnt lgkmcnt(8)\n"
    "v_mfma_f32_32x32x16_bf16 a[128:143], v[208:211], v[76:79], a[128:143]\n"
    "ds_read_b64_tr_b16 v[200:201], v242 offset:12288\n"
    "ds_read_b64_tr_b16 v[202:203], v242 offset:13312\n"
    "v_accvgpr_read_b32 v237, a109\n"
    "v_accvgpr_read_b32 v238, a110\n"
    "v_accvgpr_read_b32 v239, a111\n"
    "v_cvt_pk_bf16_f32 v224, v224, v225\n"
    "v_cvt_pk_bf16_f32 v225, v226, v227\n"
    "v_mfma_f32_32x32x16_bf16 a[144:159], v[212:215], v[76:79], a[144:159]\n"
    "ds_read_b64_tr_b16 v[204:205], v242 offset:12800\n"
    "ds_read_b64_tr_b16 v[206:207], v242 offset:13824\n"
    "v_cvt_pk_bf16_f32 v226, v228, v229\n"
    "v_cvt_pk_bf16_f32 v227, v230, v231\n"
    "v_cvt_pk_bf16_f32 v228, v232, v233\n"
    "v_cvt_pk_bf16_f32 v229, v234, v235\n"
    "s_waitcnt lgkmcnt(8)\n"
    "v_mfma_f32_32x32x16_bf16 a[128:143], v[216:219], v[80:83], a[128:143]\n"
    "ds_read_b64_tr_b16 v[208:209], v242 offset:14336\n"
    "ds_read_b64_tr_b16 v[210:211], v242 offset:15360\n"
    "v_cvt_pk_bf16_f32 v230, v236, v237\n"
    "v_cvt_pk_bf16_f32 v231, v238, v239\n"
    "s_add_u32 s67, s77, 12288\n"
    "buffer_store_dwordx4 v[224:227], v254, s[56:59], s67 offen sc1\n"
    "buffer_store_dwordx4 v[228:231], v254, s[56:59], s67 offen offset:1024 sc1\n"
    "v_mfma_f32_32x32x16_bf16 a[144:159], v[220:223], v[80:83], a[144:159]\n"
    "ds_read_b64_tr_b16 v[212:213], v242 offset:14848\n"
    "ds_read_b64_tr_b16 v[214:215], v242 offset:15872\n"
    "v_accvgpr_read_b32 v224, a112\n"
    "v_accvgpr_read_b32 v225, a113\n"
    "v_accvgpr_read_b32 v226, a114\n"
    "v_accvgpr_read_b32 v227, a115\n"
    "s_waitcnt lgkmcnt(8)\n"
    "v_mfma_f32_32x32x16_bf16 a[128:143], v[192:195], v[84:87], a[128:143]\n"
    "s_waitcnt vmcnt(22)\n"
    "s_barrier\n"
    "ds_read_b64_tr_b16 v[216:217], v242 offset:32768\n"
    "ds_read_b64_tr_b16 v[218:219], v242 offset:33792\n"
    "v_accvgpr_read_b32 v228, a116\n"
    "v_accvgpr_read_b32 v229, a117\n"
    "v_accvgpr_read_b32 v230, a118\n"
    "v_accvgpr_read_b32 v231, a119\n"
    "v_mfma_f32_32x32x16_bf16 a[144:159], v[196:199], v[84:87], a[144:159]\n"
    "ds_read_b64_tr_b16 v[220:221], v242 offset:33280\n"
    "ds_read_b64_tr_b16 v[222:223], v242 offset:34304\n"
    "v_accvgpr_read_b32 v232, a120\n"
    "v_accvgpr_read_b32 v233, a121\n"
    "v_accvgpr_read_b32 v234, a122\n"
    "v_accvgpr_read_b32 v235, a123\n"
    "s_waitcnt lgkmcnt(8)\n"
    "v_mfma_f32_32x32x16_bf16 a[128:143], v[200:203], v[88:91], a[128:143]\n"
    "ds_read_b64_tr_b16 v[192:193], v242 offset:34816\n"
    "ds_read_b64_tr_b16 v[194:195], v242 offset:35840\n"
    "v_accvgpr_read_b32 v236, a124\n"
    "v_accvgpr_read_b32 v237, a125\n"
    "v_accvgpr_read_b32 v238, a126\n"
    "v_accvgpr_read_b32 v239, a127\n"
    "v_cvt_pk_bf16_f32 v224, v224, v225\n"
    "v_mfma_f32_32x32x16_bf16 a[144:159], v[204:207], v[88:91], a[144:159]\n"
    "ds_read_b64_tr_b16 v[196:197], v242 offset:35328\n"
    "ds_read_b64_tr_b16 v[198:199], v242 offset:36352\n"
    "v_cvt_pk_bf16_f32 v225, v226, v227\n"
    "v_cvt_pk_bf16_f32 v226, v228, v229\n"
    "v_cvt_pk_bf16_f32 v227, v230, v231\n"
    "v_cvt_pk_bf16_f32 v228, v232, v233\n"
    "s_waitcnt lgkmcnt(8)\n"
    "v_mfma_f32_32x32x16_bf16 a[128:143], v[208:211], v[92:95], a[128:143]\n"
    "ds_read_b64_tr_b16 v[200:201], v242 offset:36864\n"
    "ds_read_b64_tr_b16 v[202:203], v242 offset:37888\n"
    "v_cvt_pk_bf16_f32 v229, v234, v235\n"
    "v_cvt_pk_bf16_f32 v230, v236, v237\n"
    "v_cvt_pk_bf16_f32 v231, v238, v239\n"
    "s_add_u32 s67, s77, 14336\n"
    "buffer_store_dwordx4 v[224:227], v254, s[56:59], s67 offen sc1\n"
    "v_mfma_f32_32x32x16_bf16 a[144:159], v[212:215], v[92:95], a[144:159]\n"
    "ds_read_b64_tr_b16 v[204:205], v242 offset:37376\n"
    "ds_read_b64_tr_b16 v[206:207], v242 offset:38400\n"
    "buffer_store_dwordx4 v[228:231], v254, s[56:59], s67 offen offset:1024 sc1\n"
    "s_waitcnt lgkmcnt(8)\n"
    "v_mfma_f32_32x32x16_bf16 a[160:175], v[216:219], v[64:67], a[160:175]\n"
    "ds_read_b64_tr_b16 v[208:209], v242 offset:38912\n"
    "ds_read_b64_tr_b16 v[210:211], v242 offset:39936\n"
    "v_mfma_f32_32x32x16_bf16 a[176:191], v[220:223], v[64:67], a[176:191]\n"
    "ds_read_b64_tr_b16 v[212:213], v242 offset:39424\n"
    "ds_read_b64_tr_b16 v[214:215], v242 offset:40448\n"
    "s_waitcnt lgkmcnt(8)\n"
    "v_mfma_f32_32x32x16_bf16 a[160:175], v[192:195], v[68:71], a[160:175]\n"
    "ds_read_b64_tr_b16 v[216:217], v242 offset:40960\n"
    "ds_read_b64_tr_b16 v[218:219], v242 offset:41984\n"
    "v_mfma_f32_32x32x16_bf16 a[176:191], v[196:199], v[68:71], a[176:191]\n"
    "ds_read_b64_tr_b16 v[220:221], v242 offset:41472\n"
    "ds_read_b64_tr_b16 v[222:223], v242 offset:42496\n"
    "v_accvgpr_read_b32 v224, a128\n"
    "v_accvgpr_read_b32 v225, a129\n"
    "v_accvgpr_read_b32 v226, a130\n"
    "v_accvgpr_read_b32 v227, a131\n"
    "s_waitcnt lgkmcnt(8)\n"
    "v_mfma_f32_32x32x16_bf16 a[160:175], v[200:203], v[72:75], a[160:175]\n"
    "ds_read_b64_tr_b16 v[192:193], v242 offset:43008\n"
    "ds_read_b64_tr_b16 v[194:195], v242 offset:44032\n"
    "v_accvgpr_read_b32 v228, a132\n"
    "v_accvgpr_read_b32 v229, a133\n"
    "v_accvgpr_read_b32 v230, a134\n"
    "v_accvgpr_read_b32 v231, a135\n"
    "v_accvgpr_read_b32 v232, a136\n"
    "v_mfma_f32_32x32x16_bf16 a[176:191], v[204:207], v[72:75], a[176:191]\n"
    "ds_read_b64_tr_b16 v[196:197], v242 offset:43520\n"
    "ds_read_b64_tr_b16 v[198:199], v242 offset:44544\n"
    "v_accvgpr_read_b32 v233, a137\n"
    "v_accvgpr_read_b32 v234, a138\n"
    "v_accvgpr_read_b32 v235, a139\n"
    "v_accvgpr_read_b32 v236, a140\n"
    "s_waitcnt lgkmcnt(8)\n"
    "v_mfma_f32_32x32x16_bf16 a[160:175], v[208:211], v[76:79], a[160:175]\n"
    "ds_read_b64_tr_b16 v[200:201], v242 offset:45056\n"
    "ds_read_b64_tr_b16 v[202:203], v242 offset:46080\n"
    "v_accvgpr_read_b32 v237, a141\n"
    "v_accvgpr_read_b32 v238, a142\n"
    "v_accvgpr_read_b32 v239, a143\n"
    "v_cvt_pk_bf16_f32 v224, v224, v225\n"
    "v_cvt_pk_bf16_f32 v225, v226, v227\n"
    "v_mfma_f32_32x32x16_bf16 a[176:191], v[212:215], v[76:79], a[176:191]\n"
    "ds_read_b64_tr_b16 v[204:205], v242 offset:45568\n"
    "ds_read_b64_tr_b16 v[206:207], v242 offset:46592\n"
    "v_cvt_pk_bf16_f32 v226, v228, v229\n"
    "v_cvt_pk_bf16_f32 v227, v230, v231\n"
    "v_cvt_pk_bf16_f32 v228, v232, v233\n"
    "v_cvt_pk_bf16_f32 v229, v234, v235\n"
    "s_waitcnt lgkmcnt(8)\n"
    "v_mfma_f32_32x32x16_bf16 a[160:175], v[216:219], v[80:83], a[160:175]\n"
    "ds_read_b64_tr_b16 v[208:209], v242 offset:47104\n"
    "ds_read_b64_tr_b16 v[210:211], v242 offset:48128\n"
    "v_cvt_pk_bf16_f32 v230, v236, v237\n"
    "v_cvt_pk_bf16_f32 v231, v238, v239\n"
    "s_add_u32 s67, s77, 16384\n"
    "buffer_store_dwordx4 v[224:227], v254, s[56:59], s67 offen sc1\n"
    "buffer_store_dwordx4 v[228:231], v254, s[56:59], s67 offen offset:1024 sc1\n"
    "v_mfma_f32_32x32x16_bf16 a[176:191], v[220:223], v[80:83], a[176:191]\n"
    "ds_read_b64_tr_b16 v[212:213], v242 offset:47616\n"
    "ds_read_b64_tr_b16 v[214:215], v242 offset:48640\n"
    "v_accvgpr_read_b32 v224, a144\n"
    "v_accvgpr_read_b32 v225, a145\n"
    "v_accvgpr_read_b32 v226, a146\n"
    "v_accvgpr_read_b32 v227, a147\n"
    "s_waitcnt lgkmcnt(8)\n"
    "v_mfma_f32_32x32x16_bf16 a[160:175], v[192:195], v[84:87], a[160:175]\n"
    "s_waitcnt vmcnt(22)\n"
    "s_barrier\n"
    "ds_read_b64_tr_b16 v[216:217], v242 offset:49152\n"
    "ds_read_b64_tr_b16 v[218:219], v242 offset:50176\n"
    "v_accvgpr_read_b32 v228, a148\n"
    "v_accvgpr_read_b32 v229, a149\n"
    "v_accvgpr_read_b32 v230, a150\n"
    "v_accvgpr_read_b32 v231, a151\n"
    "v_mfma_f32_32x32x16_bf16 a[176:191], v[196:199], v[84:87], a[176:191]\n"
    "ds_read_b64_tr_b16 v[220:221], v242 offset:49664\n"
    "ds_read_b64_tr_b16 v[222:223], v242 offset:50688\n"
    "v_accvgpr_read_b32 v232, a152\n"
    "v_accvgpr_read_b32 v233, a153\n"
    "v_accvgpr_read_b32 v234, a154\n"
    "v_accvgpr_read_b32 v235, a155\n"
    "s_waitcnt lgkmcnt(8)\n"
    "v_mfma_f32_32x32x16_bf16 a[160:175], v[200:203], v[88:91], a[160:175]\n"
    "ds_read_b64_tr_b16 v[192:193], v242 offset:51200\n"
    "ds_read_b64_tr_b16 v[194:195], v242 offset:52224\n"
    "v_accvgpr_read_b32 v236, a156\n"
    "v_accvgpr_read_b32 v237, a157\n"
    "v_accvgpr_read_b32 v238, a158\n"
    "v_accvgpr_read_b32 v239, a159\n"
    "v_cvt_pk_bf16_f32 v224, v224, v225\n"
    "v_mfma_f32_32x32x16_bf16 a[176:191], v[204:207], v[88:91], a[176:191]\n"
    "ds_read_b64_tr_b16 v[196:197], v242 offset:51712\n"
    "ds_read_b64_tr_b16 v[198:199], v242 offset:52736\n"
    "v_cvt_pk_bf16_f32 v225, v226, v227\n"
    "v_cvt_pk_bf16_f32 v226, v228, v229\n"
    "v_cvt_pk_bf16_f32 v227, v230, v231\n"
    "v_cvt_pk_bf16_f32 v228, v232, v233\n"
    "s_waitcnt lgkmcnt(8)\n"
    "v_mfma_f32_32x32x16_bf16 a[160:175], v[208:211], v[92:95], a[160:175]\n"
    "ds_read_b64_tr_b16 v[200:201], v242 offset:53248\n"
    "ds_read_b64_tr_b16 v[202:203], v242 offset:54272\n"
    "v_cvt_pk_bf16_f32 v229, v234, v235\n"
    "v_cvt_pk_bf16_f32 v230, v236, v237\n"
    "v_cvt_pk_bf16_f32 v231, v238, v239\n"
    "s_add_u32 s67, s77, 18432\n"
    "buffer_store_dwordx4 v[224:227], v254, s[56:59], s67 offen sc1\n"
    "v_mfma_f32_32x32x16_bf16 a[176:191], v[212:215], v[92:95], a[176:191]\n"
    "ds_read_b64_tr_b16 v[204:205], v242 offset:53760\n"
    "ds_read_b64_tr_b16 v[206:207], v242 offset:54784\n"
    "buffer_store_dwordx4 v[228:231], v254, s[56:59], s67 offen offset:1024 sc1\n"
    "s_waitcnt lgkmcnt(8)\n"
    "v_mfma_f32_32x32x16_bf16 a[192:207], v[216:219], v[64:67], a[192:207]\n"
    "ds_read_b64_tr_b16 v[208:209], v242 offset:55296\n"
    "ds_read_b64_tr_b16 v[210:211], v242 offset:56320\n"
    "v_mfma_f32_32x32x16_bf16 a[208:223], v[220:223], v[64:67], a[208:223]\n"
    "ds_read_b64_tr_b16 v[212:213], v242 offset:55808\n"
    "ds_read_b64_tr_b16 v[214:215], v242 offset:56832\n"
    "s_waitcnt lgkmcnt(8)\n"
    "v_mfma_f32_32x32x16_bf16 a[192:207], v[192:195], v[68:71], a[192:207]\n"
    "ds_read_b64_tr_b16 v[216:217], v242 offset:57344\n"
    "ds_read_b64_tr_b16 v[218:219], v242 offset:58368\n"
    "v_mfma_f32_32x32x16_bf16 a[208:223], v[196:199], v[68:71], a[208:223]\n"
    "ds_read_b64_tr_b16 v[220:221], v242 offset:57856\n"
    "ds_read_b64_tr_b16 v[222:223], v242 offset:58880\n"
    "v_accvgpr_read_b32 v224, a160\n"
    "v_accvgpr_read_b32 v225, a161\n"
    "v_accvgpr_read_b32 v226, a162\n"
    "v_accvgpr_read_b32 v227, a163\n"
    "s_waitcnt lgkmcnt(8)\n"
    "v_mfma_f32_32x32x16_bf16 a[192:207], v[200:203], v[72:75], a[192:207]\n"
    "ds_read_b64_tr_b16 v[192:193], v242 offset:59392\n"
    "ds_read_b64_tr_b16 v[194:195], v242 offset:60416\n"
    "v_accvgpr_read_b32 v228, a164\n"
    "v_accvgpr_read_b32 v229, a165\n"
    "v_accvgpr_read_b32 v230, a166\n"
    "v_accvgpr_read_b32 v231, a167\n"
    "v_accvgpr_read_b32 v232, a168\n"
    "v_mfma_f32_32x32x16_bf16 a[208:223], v[204:207], v[72:75], a[208:223]\n"
    "ds_read_b64_tr_b16 v[196:197], v242 offset:59904\n"
    "ds_read_b64_tr_b16 v[198:199], v242 offset:60928\n"
    "v_accvgpr_read_b32 v233, a169\n"
    "v_accvgpr_read_b32 v234, a170\n"
    "v_accvgpr_read_b32 v235, a171\n"
    "v_accvgpr_read_b32 v236, a172\n"
    "s_waitcnt lgkmcnt(8)\n"
    "v_mfma_f32_32x32x16_bf16 a[192:207], v[208:211], v[76:79], a[192:207]\n"
    "ds_read_b64_tr_b16 v[200:201], v242 offset:61440\n"
    "ds_read_b64_tr_b16 v[202:203], v242 offset:62464\n"
    "v_accvgpr_read_b32 v237, a173\n"
    "v_accvgpr_read_b32 v238, a174\n"
    "v_accvgpr_read_b32 v239, a175\n"
    "v_cvt_pk_bf16_f32 v224, v224, v225\n"
    "v_cvt_pk_bf16_f32 v225, v226, v227\n"
    "v_mfma_f32_32x32x16_bf16 a[208:223], v[212:215], v[76:79], a[208:223]\n"
    "ds_read_b64_tr_b16 v[204:205], v242 offset:61952\n"
    "ds_read_b64_tr_b16 v[206:207], v242 offset:62976\n"
    "v_cvt_pk_bf16_f32 v226, v228, v229\n"
    "v_cvt_pk_bf16_f32 v227, v230, v231\n"
    "v_cvt_pk_bf16_f32 v228, v232, v233\n"
    "v_cvt_pk_bf16_f32 v229, v234, v235\n"
    "s_waitcnt lgkmcnt(8)\n"
    "v_mfma_f32_32x32x16_bf16 a[192:207], v[216:219], v[80:83], a[192:207]\n"
    "ds_read_b64_tr_b16 v[208:209], v242 offset:63488\n"
    "ds_read_b64_tr_b16 v[210:211], v242 offset:64512\n"
    "v_cvt_pk_bf16_f32 v230, v236, v237\n"
    "v_cvt_pk_bf16_f32 v231, v238, v239\n"
    "s_add_u32 s67, s77, 20480\n"
    "buffer_store_dwordx4 v[224:227], v254, s[56:59], s67 offen sc1\n"
    "buffer_store_dwordx4 v[228:231], v254, s[56:59], s67 offen offset:1024 sc1\n"
    "v_mfma_f32_32x32x16_bf16 a[208:223], v[220:223], v[80:83], a[208:223]\n"
    "ds_read_b64_tr_b16 v[212:213], v242 offset:64000\n"
    "ds_read_b64_tr_b16 v[214:215], v242 offset:65024\n"
    "v_accvgpr_read_b32 v224, a176\n"
    "v_accvgpr_read_b32 v225, a177\n"
    "v_accvgpr_read_b32 v226, a178\n"
    "v_accvgpr_read_b32 v227, a179\n"
    "s_waitcnt lgkmcnt(8)\n"
    "v_mfma_f32_32x32x16_bf16 a[192:207], v[192:195], v[84:87], a[192:207]\n"
    "s_waitcnt vmcnt(22)\n"
    "s_barrier\n"
    "ds_read_b64_tr_b16 v[216:217], v242 offset:16384\n"
    "ds_read_b64_tr_b16 v[218:219], v242 offset:17408\n"
    "v_accvgpr_read_b32 v228, a180\n"
    "v_accvgpr_read_b32 v229, a181\n"
    "v_accvgpr_read_b32 v230, a182\n"
    "v_accvgpr_read_b32 v231, a183\n"
    "v_mfma_f32_32x32x16_bf16 a[208:223], v[196:199], v[84:87], a[208:223]\n"
    "ds_read_b64_tr_b16 v[220:221], v242 offset:16896\n"
    "ds_read_b64_tr_b16 v[222:223], v242 offset:17920\n"
    "v_accvgpr_read_b32 v232, a184\n"
    "v_accvgpr_read_b32 v233, a185\n"
    "v_accvgpr_read_b32 v234, a186\n"
    "v_accvgpr_read_b32 v235, a187\n"
    "s_waitcnt lgkmcnt(8)\n"
    "v_mfma_f32_32x32x16_bf16 a[192:207], v[200:203], v[88:91], a[192:207]\n"
    "ds_read_b64_tr_b16 v[192:193], v242 offset:18432\n"
    "ds_read_b64_tr_b16 v[194:195], v242 offset:19456\n"
    "v_accvgpr_read_b32 v236, a188\n"
    "v_accvgpr_read_b32 v237, a189\n"
    "v_accvgpr_read_b32 v238, a190\n"
    "v_accvgpr_read_b32 v239, a191\n"
    "v_cvt_pk_bf16_f32 v224, v224, v225\n"
    "v_mfma_f32_32x32x16_bf16 a[208:223], v[204:207], v[88:91], a[208:223]\n"
    "ds_read_b64_tr_b16 v[196:197], v242 offset:18944\n"
    "ds_read_b64_tr_b16 v[198:199], v242 offset:19968\n"
    "v_cvt_pk_bf16_f32 v225, v226, v227\n"
    "v_cvt_pk_bf16_f32 v226, v228, v229\n"
    "v_cvt_pk_bf16_f32 v227, v230, v231\n"
    "v_cvt_pk_bf16_f32 v228, v232, v233\n"
    "s_waitcnt lgkmcnt(8)\n"
    "v_mfma_f32_32x32x16_bf16 a[192:207], v[208:211], v[92:95], a[192:207]\n"
    "ds_read_b64_tr_b16 v[200:201], v242 offset:20480\n"
    "ds_read_b64_tr_b16 v[202:203], v242 offset:21504\n"
    "v_cvt_pk_bf16_f32 v229, v234, v235\n"
    "v_cvt_pk_bf16_f32 v230, v236, v237\n"
    "v_cvt_pk_bf16_f32 v231, v238, v239\n"
    "s_add_u32 s67, s77, 22528\n"
    "buffer_store_dwordx4 v[224:227], v254, s[56:59], s67 offen sc1\n"
    "v_mfma_f32_32x32x16_bf16 a[208:223], v[212:215], v[92:95], a[208:223]\n"
    "ds_read_b64_tr_b16 v[204:205], v242 offset:20992\n"
    "ds_read_b64_tr_b16 v[206:207], v242 offset:22016\n"
    "buffer_store_dwordx4 v[228:231], v254, s[56:59], s67 offen offset:1024 sc1\n"
    "s_waitcnt lgkmcnt(8)\n"
    "v_mfma_f32_32x32x16_bf16 a[224:239], v[216:219], v[64:67], a[224:239]\n"
    "ds_read_b64_tr_b16 v[208:209], v242 offset:22528\n"
    "ds_read_b64_tr_b16 v[210:211], v242 offset:23552\n"
    "v_mfma_f32_32x32x16_bf16 a[240:255], v[220:223], v[64:67], a[240:255]\n"
    "ds_read_b64_tr_b16 v[212:213], v242 offset:23040\n"
    "ds_read_b64_tr_b16 v[214:215], v242 offset:24064\n"
    "s_waitcnt lgkmcnt(8)\n"
    "v_mfma_f32_32x32x16_bf16 a[224:239], v[192:195], v[68:71], a[224:239]\n"
    "ds_read_b64_tr_b16 v[216:217], v242 offset:24576\n"
    "ds_read_b64_tr_b16 v[218:219], v242 offset:25600\n"
    "v_mfma_f32_32x32x16_bf16 a[240:255], v[196:199], v[68:71], a[240:255]\n"
    "ds_read_b64_tr_b16 v[220:221], v242 offset:25088\n"
    "ds_read_b64_tr_b16 v[222:223], v242 offset:26112\n"
    "v_accvgpr_read_b32 v224, a192\n"
    "v_accvgpr_read_b32 v225, a193\n"
    "v_accvgpr_read_b32 v226, a194\n"
    "v_accvgpr_read_b32 v227, a195\n"
    "s_waitcnt lgkmcnt(8)\n"
    "v_mfma_f32_32x32x16_bf16 a[224:239], v[200:203], v[72:75], a[224:239]\n"
    "ds_read_b64_tr_b16 v[192:193], v242 offset:26624\n"
    "ds_read_b64_tr_b16 v[194:195], v242 offset:27648\n"
    "v_accvgpr_read_b32 v228, a196\n"
    "v_accvgpr_read_b32 v229, a197\n"
    "v_accvgpr_read_b32 v230, a198\n"
    "v_accvgpr_read_b32 v231, a199\n"
    "v_accvgpr_read_b32 v232, a200\n"
    "v_mfma_f32_32x32x16_bf16 a[240:255], v[204:207], v[72:75], a[240:255]\n"
    "ds_read_b64_tr_b16 v[196:197], v242 offset:27136\n"
    "ds_read_b64_tr_b16 v[198:199], v242 offset:28160\n"
    "v_accvgpr_read_b32 v233, a201\n"
    "v_accvgpr_read_b32 v234, a202\n"
    "v_accvgpr_read_b32 v235, a203\n"
    "v_accvgpr_read_b32 v236, a204\n"
    "s_waitcnt lgkmcnt(8)\n"
    "v_mfma_f32_32x32x16_bf16 a[224:239], v[208:211], v[76:79], a[224:239]\n"
    "ds_read_b64_tr_b16 v[200:201], v242 offset:28672\n"
    "ds_read_b64_tr_b16 v[202:203], v242 offset:29696\n"
    "v_accvgpr_read_b32 v237, a205\n"
    "v_accvgpr_read_b32 v238, a206\n"
    "v_accvgpr_read_b32 v239, a207\n"
    "v_cvt_pk_bf16_f32 v224, v224, v225\n"
    "v_cvt_pk_bf16_f32 v225, v226, v227\n"
    "v_mfma_f32_32x32x16_bf16 a[240:255], v[212:215], v[76:79], a[240:255]\n"
    "ds_read_b64_tr_b16 v[204:205], v242 offset:29184\n"
    "ds_read_b64_tr_b16 v[206:207], v242 offset:30208\n"
    "v_cvt_pk_bf16_f32 v226, v228, v229\n"
    "v_cvt_pk_bf16_f32 v227, v230, v231\n"
    "v_cvt_pk_bf16_f32 v228, v232, v233\n"
    "v_cvt_pk_bf16_f32 v229, v234, v235\n"
    "s_waitcnt lgkmcnt(8)\n"
    "v_mfma_f32_32x32x16_bf16 a[224:239], v[216:219], v[80:83], a[224:239]\n"
    "ds_read_b64_tr_b16 v[208:209], v242 offset:30720\n"
    "ds_read_b64_tr_b16 v[210:211], v242 offset:31744\n"
    "v_cvt_pk_bf16_f32 v230, v236, v237\n"
    "v_cvt_pk_bf16_f32 v231, v238, v239\n"
    "s_add_u32 s67, s77, 24576\n"
    "buffer_store_dwordx4 v[224:227], v254, s[56:59], s67 offen sc1\n"
    "buffer_store_dwordx4 v[228:231], v254, s[56:59], s67 offen offset:1024 sc1\n"
    "v_mfma_f32_32x32x16_bf16 a[240:255], v[220:223], v[80:83], a[240:255]\n"
    "ds_read_b64_tr_b16 v[212:213], v242 offset:31232\n"
    "ds_read_b64_tr_b16 v[214:215], v242 offset:32256\n"
    "v_accvgpr_read_b32 v224, a208\n"
    "v_accvgpr_read_b32 v225, a209\n"
    "v_accvgpr_read_b32 v226, a210\n"
    "v_accvgpr_read_b32 v227, a211\n"
    "s_waitcnt lgkmcnt(8)\n"
    "v_mfma_f32_32x32x16_bf16 a[224:239], v[192:195], v[84:87], a[224:239]\n"
    "s_barrier\n"
    "v_accvgpr_read_b32 v228, a212\n"
    "v_accvgpr_read_b32 v229, a213\n"
    "v_accvgpr_read_b32 v230, a214\n"
    "v_accvgpr_read_b32 v231, a215\n"
    "v_accvgpr_read_b32 v232, a216\n"
    "v_accvgpr_read_b32 v233, a217\n"
    "v_mfma_f32_32x32x16_bf16 a[240:255], v[196:199], v[84:87], a[240:255]\n"
    "v_accvgpr_read_b32 v234, a218\n"
    "v_accvgpr_read_b32 v235, a219\n"
    "v_accvgpr_read_b32 v236, a220\n"
    "v_accvgpr_read_b32 v237, a221\n"
    "v_accvgpr_read_b32 v238, a222\n"
    "v_accvgpr_read_b32 v239, a223\n"
    "s_waitcnt lgkmcnt(4)\n"
    "v_mfma_f32_32x32x16_bf16 a[224:239], v[200:203], v[88:91], a[224:239]\n"
    "v_cvt_pk_bf16_f32 v224, v224, v225\n"
    "v_cvt_pk_bf16_f32 v225, v226, v227\n"
    "v_cvt_pk_bf16_f32 v226, v228, v229\n"
    "v_cvt_pk_bf16_f32 v227, v230, v231\n"
    "v_cvt_pk_bf16_f32 v228, v232, v233\n"
    "v_cvt_pk_bf16_f32 v229, v234, v235\n"
    "v_cvt_pk_bf16_f32 v230, v236, v237\n"
    "v_mfma_f32_32x32x16_bf16 a[240:255], v[204:207], v[88:91], a[240:255]\n"
    "v_cvt_pk_bf16_f32 v231, v238, v239\n"
    "s_add_u32 s67, s77, 26624\n"
    "buffer_store_dwordx4 v[224:227], v254, s[56:59], s67 offen sc1\n"
    "buffer_store_dwordx4 v[228:231], v254, s[56:59], s67 offen offset:1024 sc1\n"
    "s_waitcnt lgkmcnt(0)\n"
    "v_mfma_f32_32x32x16_bf16 a[224:239], v[208:211], v[92:95], a[224:239]\n"
    "v_mfma_f32_32x32x16_bf16 a[240:255], v[212:215], v[92:95], a[240:255]\n"
    "s_nop 10\n"
    "v_accvgpr_read_b32 v224, a224\n"
    "v_accvgpr_read_b32 v225, a225\n"
    "v_accvgpr_read_b32 v226, a226\n"
    "v_accvgpr_read_b32 v227, a227\n"
    "v_accvgpr_read_b32 v228, a228\n"
    "v_accvgpr_read_b32 v229, a229\n"
    "v_accvgpr_read_b32 v230, a230\n"
    "v_accvgpr_read_b32 v231, a231\n"
    "v_accvgpr_read_b32 v232, a232\n"
    "v_accvgpr_read_b32 v233, a233\n"
    "v_accvgpr_read_b32 v234, a234\n"
    "v_accvgpr_read_b32 v235, a235\n"
    "v_accvgpr_read_b32 v236, a236\n"
    "v_accvgpr_read_b32 v237, a237\n"
    "v_accvgpr_read_b32 v238, a238\n"
    "v_accvgpr_read_b32 v239, a239\n"
    "v_cvt_pk_bf16_f32 v224, v224, v225\n"
    "v_cvt_pk_bf16_f32 v225, v226, v227\n"
    "v_cvt_pk_bf16_f32 v226, v228, v229\n"
    "v_cvt_pk_bf16_f32 v227, v230, v231\n"
    "v_cvt_pk_bf16_f32 v228, v232, v233\n"
    "v_cvt_pk_bf16_f32 v229, v234, v235\n"
    "v_cvt_pk_bf16_f32 v230, v236, v237\n"
    "v_cvt_pk_bf16_f32 v231, v238, v239\n"
    "s_add_u32 s67, s77, 28672\n"
    "buffer_store_dwordx4 v[224:227], v254, s[56:59], s67 offen sc1\n"
    "buffer_store_dwordx4 v[228:231], v254, s[56:59], s67 offen offset:1024 sc1\n"
    "s_nop 0\n"
    "v_accvgpr_read_b32 v224, a240\n"
    "v_accvgpr_read_b32 v225, a241\n"
    "v_accvgpr_read_b32 v226, a242\n"
    "v_accvgpr_read_b32 v227, a243\n"
    "v_accvgpr_read_b32 v228, a244\n"
    "v_accvgpr_read_b32 v229, a245\n"
    "v_accvgpr_read_b32 v230, a246\n"
    "v_accvgpr_read_b32 v231, a247\n"
    "v_accvgpr_read_b32 v232, a248\n"
    "v_accvgpr_read_b32 v233, a249\n"
    "v_accvgpr_read_b32 v234, a250\n"
    "v_accvgpr_read_b32 v235, a251\n"
    "v_accvgpr_read_b32 v236, a252\n"
    "v_accvgpr_read_b32 v237, a253\n"
    "v_accvgpr_read_b32 v238, a254\n"
    "v_accvgpr_read_b32 v239, a255\n"
    "v_cvt_pk_bf16_f32 v224, v224, v225\n"
    "v_cvt_pk_bf16_f32 v225, v226, v227\n"
    "v_cvt_pk_bf16_f32 v226, v228, v229\n"
    "v_cvt_pk_bf16_f32 v227, v230, v231\n"
    "v_cvt_pk_bf16_f32 v228, v232, v233\n"
    "v_cvt_pk_bf16_f32 v229, v234, v235\n"
    "v_cvt_pk_bf16_f32 v230, v236, v237\n"
    "v_cvt_pk_bf16_f32 v231, v238, v239\n"
    "s_add_u32 s67, s77, 30720\n"
    "buffer_store_dwordx4 v[224:227], v254, s[56:59], s67 offen sc1\n"
    "buffer_store_dwordx4 v[228:231], v254, s[56:59], s67 offen offset:1024 sc1\n"
    "s_endpgm\n"
    "L_stub2_i0_1:\n"
    "s_call_b64 s[70:71], L_rare2\n"
    "s_branch L_back2_i0_1\n"
    "L_stub1_i0_1:\n"
    "s_call_b64 s[70:71], L_rare1\n"
    "s_branch L_back1_i0_1\n"
    "L_stub2_i0_2:\n"
    "s_call_b64 s[70:71], L_rare2\n"
    "s_branch L_back2_i0_2\n"
    "L_stub1_i0_2:\n"
    "s_call_b64 s[70:71], L_rare1\n"
    "s_branch L_back1_i0_2\n"
    "L_stub2_i0_3:\n"
    "s_call_b64 s[70:71], L_rare2\n"
    "s_branch L_back2_i0_3\n"
    "L_stub1_i0_3:\n"
    "s_call_b64 s[70:71], L_rare1\n"
    "s_branch L_back1_i0_3\n"
    "L_stub2_i0_4:\n"
    "s_call_b64 s[70:71], L_rare2\n"
    "s_branch L_back2_i0_4\n"
    "L_stub1_i0_4:\n"
    "s_call_b64 s[70:71], L_rare1\n"
    "s_branch L_back1_i0_4\n"
    "L_stub2_i0_5:\n"
    "s_call_b64 s[70:71], L_rare2\n"
    "s_branch L_back2_i0_5\n"
    "L_stub1_i0_5:\n"
    "s_call_b64 s[70:71], L_rare1\n"
    "s_branch L_back1_i0_5\n"
    "L_stub2_i0_6:\n"
    "s_call_b64 s[70:71], L_rare2\n"
    "s_branch L_back2_i0_6\n"
    "L_stub1_i0_6:\n"
    "s_call_b64 s[70:71], L_rare1\n"
    "s_branch L_back1_i0_6\n"
    "L_stub2_i0_7:\n"
    "s_call_b64 s[70:71], L_rare2\n"
    "s_branch L_back2_i0_7\n"
    "L_stub1_i0_7:\n"
    "s_call_b64 s[70:71], L_rare1\n"
    "s_branch L_back1_i0_7\n"
    "L_stub2_lp_1:\n"
    "s_call_b64 s[70:71], L_rare2\n"
    "s_branch L_back2_lp_1\n"
    "L_stub1_lp_1:\n"
    "s_call_b64 s[70:71], L_rare1\n"
    "s_branch L_back1_lp_1\n"
    "L_stub2_lp_2:\n"
    "s_call_b64 s[70:71], L_rare2\n"
    "s_branch L_back2_lp_2\n"
    "L_stub1_lp_2:\n"
    "s_call_b64 s[70:71], L_rare1\n"
    "s_branch L_back1_lp_2\n"
    "L_stub2_lp_3:\n"
    "s_call_b64 s[70:71], L_rare2\n"
    "s_branch L_back2_lp_3\n"
    "L_stub1_lp_3:\n"
    "s_call_b64 s[70:71], L_rare1\n"
    "s_branch L_back1_lp_3\n"
    "L_stub2_lp_4:\n"
    "s_call_b64 s[70:71], L_rare2\n"
    "s_branch L_back2_lp_4\n"
    "L_stub1_lp_4:\n"
    "s_call_b64 s[70:71], L_rare1\n"
    "s_branch L_back1_lp_4\n"
    "L_stub2_lp_5:\n"
    "s_call_b64 s[70:71], L_rare2\n"
    "s_branch L_back2_lp_5\n"
    "L_stub1_lp_5:\n"
    "s_call_b64 s[70:71], L_rare1\n"
    "s_branch L_back1_lp_5\n"
    "L_stub2_lp_6:\n"
    "s_call_b64 s[70:71], L_rare2\n"
    "s_branch L_back2_lp_6\n"
    "L_stub1_lp_6:\n"
    "s_call_b64 s[70:71], L_rare1\n"
    "s_branch L_back1_lp_6\n"
    "L_stub2_lp_7:\n"
    "s_call_b64 s[70:71], L_rare2\n"
    "s_branch L_back2_lp_7\n"
    "L_stub1_lp_7:\n"
    "s_call_b64 s[70:71], L_rare1\n"
    "s_branch L_back1_lp_7\n"
    "L_rare1:\n"
    "s_nop 7\n"
    "v_max_f32 v249, 0, v249\n"
    "v_sub_f32 v252, 0, v249\n"
    "v_exp_f32 v252, v252\n"
    "v_sub_f32 v176, v176, v249\n"
    "v_sub_f32 v177, v177, v249\n"
    "v_sub_f32 v178, v178, v249\n"
    "v_sub_f32 v179, v179, v249\n"
    "v_sub_f32 v180, v180, v249\n"
    "v_sub_f32 v181, v181, v249\n"
    "v_sub_f32 v182, v182, v249\n"
    "v_sub_f32 v183, v183, v249\n"
    "v_sub_f32 v184, v184, v249\n"
    "v_sub_f32 v185, v185, v249\n"
    "v_sub_f32 v186, v186, v249\n"
    "v_sub_f32 v187, v187, v249\n"
    "v_sub_f32 v188, v188, v249\n"
    "v_sub_f32 v189, v189, v249\n"
    "v_sub_f32 v190, v190, v249\n"
    "v_sub_f32 v191, v191, v249\n"
    "v_sub_f32 v128, v128, v249\n"
    "v_sub_f32 v129, v129, v249\n"
    "v_sub_f32 v130, v130, v249\n"
    "v_sub_f32 v131, v131, v249\n"
    "v_sub_f32 v132, v132, v249\n"
    "v_sub_f32 v133, v133, v249\n"
    "v_sub_f32 v134, v134, v249\n"
    "v_sub_f32 v135, v135, v249\n"
    "v_sub_f32 v136, v136, v249\n"
    "v_sub_f32 v137, v137, v249\n"
    "v_sub_f32 v138, v138, v249\n"
    "v_sub_f32 v139, v139, v249\n"
    "v_sub_f32 v140, v140, v249\n"
    "v_sub_f32 v141, v141, v249\n"
    "v_sub_f32 v142, v142, v249\n"
    "v_sub_f32 v143, v143, v249\n"
    "v_sub_f32 v144, v144, v249\n"
    "v_sub_f32 v145, v145, v249\n"
    "v_sub_f32 v146, v146, v249\n"
    "v_sub_f32 v147, v147, v249\n"
    "v_sub_f32 v148, v148, v249\n"
    "v_sub_f32 v149, v149, v249\n"
    "v_sub_f32 v150, v150, v249\n"
    "v_sub_f32 v151, v151, v249\n"
    "v_sub_f32 v152, v152, v249\n"
    "v_sub_f32 v153, v153, v249\n"
    "v_sub_f32 v154, v154, v249\n"
    "v_sub_f32 v155, v155, v249\n"
    "v_sub_f32 v156, v156, v249\n"
    "v_sub_f32 v157, v157, v249\n"
    "v_sub_f32 v158, v158, v249\n"
    "v_sub_f32 v159, v159, v249\n"
    "v_mul_f32 v247, v247, v252\n"
    "v_mul_f32 v248, v248, v252\n"
    "s_mov_b32 s69, 1\n"
    "s_nop 1\n"
    "s_setpc_b64 s[70:71]\n"
    "L_rare2:\n"
    "s_nop 7\n"
    "s_nop 7\n"
    "v_mul_f32 v0, v0, v252\n"
    "v_mul_f32 v1, v1, v252\n"
    "v_mul_f32 v2, v2, v252\n"
    "v_mul_f32 v3, v3, v252\n"
    "v_mul_f32 v4, v4, v252\n"
    "v_mul_f32 v5, v5, v252\n"
    "v_mul_f32 v6, v6, v252\n"
    "v_mul_f32 v7, v7, v252\n"
    "v_mul_f32 v8, v8, v252\n"
    "v_mul_f32 v9, v9, v252\n"
    "v_mul_f32 v10, v10, v252\n"
    "v_mul_f32 v11, v11, v252\n"
    "v_mul_f32 v12, v12, v252\n"
    "v_mul_f32 v13, v13, v252\n"
    "v_mul_f32 v14, v14, v252\n"
    "v_mul_f32 v15, v15, v252\n"
    "v_mul_f32 v16, v16, v252\n"
    "v_mul_f32 v17, v17, v252\n"
    "v_mul_f32 v18, v18, v252\n"
    "v_mul_f32 v19, v19, v252\n"
    "v_mul_f32 v20, v20, v252\n"
    "v_mul_f32 v21, v21, v252\n"
    "v_mul_f32 v22, v22, v252\n"
    "v_mul_f32 v23, v23, v252\n"
    "v_mul_f32 v24, v24, v252\n"
    "v_mul_f32 v25, v25, v252\n"
    "v_mul_f32 v26, v26, v252\n"
    "v_mul_f32 v27, v27, v252\n"
    "v_mul_f32 v28, v28, v252\n"
    "v_mul_f32 v29, v29, v252\n"
    "v_mul_f32 v30, v30, v252\n"
    "v_mul_f32 v31, v31, v252\n"
    "v_mul_f32 v32, v32, v252\n"
    "v_mul_f32 v33, v33, v252\n"
    "v_mul_f32 v34, v34, v252\n"
    "v_mul_f32 v35, v35, v252\n"
    "v_mul_f32 v36, v36, v252\n"
    "v_mul_f32 v37, v37, v252\n"
    "v_mul_f32 v38, v38, v252\n"
    "v_mul_f32 v39, v39, v252\n"
    "v_mul_f32 v40, v40, v252\n"
    "v_mul_f32 v41, v41, v252\n"
    "v_mul_f32 v42, v42, v252\n"
    "v_mul_f32 v43, v43, v252\n"
    "v_mul_f32 v44, v44, v252\n"
    "v_mul_f32 v45, v45, v252\n"
    "v_mul_f32 v46, v46, v252\n"
    "v_mul_f32 v47, v47, v252\n"
    "v_mul_f32 v48, v48, v252\n"
    "v_mul_f32 v49, v49, v252\n"
    "v_mul_f32 v50, v50, v252\n"
    "v_mul_f32 v51, v51, v252\n"
    "v_mul_f32 v52, v52, v252\n"
    "v_mul_f32 v53, v53, v252\n"
    "v_mul_f32 v54, v54, v252\n"
    "v_mul_f32 v55, v55, v252\n"
    "v_mul_f32 v56, v56, v252\n"
    "v_mul_f32 v57, v57, v252\n"
    "v_mul_f32 v58, v58, v252\n"
    "v_mul_f32 v59, v59, v252\n"
    "v_mul_f32 v60, v60, v252\n"
    "v_mul_f32 v61, v61, v252\n"
    "v_mul_f32 v62, v62, v252\n"
    "v_mul_f32 v63, v63, v252\n"
    "s_mov_b32 s69, 0\n"
    "s_nop 1\n"
    "s_setpc_b64 s[70:71]\n"
    "L_w3_entry:\n"
    "v_mbcnt_lo_u32_b32 v0, -1, 0\n"
    "v_mbcnt_hi_u32_b32 v0, -1, v0\n"
    "v_and_b32 v1, 31, v0\n"
    "v_lshrrev_b32 v2, 5, v0\n"
    "v_bfe_u32 v3, v0, 2, 3\n"
    "v_and_b32 v4, 3, v0\n"
    "v_bfe_u32 v5, v0, 2, 2\n"
    "v_bfe_u32 v6, v0, 4, 1\n"
    "v_bfe_u32 v7, v0, 2, 2\n"
    "v_lshrrev_b32 v8, 3, v1\n"
    "v_and_b32 v9, 7, v1\n"
    "v_xor_b32 v10, v2, v7\n"
    "v_xor_b32 v11, 2, v10\n"
    "v_lshrrev_b32 v12, 2, v3\n"
    "v_xor_b32 v12, v12, v4\n"
    "v_xor_b32 v13, 2, v12\n"
    "v_lshlrev_b32 v14, 6, v2\n"
    "v_and_b32 v15, 1, v4\n"
    "v_lshrrev_b32 v16, 1, v4\n"
    "v_lshlrev_b32 v244, 8, v3\n"
    "v_lshl_add_u32 v244, v14, 0, v244\n"
    "v_lshl_add_u32 v244, v12, 4, v244\n"
    "v_lshlrev_b32 v245, 8, v3\n"
    "v_lshl_add_u32 v245, v14, 0, v245\n"
    "v_lshl_add_u32 v245, v13, 4, v245\n"
    "v_lshlrev_b32 v246, 8, v3\n"
    "v_lshl_add_u32 v246, v14, 0, v246\n"
    "v_lshl_add_u32 v246, v4, 4, v246\n"
    "v_lshlrev_b32 v247, 10, v3\n"
    "v_lshl_add_u32 v247, v14, 0, v247\n"
    "v_lshl_add_u32 v247, v4, 4, v247\n"
    "v_lshlrev_b32 v240, 11, v2\n"
    "v_lshl_add_u32 v240, v5, 6, v240\n"
    "v_lshl_add_u32 v240, v6, 5, v240\n"
    "v_lshl_add_u32 v240, v15, 4, v240\n"
    "v_lshl_add_u32 v240, v16, 3, v240\n"
    "v_add_u32 v240, 98304, v240\n"
    "v_lshlrev_b32 v241, 10, v8\n"
    "v_lshl_add_u32 v241, v9, 6, v241\n"
    "v_lshl_add_u32 v241, v10, 4, v241\n"
    "v_add_u32 v241, 155648, v241\n"
    "v_lshlrev_b32 v242, 10, v8\n"
    "v_lshl_add_u32 v242, v9, 6, v242\n"
    "v_lshl_add_u32 v242, v11, 4, v242\n"
    "v_add_u32 v242, 155648, v242\n"
    "v_lshlrev_b32 v243, 4, v0\n"
    "v_add_u32 v243, 131072, v243\n"
    "v_lshlrev_b32 v248, 13, v3\n"
    "v_lshl_add_u32 v248, v14, 0, v248\n"
    "v_lshl_add_u32 v248, v4, 4, v248\n"
    "v_lshlrev_b32 v249, 10, v3\n"
    "v_lshl_add_u32 v249, v14, 0, v249\n"
    "v_lshl_add_u32 v249, v12, 4, v249\n"
    "v_lshlrev_b32 v250, 10, v3\n"
    "v_lshl_add_u32 v250, v14, 0, v250\n"
    "v_lshl_add_u32 v250, v13, 4, v250\n"
    "v_lshrrev_b32 v19, 1, v0\n"
    "v_and_b32 v20, 1, v0\n"
    "v_lshlrev_b32 v251, 13, v19\n"
    "v_lshl_add_u32 v251, v20, 7, v251\n"
    "v_lshlrev_b32 v252, 7, v0\n"
    "s_and_b32 s66, s60, 1\n"
    "s_lshl_b32 s66, s66, 1\n"
    "v_xor_b32 v18, s66, v12\n"
    "v_lshlrev_b32 v232, 10, v3\n"
    "v_lshl_add_u32 v232, v14, 0, v232\n"
    "v_lshl_add_u32 v232, v18, 4, v232\n"
    "v_lshlrev_b32 v233, 10, v8\n"
    "v_lshl_add_u32 v233, v9, 6, v233\n"
    "v_lshl_add_u32 v233, v10, 4, v233\n"
    "v_lshlrev_b32 v234, 10, v8\n"
    "v_lshl_add_u32 v234, v9, 6, v234\n"
    "v_lshl_add_u32 v234, v11, 4, v234\n"
    "v_add_u32 v233, 32768, v233\n"
    "v_add_u32 v234, 32768, v234\n"
    "v_add_u32 v235, 49152, v233\n"
    "v_add_u32 v236, 49152, v234\n"
    "s_add_u32 m0, s82, 32768\n"
    "s_add_u32 s66, s80, 0\n"
    "buffer_load_dwordx4 v232, s[52:55], s66 offen lds\n"
    "s_add_u32 m0, s82, 36864\n"
    "s_add_u32 s66, s80, 32768\n"
    "buffer_load_dwordx4 v232, s[52:55], s66 offen lds\n"
    "s_add_u32 m0, s82, 40960\n"
    "s_add_u32 s66, s80, 65536\n"
    "buffer_load_dwordx4 v232, s[52:55], s66 offen lds\n"
    "s_add_u32 m0, s61, 131072\n"
    "s_add_u32 s66, s79, 0\n"
    "buffer_load_dwordx4 v248, s[44:47], s66 offen lds\n"
    "s_add_u32 m0, s61, 131968\n"
    "s_nop 0\n"
    "buffer_load_dwordx4 v248, s[44:47], s66 offen offset:128 lds\n"
    "s_add_u32 m0, s61, 133120\n"
    "s_add_u32 s66, s79, 65536\n"
    "buffer_load_dwordx4 v248, s[44:47], s66 offen lds\n"
    "s_add_u32 m0, s61, 134016\n"
    "s_nop 0\n"
    "buffer_load_dwordx4 v248, s[44:47], s66 offen offset:128 lds\n"
    "s_add_u32 m0, s82, 44928\n"
    "s_add_u32 s66, s80, 0\n"
    "buffer_load_dwordx4 v232, s[52:55], s66 offen offset:128 lds\n"
    "s_add_u32 m0, s82, 49024\n"
    "s_add_u32 s66, s80, 32768\n"
    "buffer_load_dwordx4 v232, s[52:55], s66 offen offset:128 lds\n"
    "s_add_u32 m0, s82, 53120\n"
    "s_add_u32 s66, s80, 65536\n"
    "buffer_load_dwordx4 v232, s[52:55], s66 offen offset:128 lds\n"
    "s_add_u32 m0, s61, 147456\n"
    "s_add_u32 s66, s79, 524288\n"
    "buffer_load_dwordx4 v248, s[44:47], s66 offen lds\n"
    "s_add_u32 m0, s61, 148352\n"
    "s_nop 0\n"
    "buffer_load_dwordx4 v248, s[44:47], s66 offen offset:128 lds\n"
    "s_add_u32 m0, s61, 149504\n"
    "s_add_u32 s66, s79, 589824\n"
    "buffer_load_dwordx4 v248, s[44:47], s66 offen lds\n"
    "s_add_u32 m0, s61, 150400\n"
    "s_nop 0\n"
    "buffer_load_dwordx4 v248, s[44:47], s66 offen offset:128 lds\n"
    "s_add_u32 m0, s82, 57088\n"
    "s_add_u32 s66, s80, 0\n"
    "buffer_load_dwordx4 v232, s[52:55], s66 offen offset:256 lds\n"
    "s_add_u32 m0, s82, 61184\n"
    "s_add_u32 s66, s80, 32768\n"
    "buffer_load_dwordx4 v232, s[52:55], s66 offen offset:256 lds\n"
    "s_add_u32 m0, s82, 65280\n"
    "s_add_u32 s66, s80, 65536\n"
    "buffer_load_dwordx4 v232, s[52:55], s66 offen offset:256 lds\n"
    "s_add_u32 m0, s61, 0\n"
    "s_add_u32 s66, s79, 1048576\n"
    "buffer_load_dwordx4 v248, s[44:47], s66 offen lds\n"
    "s_add_u32 m0, s61, 896\n"
    "s_nop 0\n"
    "buffer_load_dwordx4 v248, s[44:47], s66 offen offset:128 lds\n"
    "s_add_u32 m0, s61, 2048\n"
    "s_add_u32 s66, s79, 1114112\n"
    "buffer_load_dwordx4 v248, s[44:47], s66 offen lds\n"
    "s_add_u32 m0, s61, 2944\n"
    "s_nop 0\n"
    "buffer_load_dwordx4 v248, s[44:47], s66 offen offset:128 lds\n"
    "s_add_u32 m0, s82, 69248\n"
    "s_add_u32 s66, s80, 0\n"
    "buffer_load_dwordx4 v232, s[52:55], s66 offen offset:384 lds\n"
    "s_add_u32 m0, s82, 73344\n"
    "s_add_u32 s66, s80, 32768\n"
    "buffer_load_dwordx4 v232, s[52:55], s66 offen offset:384 lds\n"
    "s_add_u32 m0, s82, 77440\n"
    "s_add_u32 s66, s80, 65536\n"
    "buffer_load_dwordx4 v232, s[52:55], s66 offen offset:384 lds\n"
    "s_add_u32 m0, s61, 16384\n"
    "s_add_u32 s66, s79, 1572864\n"
    "buffer_load_dwordx4 v248, s[44:47], s66 offen lds\n"
    "s_add_u32 m0, s61, 17280\n"
    "s_nop 0\n"
    "buffer_load_dwordx4 v248, s[44:47], s66 offen offset:128 lds\n"
    "s_add_u32 m0, s61, 18432\n"
    "s_add_u32 s66, s79, 1638400\n"
    "buffer_load_dwordx4 v248, s[44:47], s66 offen lds\n"
    "s_add_u32 m0, s61, 19328\n"
    "s_nop 0\n"
    "buffer_load_dwordx4 v248, s[44:47], s66 offen offset:128 lds\n"
    "s_add_u32 m0, s82, 81408\n"
    "s_add_u32 s66, s80, 0\n"
    "buffer_load_dwordx4 v232, s[52:55], s66 offen offset:512 lds\n"
    "s_add_u32 m0, s82, 85504\n"
    "s_add_u32 s66, s80, 32768\n"
    "buffer_load_dwordx4 v232, s[52:55], s66 offen offset:512 lds\n"
    "s_add_u32 m0, s82, 89600\n"
    "s_add_u32 s66, s80, 65536\n"
    "buffer_load_dwordx4 v232, s[52:55], s66 offen offset:512 lds\n"
    "s_add_u32 m0, s82, 93568\n"
    "s_add_u32 s66, s80, 0\n"
    "buffer_load_dwordx4 v232, s[52:55], s66 offen offset:640 lds\n"
    "s_add_u32 m0, s82, 97664\n"
    "s_add_u32 s66, s80, 32768\n"
    "buffer_load_dwordx4 v232, s[52:55], s66 offen offset:640 lds\n"
    "s_add_u32 m0, s82, 101760\n"
    "s_add_u32 s66, s80, 65536\n"
    "buffer_load_dwordx4 v232, s[52:55], s66 offen offset:640 lds\n"
    "s_add_u32 m0, s82, 105728\n"
    "s_add_u32 s66, s80, 0\n"
    "buffer_load_dwordx4 v232, s[52:55], s66 offen offset:768 lds\n"
    "s_add_u32 m0, s82, 109824\n"
    "s_add_u32 s66, s80, 32768\n"
    "buffer_load_dwordx4 v232, s[52:55], s66 offen offset:768 lds\n"
    "s_add_u32 m0, s82, 113920\n"
    "s_add_u32 s66, s80, 65536\n"
    "buffer_load_dwordx4 v232, s[52:55], s66 offen offset:768 lds\n"
    "s_add_u32 m0, s82, 117888\n"
    "s_add_u32 s66, s80, 0\n"
    "buffer_load_dwordx4 v232, s[52:55], s66 offen offset:896 lds\n"
    "s_add_u32 m0, s82, 121984\n"
    "s_add_u32 s66, s80, 32768\n"
    "buffer_load_dwordx4 v232, s[52:55], s66 offen offset:896 lds\n"
    "s_add_u32 m0, s82, 126080\n"
    "s_add_u32 s66, s80, 65536\n"
    "buffer_load_dwordx4 v232, s[52:55], s66 offen offset:896 lds\n"
    "s_add_u32 s85, s75, s83\n"
    "buffer_load_dword v253, v251, s[44:47], s85 offen\n"
    "s_waitcnt vmcnt(38)\n"
    "s_waitcnt vmcnt(34)\n"
    "s_barrier\n"
    "ds_read_b128 a[0:3], v233 offset:0\n"
    "ds_read_b128 a[4:7], v234 offset:0\n"
    "ds_read_b128 a[8:11], v233 offset:512\n"
    "ds_read_b128 a[12:15], v234 offset:512\n"
    "ds_read_b128 a[128:131], v233 offset:4096\n"
    "ds_read_b128 a[132:135], v234 offset:4096\n"
    "ds_read_b128 a[136:139], v233 offset:4608\n"
    "ds_read_b128 a[140:143], v234 offset:4608\n"
    "s_waitcnt lgkmcnt(0)\n"
    "s_waitcnt vmcnt(27)\n"
    "s_barrier\n"
    "ds_read_b128 a[16:19], v233 offset:12288\n"
    "ds_read_b128 a[20:23], v234 offset:12288\n"
    "ds_read_b128 a[24:27], v233 offset:12800\n"
    "ds_read_b128 a[28:31], v234 offset:12800\n"
    "ds_read_b128 a[144:147], v233 offset:16384\n"
    "ds_read_b128 a[148:151], v234 offset:16384\n"
    "ds_read_b128 a[152:155], v233 offset:16896\n"
    "ds_read_b128 a[156:159], v234 offset:16896\n"
    "s_waitcnt lgkmcnt(0)\n"
    "s_waitcnt vmcnt(20)\n"
    "s_barrier\n"
    "s_add_u32 m0, s61, 131072\n"
    "s_add_u32 s66, s79, 2097152\n"
    "buffer_load_dwordx4 v248, s[44:47], s66 offen lds\n"
    "s_add_u32 m0, s61, 131968\n"
    "s_nop 0\n"
    "buffer_load_dwordx4 v248, s[44:47], s66 offen offset:128 lds\n"
    "s_add_u32 m0, s61, 133120\n"
    "s_add_u32 s66, s79, 2162688\n"
    "buffer_load_dwordx4 v248, s[44:47], s66 offen lds\n"
    "s_add_u32 m0, s61, 134016\n"
    "s_nop 0\n"
    "buffer_load_dwordx4 v248, s[44:47], s66 offen offset:128 lds\n"
    "ds_read_b128 a[32:35], v233 offset:24576\n"
    "ds_read_b128 a[36:39], v234 offset:24576\n"
    "ds_read_b128 a[40:43], v233 offset:25088\n"
    "ds_read_b128 a[44:47], v234 offset:25088\n"
    "ds_read_b128 a[160:163], v233 offset:28672\n"
    "ds_read_b128 a[164:167], v234 offset:28672\n"
    "ds_read_b128 a[168:171], v233 offset:29184\n"
    "ds_read_b128 a[172:175], v234 offset:29184\n"
    "s_waitcnt lgkmcnt(0)\n"
    "s_waitcnt vmcnt(17)\n"
    "s_barrier\n"
    "s_add_u32 m0, s61, 147456\n"
    "s_add_u32 s66, s79, 2621440\n"
    "buffer_load_dwordx4 v248, s[44:47], s66 offen lds\n"
    "s_add_u32 m0, s61, 148352\n"
    "s_nop 0\n"
    "buffer_load_dwordx4 v248, s[44:47], s66 offen offset:128 lds\n"
    "s_add_u32 m0, s61, 149504\n"
    "s_add_u32 s66, s79, 2686976\n"
    "buffer_load_dwordx4 v248, s[44:47], s66 offen lds\n"
    "s_add_u32 m0, s61, 150400\n"
    "s_nop 0\n"
    "buffer_load_dwordx4 v248, s[44:47], s66 offen offset:128 lds\n"
    "s_add_u32 m0, s61, 32768\n"
    "s_add_u32 s66, s62, 0\n"
    "buffer_load_dwordx4 v246, s[40:43], s66 offen nt lds\n"
    "buffer_load_dwordx4 v246, s[40:43], s66 offen offset:2048 nt lds\n"
    "s_add_u32 m0, s61, 33664\n"
    "s_nop 0\n"
    "buffer_load_dwordx4 v246, s[40:43], s66 offen offset:128 nt lds\n"
    "buffer_load_dwordx4 v246, s[40:43], s66 offen offset:2176 nt lds\n"
    "ds_read_b128 a[48:51], v233 offset:36864\n"
    "ds_read_b128 a[52:55], v234 offset:36864\n"
    "ds_read_b128 a[56:59], v233 offset:37376\n"
    "ds_read_b128 a[60:63], v234 offset:37376\n"
    "ds_read_b128 a[176:179], v233 offset:40960\n"
    "ds_read_b128 a[180:183], v234 offset:40960\n"
    "ds_read_b128 a[184:187], v233 offset:41472\n"
    "ds_read_b128 a[188:191], v234 offset:41472\n"
    "s_waitcnt lgkmcnt(0)\n"
    "s_waitcnt vmcnt(8)\n"
    "s_barrier\n"
    "s_add_u32 m0, s61, 49152\n"
    "s_add_u32 s66, s79, 3145728\n"
    "buffer_load_dwordx4 v248, s[44:47], s66 offen lds\n"
    "s_add_u32 m0, s61, 50048\n"
    "s_nop 0\n"
    "buffer_load_dwordx4 v248, s[44:47], s66 offen offset:128 lds\n"
    "s_add_u32 m0, s61, 51200\n"
    "s_add_u32 s66, s79, 3211264\n"
    "buffer_load_dwordx4 v248, s[44:47], s66 offen lds\n"
    "s_add_u32 m0, s61, 52096\n"
    "s_nop 0\n"
    "buffer_load_dwordx4 v248, s[44:47], s66 offen offset:128 lds\n"
    "s_add_u32 m0, s61, 0\n"
    "s_add_u32 s66, s62, 0\n"
    "buffer_load_dwordx4 v244, s[36:39], s66 offen nt lds\n"
    "buffer_load_dwordx4 v245, s[36:39], s66 offen offset:2048 nt lds\n"
    "s_add_u32 m0, s61, 896\n"
    "s_nop 0\n"
    "buffer_load_dwordx4 v244, s[36:39], s66 offen offset:128 nt lds\n"
    "buffer_load_dwordx4 v245, s[36:39], s66 offen offset:2176 nt lds\n"
    "ds_read_b128 a[64:67], v235 offset:0\n"
    "ds_read_b128 a[68:71], v236 offset:0\n"
    "ds_read_b128 a[72:75], v235 offset:512\n"
    "ds_read_b128 a[76:79], v236 offset:512\n"
    "ds_read_b128 a[192:195], v235 offset:4096\n"
    "ds_read_b128 a[196:199], v236 offset:4096\n"
    "ds_read_b128 a[200:203], v235 offset:4608\n"
    "ds_read_b128 a[204:207], v236 offset:4608\n"
    "s_waitcnt lgkmcnt(0)\n"
    "s_waitcnt vmcnt(12)\n"
    "s_barrier\n"
    "s_add_u32 m0, s61, 65536\n"
    "s_add_u32 s66, s79, 3670016\n"
    "buffer_load_dwordx4 v248, s[44:47], s66 offen lds\n"
    "s_add_u32 m0, s61, 66432\n"
    "s_nop 0\n"
    "buffer_load_dwordx4 v248, s[44:47], s66 offen offset:128 lds\n"
    "s_add_u32 m0, s61, 67584\n"
    "s_add_u32 s66, s79, 3735552\n"
    "buffer_load_dwordx4 v248, s[44:47], s66 offen lds\n"
    "s_add_u32 m0, s61, 68480\n"
    "s_nop 0\n"
    "buffer_load_dwordx4 v248, s[44:47], s66 offen offset:128 lds\n"
    "s_add_u32 m0, s61, 16384\n"
    "s_add_u32 s66, s62, 16384\n"
    "buffer_load_dwordx4 v244, s[36:39], s66 offen nt lds\n"
    "buffer_load_dwordx4 v245, s[36:39], s66 offen offset:2048 nt lds\n"
    "s_add_u32 m0, s61, 17280\n"
    "s_nop 0\n"
    "buffer_load_dwordx4 v244, s[36:39], s66 offen offset:128 nt lds\n"
    "buffer_load_dwordx4 v245, s[36:39], s66 offen offset:2176 nt lds\n"
    "ds_read_b128 a[80:83], v235 offset:12288\n"
    "ds_read_b128 a[84:87], v236 offset:12288\n"
    "ds_read_b128 a[88:91], v235 offset:12800\n"
    "ds_read_b128 a[92:95], v236 offset:12800\n"
    "ds_read_b128 a[208:211], v235 offset:16384\n"
    "ds_read_b128 a[212:215], v236 offset:16384\n"
    "ds_read_b128 a[216:219], v235 offset:16896\n"
    "ds_read_b128 a[220:223], v236 offset:16896\n"
    "s_waitcnt lgkmcnt(0)\n"
    "s_waitcnt vmcnt(12)\n"
    "s_barrier\n"
    "ds_read_b128 a[96:99], v235 offset:24576\n"
    "ds_read_b128 a[100:103], v236 offset:24576\n"
    "ds_read_b128 a[104:107], v235 offset:25088\n"
    "ds_read_b128 a[108:111], v236 offset:25088\n"
    "ds_read_b128 a[224:227], v235 offset:28672\n"
    "ds_read_b128 a[228:231], v236 offset:28672\n"
    "ds_read_b128 a[232:235], v235 offset:29184\n"
    "ds_read_b128 a[236:239], v236 offset:29184\n"
    "s_waitcnt lgkmcnt(0)\n"
    "s_waitcnt vmcnt(4)\n"
    "s_barrier\n"
    "ds_read_b128 a[112:115], v235 offset:36864\n"
    "ds_read_b128 a[116:119], v236 offset:36864\n"
    "ds_read_b128 a[120:123], v235 offset:37376\n"
    "ds_read_b128 a[124:127], v236 offset:37376\n"
    "ds_read_b128 a[240:243], v235 offset:40960\n"
    "ds_read_b128 a[244:247], v236 offset:40960\n"
    "ds_read_b128 a[248:251], v235 offset:41472\n"
    "ds_read_b128 a[252:255], v236 offset:41472\n"
    "s_mov_b32 m0, 155648\n"
    "s_mov_b32 s67, 65536\n"
    "buffer_load_dwordx4 v249, s[52:55], s67 offen lds\n"
    "s_mov_b32 m0, 156672\n"
    "s_mov_b32 s67, 73728\n"
    "buffer_load_dwordx4 v250, s[52:55], s67 offen lds\n"
    "s_mov_b32 m0, 157696\n"
    "s_mov_b32 s67, 81920\n"
    "buffer_load_dwordx4 v249, s[52:55], s67 offen lds\n"
    "s_mov_b32 m0, 158720\n"
    "s_mov_b32 s67, 90112\n"
    "buffer_load_dwordx4 v250, s[52:55], s67 offen lds\n"
    "s_mov_b32 m0, 159744\n"
    "s_mov_b32 s67, 65664\n"
    "buffer_load_dwordx4 v249, s[52:55], s67 offen lds\n"
    "s_mov_b32 m0, 160768\n"
    "s_mov_b32 s67, 73856\n"
    "buffer_load_dwordx4 v250, s[52:55], s67 offen lds\n"
    "s_mov_b32 m0, 161792\n"
    "s_mov_b32 s67, 82048\n"
    "buffer_load_dwordx4 v249, s[52:55], s67 offen lds\n"
    "s_mov_b32 m0, 162816\n"
    "s_mov_b32 s67, 90240\n"
    "buffer_load_dwordx4 v250, s[52:55], s67 offen lds\n"
    "s_waitcnt lgkmcnt(0)\n"
    "s_barrier\n"
    "s_barrier\n"
    "s_mov_b32 m0, 98304\n"
    "s_add_u32 s67, s75, 0\n"
    "buffer_load_dwordx4 v248, s[44:47], s67 offen lds\n"
    "s_mov_b32 m0, 99200\n"
    "s_nop 0\n"
    "buffer_load_dwordx4 v248, s[44:47], s67 offen offset:128 lds\n"
    "s_mov_b32 m0, 100352\n"
    "s_add_u32 s67, s75, 65536\n"
    "buffer_load_dwordx4 v248, s[44:47], s67 offen lds\n"
    "s_mov_b32 m0, 101248\n"
    "s_nop 0\n"
    "buffer_load_dwordx4 v248, s[44:47], s67 offen offset:128 lds\n"
    "s_mov_b32 m0, 102400\n"
    "s_add_u32 s67, s75, 131072\n"
    "buffer_load_dwordx4 v248, s[44:47], s67 offen lds\n"
    "s_mov_b32 m0, 103296\n"
    "s_nop 0\n"
    "buffer_load_dwordx4 v248, s[44:47], s67 offen offset:128 lds\n"
    "s_mov_b32 m0, 104448\n"
    "s_add_u32 s67, s75, 196608\n"
    "buffer_load_dwordx4 v248, s[44:47], s67 offen lds\n"
    "s_mov_b32 m0, 105344\n"
    "s_nop 0\n"
    "buffer_load_dwordx4 v248, s[44:47], s67 offen offset:128 lds\n"
    "s_mov_b32 m0, 106496\n"
    "s_add_u32 s67, s75, 262144\n"
    "buffer_load_dwordx4 v248, s[44:47], s67 offen lds\n"
    "s_mov_b32 m0, 107392\n"
    "s_nop 0\n"
    "buffer_load_dwordx4 v248, s[44:47], s67 offen offset:128 lds\n"
    "s_mov_b32 m0, 108544\n"
    "s_add_u32 s67, s75, 327680\n"
    "buffer_load_dwordx4 v248, s[44:47], s67 offen lds\n"
    "s_mov_b32 m0, 109440\n"
    "s_nop 0\n"
    "buffer_load_dwordx4 v248, s[44:47], s67 offen offset:128 lds\n"
    "s_mov_b32 m0, 110592\n"
    "s_add_u32 s67, s75, 393216\n"
    "buffer_load_dwordx4 v248, s[44:47], s67 offen lds\n"
    "s_mov_b32 m0, 111488\n"
    "s_nop 0\n"
    "buffer_load_dwordx4 v248, s[44:47], s67 offen offset:128 lds\n"
    "s_mov_b32 m0, 112640\n"
    "s_add_u32 s67, s75, 458752\n"
    "buffer_load_dwordx4 v248, s[44:47], s67 offen lds\n"
    "s_mov_b32 m0, 113536\n"
    "s_nop 0\n"
    "buffer_load_dwordx4 v248, s[44:47], s67 offen offset:128 lds\n"
    "s_mov_b32 m0, 114688\n"
    "s_add_u32 s67, s75, 524288\n"
    "buffer_load_dwordx4 v248, s[44:47], s67 offen lds\n"
    "s_mov_b32 m0, 115584\n"
    "s_nop 0\n"
    "buffer_load_dwordx4 v248, s[44:47], s67 offen offset:128 lds\n"
    "s_mov_b32 m0, 116736\n"
    "s_add_u32 s67, s75, 589824\n"
    "buffer_load_dwordx4 v248, s[44:47], s67 offen lds\n"
    "s_mov_b32 m0, 117632\n"
    "s_nop 0\n"
    "buffer_load_dwordx4 v248, s[44:47], s67 offen offset:128 lds\n"
    "s_mov_b32 m0, 118784\n"
    "s_add_u32 s67, s75, 655360\n"
    "buffer_load_dwordx4 v248, s[44:47], s67 offen lds\n"
    "s_mov_b32 m0, 119680\n"
    "s_nop 0\n"
    "buffer_load_dwordx4 v248, s[44:47], s67 offen offset:128 lds\n"
    "s_mov_b32 m0, 120832\n"
    "s_add_u32 s67, s75, 720896\n"
    "buffer_load_dwordx4 v248, s[44:47], s67 offen lds\n"
    "s_mov_b32 m0, 121728\n"
    "s_nop 0\n"
    "buffer_load_dwordx4 v248, s[44:47], s67 offen offset:128 lds\n"
    "s_mov_b32 m0, 122880\n"
    "s_add_u32 s67, s75, 786432\n"
    "buffer_load_dwordx4 v248, s[44:47], s67 offen lds\n"
    "s_mov_b32 m0, 123776\n"
    "s_nop 0\n"
    "buffer_load_dwordx4 v248, s[44:47], s67 offen offset:128 lds\n"
    "s_mov_b32 m0, 124928\n"
    "s_add_u32 s67, s75, 851968\n"
    "buffer_load_dwordx4 v248, s[44:47], s67 offen lds\n"
    "s_mov_b32 m0, 125824\n"
    "s_nop 0\n"
    "buffer_load_dwordx4 v248, s[44:47], s67 offen offset:128 lds\n"
    "s_mov_b32 m0, 126976\n"
    "s_add_u32 s67, s75, 917504\n"
    "buffer_load_dwordx4 v248, s[44:47], s67 offen lds\n"
    "s_mov_b32 m0, 127872\n"
    "s_nop 0\n"
    "buffer_load_dwordx4 v248, s[44:47], s67 offen offset:128 lds\n"
    "s_mov_b32 m0, 129024\n"
    "s_add_u32 s67, s75, 983040\n"
    "buffer_load_dwordx4 v248, s[44:47], s67 offen lds\n"
    "s_mov_b32 m0, 129920\n"
    "s_nop 0\n"
    "buffer_load_dwordx4 v248, s[44:47], s67 offen offset:128 lds\n"
    "s_barrier\n"
    "s_add_u32 s85, s76, s83\n"
    "buffer_load_dword v253, v251, s[44:47], s85 offen\n"
    "s_add_u32 s85, s65, s84\n"
    "buffer_load_dword v253, v252, s[48:51], s85 offen\n"
    "s_add_u32 m0, s61, 0\n"
    "s_add_u32 s66, s62, 32768\n"
    "buffer_load_dwordx4 v244, s[36:39], s66 offen nt lds\n"
    "buffer_load_dwordx4 v245, s[36:39], s66 offen offset:2048 nt lds\n"
    "s_add_u32 m0, s61, 896\n"
    "s_nop 0\n"
    "buffer_load_dwordx4 v244, s[36:39], s66 offen offset:128 nt lds\n"
    "buffer_load_dwordx4 v245, s[36:39], s66 offen offset:2176 nt lds\n"
    "s_waitcnt vmcnt(46)\n"
    "s_barrier\n"
    "s_add_u32 m0, s61, 49152\n"
    "s_add_u32 s66, s62, 16384\n"
    "buffer_load_dwordx4 v246, s[40:43], s66 offen nt lds\n"
    "buffer_load_dwordx4 v246, s[40:43], s66 offen offset:2048 nt lds\n"
    "s_add_u32 m0, s61, 50048\n"
    "s_nop 0\n"
    "buffer_load_dwordx4 v246, s[40:43], s66 offen offset:128 nt lds\n"
    "buffer_load_dwordx4 v246, s[40:43], s66 offen offset:2176 nt lds\n"
    "s_waitcnt vmcnt(34)\n"
    "ds_read_b64_tr_b16 v[192:193], v240 offset:0\n"
    "ds_read_b64_tr_b16 v[194:195], v240 offset:256\n"
    "ds_read_b64_tr_b16 v[196:197], v240 offset:512\n"
    "ds_read_b64_tr_b16 v[198:199], v240 offset:768\n"
    "ds_read_b64_tr_b16 v[200:201], v240 offset:1024\n"
    "ds_read_b64_tr_b16 v[202:203], v240 offset:1280\n"
    "ds_read_b64_tr_b16 v[204:205], v240 offset:1536\n"
    "ds_read_b64_tr_b16 v[206:207], v240 offset:1792\n"
    "ds_read_b128 v[224:227], v241 offset:0\n"
    "s_waitcnt lgkmcnt(0)\n"
    "v_mfma_f32_32x32x16_bf16 v[0:15], v[192:195], a[0:3], 0\n"
    "ds_read_b64_tr_b16 v[208:209], v240 offset:4096\n"
    "v_mfma_f32_32x32x16_bf16 v[16:31], v[192:195], a[128:131], 0\n"
    "ds_read_b64_tr_b16 v[210:211], v240 offset:4352\n"
    "v_mfma_f32_32x32x16_bf16 v[32:47], v[192:195], v[224:227], 0\n"
    "ds_read_b64_tr_b16 v[212:213], v240 offset:4608\n"
    "v_mfma_f32_32x32x16_bf16 v[48:63], v[196:199], a[0:3], 0\n"
    "ds_read_b64_tr_b16 v[214:215], v240 offset:4864\n"
    "v_mfma_f32_32x32x16_bf16 v[64:79], v[196:199], a[128:131], 0\n"
    "ds_read_b64_tr_b16 v[216:217], v240 offset:5120\n"
    "v_mfma_f32_32x32x16_bf16 v[80:95], v[196:199], v[224:227], 0\n"
    "ds_read_b64_tr_b16 v[218:219], v240 offset:5376\n"
    "v_mfma_f32_32x32x16_bf16 v[96:111], v[200:203], a[0:3], 0\n"
    "ds_read_b64_tr_b16 v[220:221], v240 offset:5632\n"
    "v_mfma_f32_32x32x16_bf16 v[112:127], v[200:203], a[128:131], 0\n"
    "ds_read_b64_tr_b16 v[222:223], v240 offset:5888\n"
    "v_mfma_f32_32x32x16_bf16 v[128:143], v[200:203], v[224:227], 0\n"
    "ds_read_b128 v[228:231], v242 offset:0\n"
    "v_mfma_f32_32x32x16_bf16 v[144:159], v[204:207], a[0:3], 0\n"
    "v_mfma_f32_32x32x16_bf16 v[160:175], v[204:207], a[128:131], 0\n"
    "v_mfma_f32_32x32x16_bf16 v[176:191], v[204:207], v[224:227], 0\n"
    "s_waitcnt lgkmcnt(0)\n"
    "v_mfma_f32_32x32x16_bf16 v[0:15], v[208:211], a[4:7], v[0:15]\n"
    "s_waitcnt vmcnt(26)\n"
    "ds_read_b64_tr_b16 v[192:193], v240 offset:8192\n"
    "v_mfma_f32_32x32x16_bf16 v[16:31], v[208:211], a[132:135], v[16:31]\n"
    "ds_read_b64_tr_b16 v[194:195], v240 offset:8448\n"
    "s_mov_b32 m0, 98304\n"
    "s_add_u32 s67, s75, 1048576\n"
    "buffer_load_dwordx4 v248, s[44:47], s67 offen lds\n"
    "v_mfma_f32_32x32x16_bf16 v[32:47], v[208:211], v[228:231], v[32:47]\n"
    "ds_read_b64_tr_b16 v[196:197], v240 offset:8704\n"
    "s_mov_b32 m0, 99200\n"
    "s_nop 0\n"
    "buffer_load_dwordx4 v248, s[44:47], s67 offen offset:128 lds\n"
    "v_mfma_f32_32x32x16_bf16 v[48:63], v[212:215], a[4:7], v[48:63]\n"
    "ds_read_b64_tr_b16 v[198:199], v240 offset:8960\n"
    "s_mov_b32 m0, 100352\n"
    "s_add_u32 s67, s75, 1114112\n"
    "buffer_load_dwordx4 v248, s[44:47], s67 offen lds\n"
    "v_mfma_f32_32x32x16_bf16 v[64:79], v[212:215], a[132:135], v[64:79]\n"
    "ds_read_b64_tr_b16 v[200:201], v240 offset:9216\n"
    "s_mov_b32 m0, 101248\n"
    "s_nop 0\n"
    "buffer_load_dwordx4 v248, s[44:47], s67 offen offset:128 lds\n"
    "v_mfma_f32_32x32x16_bf16 v[80:95], v[212:215], v[228:231], v[80:95]\n"
    "ds_read_b64_tr_b16 v[202:203], v240 offset:9472\n"
    "s_mov_b32 m0, 102400\n"
    "s_add_u32 s67, s75, 1179648\n"
    "buffer_load_dwordx4 v248, s[44:47], s67 offen lds\n"
    "v_mfma_f32_32x32x16_bf16 v[96:111], v[216:219], a[4:7], v[96:111]\n"
    "ds_read_b64_tr_b16 v[204:205], v240 offset:9728\n"
    "s_mov_b32 m0, 103296\n"
    "s_nop 0\n"
    "buffer_load_dwordx4 v248, s[44:47], s67 offen offset:128 lds\n"
    "v_mfma_f32_32x32x16_bf16 v[112:127], v[216:219], a[132:135], v[112:127]\n"
    "ds_read_b64_tr_b16 v[206:207], v240 offset:9984\n"
    "s_mov_b32 m0, 104448\n"
    "s_add_u32 s67, s75, 1245184\n"
    "buffer_load_dwordx4 v248, s[44:47], s67 offen lds\n"
    "v_mfma_f32_32x32x16_bf16 v[128:143], v[216:219], v[228:231], v[128:143]\n"
    "ds_read_b128 v[224:227], v241 offset:512\n"
    "s_mov_b32 m0, 105344\n"
    "s_nop 0\n"
    "buffer_load_dwordx4 v248, s[44:47], s67 offen offset:128 lds\n"
    "v_mfma_f32_32x32x16_bf16 v[144:159], v[220:223], a[4:7], v[144:159]\n"
    "v_mfma_f32_32x32x16_bf16 v[160:175], v[220:223], a[132:135], v[160:175]\n"
    "v_mfma_f32_32x32x16_bf16 v[176:191], v[220:223], v[228:231], v[176:191]\n"
    "s_waitcnt lgkmcnt(0)\n"
    "v_mfma_f32_32x32x16_bf16 v[0:15], v[192:195], a[8:11], v[0:15]\n"
    "ds_read_b64_tr_b16 v[208:209], v240 offset:12288\n"
    "v_mfma_f32_32x32x16_bf16 v[16:31], v[192:195], a[136:139], v[16:31]\n"
    "ds_read_b64_tr_b16 v[210:211], v240 offset:12544\n"
    "v_mfma_f32_32x32x16_bf16 v[32:47], v[192:195], v[224:227], v[32:47]\n"
    "ds_read_b64_tr_b16 v[212:213], v240 offset:12800\n"
    "v_mfma_f32_32x32x16_bf16 v[48:63], v[196:199], a[8:11], v[48:63]\n"
    "ds_read_b64_tr_b16 v[214:215], v240 offset:13056\n"
    "v_mfma_f32_32x32x16_bf16 v[64:79], v[196:199], a[136:139], v[64:79]\n"
    "ds_read_b64_tr_b16 v[216:217], v240 offset:13312\n"
    "v_mfma_f32_32x32x16_bf16 v[80:95], v[196:199], v[224:227], v[80:95]\n"
    "ds_read_b64_tr_b16 v[218:219], v240 offset:13568\n"
    "v_mfma_f32_32x32x16_bf16 v[96:111], v[200:203], a[8:11], v[96:111]\n"
    "ds_read_b64_tr_b16 v[220:221], v240 offset:13824\n"
    "v_mfma_f32_32x32x16_bf16 v[112:127], v[200:203], a[136:139], v[112:127]\n"
    "ds_read_b64_tr_b16 v[222:223], v240 offset:14080\n"
    "v_mfma_f32_32x32x16_bf16 v[128:143], v[200:203], v[224:227], v[128:143]\n"
    "ds_read_b128 v[228:231], v242 offset:512\n"
    "v_mfma_f32_32x32x16_bf16 v[144:159], v[204:207], a[8:11], v[144:159]\n"
    "v_mfma_f32_32x32x16_bf16 v[160:175], v[204:207], a[136:139], v[160:175]\n"
    "v_mfma_f32_32x32x16_bf16 v[176:191], v[204:207], v[224:227], v[176:191]\n"
    "s_waitcnt lgkmcnt(0)\n"
    "v_mfma_f32_32x32x16_bf16 v[0:15], v[208:211], a[12:15], v[0:15]\n"
    "s_waitcnt vmcnt(26)\n"
    "ds_read_b64_tr_b16 v[192:193], v240 offset:16384\n"
    "v_mfma_f32_32x32x16_bf16 v[16:31], v[208:211], a[140:143], v[16:31]\n"
    "ds_read_b64_tr_b16 v[194:195], v240 offset:16640\n"
    "s_mov_b32 m0, 106496\n"
    "s_add_u32 s67, s75, 1310720\n"
    "buffer_load_dwordx4 v248, s[44:47], s67 offen lds\n"
    "v_mfma_f32_32x32x16_bf16 v[32:47], v[208:211], v[228:231], v[32:47]\n"
    "ds_read_b64_tr_b16 v[196:197], v240 offset:16896\n"
    "s_mov_b32 m0, 107392\n"
    "s_nop 0\n"
    "buffer_load_dwordx4 v248, s[44:47], s67 offen offset:128 lds\n"
    "v_mfma_f32_32x32x16_bf16 v[48:63], v[212:215], a[12:15], v[48:63]\n"
    "ds_read_b64_tr_b16 v[198:199], v240 offset:17152\n"
    "s_mov_b32 m0, 108544\n"
    "s_add_u32 s67, s75, 1376256\n"
    "buffer_load_dwordx4 v248, s[44:47], s67 offen lds\n"
    "v_mfma_f32_32x32x16_bf16 v[64:79], v[212:215], a[140:143], v[64:79]\n"
    "ds_read_b64_tr_b16 v[200:201], v240 offset:17408\n"
    "s_mov_b32 m0, 109440\n"
    "s_nop 0\n"
    "buffer_load_dwordx4 v248, s[44:47], s67 offen offset:128 lds\n"
    "v_mfma_f32_32x32x16_bf16 v[80:95], v[212:215], v[228:231], v[80:95]\n"
    "ds_read_b64_tr_b16 v[202:203], v240 offset:17664\n"
    "s_mov_b32 m0, 110592\n"
    "s_add_u32 s67, s75, 1441792\n"
    "buffer_load_dwordx4 v248, s[44:47], s67 offen lds\n"
    "v_mfma_f32_32x32x16_bf16 v[96:111], v[216:219], a[12:15], v[96:111]\n"
    "ds_read_b64_tr_b16 v[204:205], v240 offset:17920\n"
    "s_mov_b32 m0, 111488\n"
    "s_nop 0\n"
    "buffer_load_dwordx4 v248, s[44:47], s67 offen offset:128 lds\n"
    "v_mfma_f32_32x32x16_bf16 v[112:127], v[216:219], a[140:143], v[112:127]\n"
    "ds_read_b64_tr_b16 v[206:207], v240 offset:18176\n"
    "s_mov_b32 m0, 112640\n"
    "s_add_u32 s67, s75, 1507328\n"
    "buffer_load_dwordx4 v248, s[44:47], s67 offen lds\n"
    "v_mfma_f32_32x32x16_bf16 v[128:143], v[216:219], v[228:231], v[128:143]\n"
    "ds_read_b128 v[224:227], v241 offset:4096\n"
    "s_mov_b32 m0, 113536\n"
    "s_nop 0\n"
    "buffer_load_dwordx4 v248, s[44:47], s67 offen offset:128 lds\n"
    "v_mfma_f32_32x32x16_bf16 v[144:159], v[220:223], a[12:15], v[144:159]\n"
    "s_barrier\n"
    "s_mov_b32 m0, 155648\n"
    "s_mov_b32 s67, 65792\n"
    "buffer_load_dwordx4 v249, s[52:55], s67 offen lds\n"
    "v_mfma_f32_32x32x16_bf16 v[160:175], v[220:223], a[140:143], v[160:175]\n"
    "s_mov_b32 m0, 156672\n"
    "s_mov_b32 s67, 73984\n"
    "buffer_load_dwordx4 v250, s[52:55], s67 offen lds\n"
    "v_mfma_f32_32x32x16_bf16 v[176:191], v[220:223], v[228:231], v[176:191]\n"
    "s_mov_b32 m0, 157696\n"
    "s_mov_b32 s67, 82176\n"
    "buffer_load_dwordx4 v249, s[52:55], s67 offen lds\n"
    "s_mov_b32 m0, 158720\n"
    "s_mov_b32 s67, 90368\n"
    "buffer_load_dwordx4 v250, s[52:55], s67 offen lds\n"
    "s_waitcnt lgkmcnt(0)\n"
    "v_mfma_f32_32x32x16_bf16 v[0:15], v[192:195], a[16:19], v[0:15]\n"
    "ds_read_b64_tr_b16 v[208:209], v240 offset:20480\n"
    "v_mfma_f32_32x32x16_bf16 v[16:31], v[192:195], a[144:147], v[16:31]\n"
    "ds_read_b64_tr_b16 v[210:211], v240 offset:20736\n"
    "v_mfma_f32_32x32x16_bf16 v[32:47], v[192:195], v[224:227], v[32:47]\n"
    "ds_read_b64_tr_b16 v[212:213], v240 offset:20992\n"
    "v_mfma_f32_32x32x16_bf16 v[48:63], v[196:199], a[16:19], v[48:63]\n"
    "ds_read_b64_tr_b16 v[214:215], v240 offset:21248\n"
    "v_mfma_f32_32x32x16_bf16 v[64:79], v[196:199], a[144:147], v[64:79]\n"
    "ds_read_b64_tr_b16 v[216:217], v240 offset:21504\n"
    "v_mfma_f32_32x32x16_bf16 v[80:95], v[196:199], v[224:227], v[80:95]\n"
    "ds_read_b64_tr_b16 v[218:219], v240 offset:21760\n"
    "v_mfma_f32_32x32x16_bf16 v[96:111], v[200:203], a[16:19], v[96:111]\n"
    "ds_read_b64_tr_b16 v[220:221], v240 offset:22016\n"
    "v_mfma_f32_32x32x16_bf16 v[112:127], v[200:203], a[144:147], v[112:127]\n"
    "ds_read_b64_tr_b16 v[222:223], v240 offset:22272\n"
    "v_mfma_f32_32x32x16_bf16 v[128:143], v[200:203], v[224:227], v[128:143]\n"
    "ds_read_b128 v[228:231], v242 offset:4096\n"
    "v_mfma_f32_32x32x16_bf16 v[144:159], v[204:207], a[16:19], v[144:159]\n"
    "v_mfma_f32_32x32x16_bf16 v[160:175], v[204:207], a[144:147], v[160:175]\n"
    "v_mfma_f32_32x32x16_bf16 v[176:191], v[204:207], v[224:227], v[176:191]\n"
    "s_waitcnt lgkmcnt(0)\n"
    "v_mfma_f32_32x32x16_bf16 v[0:15], v[208:211], a[20:23], v[0:15]\n"
    "s_waitcnt vmcnt(30)\n"
    "ds_read_b64_tr_b16 v[192:193], v240 offset:24576\n"
    "v_mfma_f32_32x32x16_bf16 v[16:31], v[208:211], a[148:151], v[16:31]\n"
    "ds_read_b64_tr_b16 v[194:195], v240 offset:24832\n"
    "s_mov_b32 m0, 114688\n"
    "s_add_u32 s67, s75, 1572864\n"
    "buffer_load_dwordx4 v248, s[44:47], s67 offen lds\n"
    "v_mfma_f32_32x32x16_bf16 v[32:47], v[208:211], v[228:231], v[32:47]\n"
    "ds_read_b64_tr_b16 v[196:197], v240 offset:25088\n"
    "s_mov_b32 m0, 115584\n"
    "s_nop 0\n"
    "buffer_load_dwordx4 v248, s[44:47], s67 offen offset:128 lds\n"
    "v_mfma_f32_32x32x16_bf16 v[48:63], v[212:215], a[20:23], v[48:63]\n"
    "ds_read_b64_tr_b16 v[198:199], v240 offset:25344\n"
    "s_mov_b32 m0, 116736\n"
    "s_add_u32 s67, s75, 1638400\n"
    "buffer_load_dwordx4 v248, s[44:47], s67 offen lds\n"
    "v_mfma_f32_32x32x16_bf16 v[64:79], v[212:215], a[148:151], v[64:79]\n"
    "ds_read_b64_tr_b16 v[200:201], v240 offset:25600\n"
    "s_mov_b32 m0, 117632\n"
    "s_nop 0\n"
    "buffer_load_dwordx4 v248, s[44:47], s67 offen offset:128 lds\n"
    "v_mfma_f32_32x32x16_bf16 v[80:95], v[212:215], v[228:231], v[80:95]\n"
    "ds_read_b64_tr_b16 v[202:203], v240 offset:25856\n"
    "s_mov_b32 m0, 118784\n"
    "s_add_u32 s67, s75, 1703936\n"
    "buffer_load_dwordx4 v248, s[44:47], s67 offen lds\n"
    "v_mfma_f32_32x32x16_bf16 v[96:111], v[216:219], a[20:23], v[96:111]\n"
    "ds_read_b64_tr_b16 v[204:205], v240 offset:26112\n"
    "s_mov_b32 m0, 119680\n"
    "s_nop 0\n"
    "buffer_load_dwordx4 v248, s[44:47], s67 offen offset:128 lds\n"
    "v_mfma_f32_32x32x16_bf16 v[112:127], v[216:219], a[148:151], v[112:127]\n"
    "s_barrier\n"
    "ds_read_b64_tr_b16 v[206:207], v240 offset:26368\n"
    "s_mov_b32 m0, 120832\n"
    "s_add_u32 s67, s75, 1769472\n"
    "buffer_load_dwordx4 v248, s[44:47], s67 offen lds\n"
    "v_mfma_f32_32x32x16_bf16 v[128:143], v[216:219], v[228:231], v[128:143]\n"
    "ds_read_b128 v[224:227], v241 offset:4608\n"
    "s_mov_b32 m0, 121728\n"
    "s_nop 0\n"
    "buffer_load_dwordx4 v248, s[44:47], s67 offen offset:128 lds\n"
    "v_mfma_f32_32x32x16_bf16 v[144:159], v[220:223], a[20:23], v[144:159]\n"
    "s_add_u32 m0, s61, 16384\n"
    "s_add_u32 s66, s62, 49152\n"
    "buffer_load_dwordx4 v244, s[36:39], s66 offen nt lds\n"
    "buffer_load_dwordx4 v245, s[36:39], s66 offen offset:2048 nt lds\n"
    "v_mfma_f32_32x32x16_bf16 v[160:175], v[220:223], a[148:151], v[160:175]\n"
    "s_add_u32 m0, s61, 17280\n"
    "s_nop 0\n"
    "buffer_load_dwordx4 v244, s[36:39], s66 offen offset:128 nt lds\n"
    "buffer_load_dwordx4 v245, s[36:39], s66 offen offset:2176 nt lds\n"
    "v_mfma_f32_32x32x16_bf16 v[176:191], v[220:223], v[228:231], v[176:191]\n"
    "s_waitcnt lgkmcnt(0)\n"
    "v_mfma_f32_32x32x16_bf16 v[0:15], v[192:195], a[24:27], v[0:15]\n"
    "ds_read_b64_tr_b16 v[208:209], v240 offset:28672\n"
    "v_mfma_f32_32x32x16_bf16 v[16:31], v[192:195], a[152:155], v[16:31]\n"
    "ds_read_b64_tr_b16 v[210:211], v240 offset:28928\n"
    "v_mfma_f32_32x32x16_bf16 v[32:47], v[192:195], v[224:227], v[32:47]\n"
    "ds_read_b64_tr_b16 v[212:213], v240 offset:29184\n"
    "v_mfma_f32_32x32x16_bf16 v[48:63], v[196:199], a[24:27], v[48:63]\n"
    "ds_read_b64_tr_b16 v[214:215], v240 offset:29440\n"
    "v_mfma_f32_32x32x16_bf16 v[64:79], v[196:199], a[152:155], v[64:79]\n"
    "ds_read_b64_tr_b16 v[216:217], v240 offset:29696\n"
    "v_mfma_f32_32x32x16_bf16 v[80:95], v[196:199], v[224:227], v[80:95]\n"
    "ds_read_b64_tr_b16 v[218:219], v240 offset:29952\n"
    "v_mfma_f32_32x32x16_bf16 v[96:111], v[200:203], a[24:27], v[96:111]\n"
    "ds_read_b64_tr_b16 v[220:221], v240 offset:30208\n"
    "v_mfma_f32_32x32x16_bf16 v[112:127], v[200:203], a[152:155], v[112:127]\n"
    "ds_read_b64_tr_b16 v[222:223], v240 offset:30464\n"
    "v_mfma_f32_32x32x16_bf16 v[128:143], v[200:203], v[224:227], v[128:143]\n"
    "ds_read_b128 v[228:231], v242 offset:4608\n"
    "v_mfma_f32_32x32x16_bf16 v[144:159], v[204:207], a[24:27], v[144:159]\n"
    "s_waitcnt vmcnt(36)\n"
    "s_barrier\n"
    "s_add_u32 m0, s61, 32768\n"
    "s_add_u32 s66, s62, 32768\n"
    "buffer_load_dwordx4 v246, s[40:43], s66 offen nt lds\n"
    "buffer_load_dwordx4 v246, s[40:43], s66 offen offset:2048 nt lds\n"
    "v_mfma_f32_32x32x16_bf16 v[160:175], v[204:207], a[152:155], v[160:175]\n"
    "s_add_u32 m0, s61, 33664\n"
    "s_nop 0\n"
    "buffer_load_dwordx4 v246, s[40:43], s66 offen offset:128 nt lds\n"
    "buffer_load_dwordx4 v246, s[40:43], s66 offen offset:2176 nt lds\n"
    "v_mfma_f32_32x32x16_bf16 v[176:191], v[204:207], v[224:227], v[176:191]\n"
    "s_waitcnt lgkmcnt(0)\n"
    "v_mfma_f32_32x32x16_bf16 v[0:15], v[208:211], a[28:31], v[0:15]\n"
    "s_waitcnt vmcnt(28)\n"
    "s_waitcnt vmcnt(16)\n"
    "ds_read_b64_tr_b16 v[192:193], v240 offset:0\n"
    "v_mfma_f32_32x32x16_bf16 v[16:31], v[208:211], a[156:159], v[16:31]\n"
    "ds_read_b64_tr_b16 v[194:195], v240 offset:256\n"
    "s_mov_b32 m0, 122880\n"
    "s_add_u32 s67, s75, 1835008\n"
    "buffer_load_dwordx4 v248, s[44:47], s67 offen lds\n"
    "v_mfma_f32_32x32x16_bf16 v[32:47], v[208:211], v[228:231], v[32:47]\n"
    "ds_read_b64_tr_b16 v[196:197], v240 offset:512\n"
    "s_mov_b32 m0, 123776\n"
    "s_nop 0\n"
    "buffer_load_dwordx4 v248, s[44:47], s67 offen offset:128 lds\n"
    "v_mfma_f32_32x32x16_bf16 v[48:63], v[212:215], a[28:31], v[48:63]\n"
    "ds_read_b64_tr_b16 v[198:199], v240 offset:768\n"
    "s_mov_b32 m0, 124928\n"
    "s_add_u32 s67, s75, 1900544\n"
    "buffer_load_dwordx4 v248, s[44:47], s67 offen lds\n"
    "v_mfma_f32_32x32x16_bf16 v[64:79], v[212:215], a[156:159], v[64:79]\n"
    "ds_read_b64_tr_b16 v[200:201], v240 offset:1024\n"
    "s_mov_b32 m0, 125824\n"
    "s_nop 0\n"
    "buffer_load_dwordx4 v248, s[44:47], s67 offen offset:128 lds\n"
    "v_mfma_f32_32x32x16_bf16 v[80:95], v[212:215], v[228:231], v[80:95]\n"
    "ds_read_b64_tr_b16 v[202:203], v240 offset:1280\n"
    "s_mov_b32 m0, 126976\n"
    "s_add_u32 s67, s75, 1966080\n"
    "buffer_load_dwordx4 v248, s[44:47], s67 offen lds\n"
    "v_mfma_f32_32x32x16_bf16 v[96:111], v[216:219], a[28:31], v[96:111]\n"
    "ds_read_b64_tr_b16 v[204:205], v240 offset:1536\n"
    "s_mov_b32 m0, 127872\n"
    "s_nop 0\n"
    "buffer_load_dwordx4 v248, s[44:47], s67 offen offset:128 lds\n"
    "v_mfma_f32_32x32x16_bf16 v[112:127], v[216:219], a[156:159], v[112:127]\n"
    "ds_read_b64_tr_b16 v[206:207], v240 offset:1792\n"
    "s_mov_b32 m0, 129024\n"
    "s_add_u32 s67, s75, 2031616\n"
    "buffer_load_dwordx4 v248, s[44:47], s67 offen lds\n"
    "v_mfma_f32_32x32x16_bf16 v[128:143], v[216:219], v[228:231], v[128:143]\n"
    "ds_read_b128 v[224:227], v241 offset:0\n"
    "s_mov_b32 m0, 129920\n"
    "s_nop 0\n"
    "buffer_load_dwordx4 v248, s[44:47], s67 offen offset:128 lds\n"
    "v_mfma_f32_32x32x16_bf16 v[144:159], v[220:223], a[28:31], v[144:159]\n"
    "s_mov_b32 m0, 159744\n"
    "s_mov_b32 s67, 65920\n"
    "buffer_load_dwordx4 v249, s[52:55], s67 offen lds\n"
    "v_mfma_f32_32x32x16_bf16 v[160:175], v[220:223], a[156:159], v[160:175]\n"
    "s_mov_b32 m0, 160768\n"
    "s_mov_b32 s67, 74112\n"
    "buffer_load_dwordx4 v250, s[52:55], s67 offen lds\n"
    "v_mfma_f32_32x32x16_bf16 v[176:191], v[220:223], v[228:231], v[176:191]\n"
    "s_mov_b32 m0, 161792\n"
    "s_mov_b32 s67, 82304\n"
    "buffer_load_dwordx4 v249, s[52:55], s67 offen lds\n"
    "s_mov_b32 m0, 162816\n"
    "s_mov_b32 s67, 90496\n"
    "buffer_load_dwordx4 v250, s[52:55], s67 offen lds\n"
    "s_waitcnt lgkmcnt(0)\n"
    "v_mfma_f32_32x32x16_bf16 v[0:15], v[192:195], a[32:35], v[0:15]\n"
    "ds_read_b64_tr_b16 v[208:209], v240 offset:4096\n"
    "v_mfma_f32_32x32x16_bf16 v[16:31], v[192:195], a[160:163], v[16:31]\n"
    "ds_read_b64_tr_b16 v[210:211], v240 offset:4352\n"
    "v_mfma_f32_32x32x16_bf16 v[32:47], v[192:195], v[224:227], v[32:47]\n"
    "ds_read_b64_tr_b16 v[212:213], v240 offset:4608\n"
    "v_mfma_f32_32x32x16_bf16 v[48:63], v[196:199], a[32:35], v[48:63]\n"
    "ds_read_b64_tr_b16 v[214:215], v240 offset:4864\n"
    "v_mfma_f32_32x32x16_bf16 v[64:79], v[196:199], a[160:163], v[64:79]\n"
    "ds_read_b64_tr_b16 v[216:217], v240 offset:5120\n"
    "v_mfma_f32_32x32x16_bf16 v[80:95], v[196:199], v[224:227], v[80:95]\n"
    "ds_read_b64_tr_b16 v[218:219], v240 offset:5376\n"
    "v_mfma_f32_32x32x16_bf16 v[96:111], v[200:203], a[32:35], v[96:111]\n"
    "ds_read_b64_tr_b16 v[220:221], v240 offset:5632\n"
    "v_mfma_f32_32x32x16_bf16 v[112:127], v[200:203], a[160:163], v[112:127]\n"
    "s_barrier\n"
    "ds_read_b64_tr_b16 v[222:223], v240 offset:5888\n"
    "v_mfma_f32_32x32x16_bf16 v[128:143], v[200:203], v[224:227], v[128:143]\n"
    "ds_read_b128 v[228:231], v242 offset:0\n"
    "v_mfma_f32_32x32x16_bf16 v[144:159], v[204:207], a[32:35], v[144:159]\n"
    "v_mfma_f32_32x32x16_bf16 v[160:175], v[204:207], a[160:163], v[160:175]\n"
    "v_mfma_f32_32x32x16_bf16 v[176:191], v[204:207], v[224:227], v[176:191]\n"
    "s_waitcnt lgkmcnt(0)\n"
    "v_mfma_f32_32x32x16_bf16 v[0:15], v[208:211], a[36:39], v[0:15]\n"
    "ds_read_b64_tr_b16 v[192:193], v240 offset:8192\n"
    "v_mfma_f32_32x32x16_bf16 v[16:31], v[208:211], a[164:167], v[16:31]\n"
    "ds_read_b64_tr_b16 v[194:195], v240 offset:8448\n"
    "s_mov_b32 m0, 98304\n"
    "s_add_u32 s67, s75, 2097152\n"
    "buffer_load_dwordx4 v248, s[44:47], s67 offen lds\n"
    "v_mfma_f32_32x32x16_bf16 v[32:47], v[208:211], v[228:231], v[32:47]\n"
    "ds_read_b64_tr_b16 v[196:197], v240 offset:8704\n"
    "s_mov_b32 m0, 99200\n"
    "s_nop 0\n"
    "buffer_load_dwordx4 v248, s[44:47], s67 offen offset:128 lds\n"
    "v_mfma_f32_32x32x16_bf16 v[48:63], v[212:215], a[36:39], v[48:63]\n"
    "ds_read_b64_tr_b16 v[198:199], v240 offset:8960\n"
    "s_mov_b32 m0, 100352\n"
    "s_add_u32 s67, s75, 2162688\n"
    "buffer_load_dwordx4 v248, s[44:47], s67 offen lds\n"
    "v_mfma_f32_32x32x16_bf16 v[64:79], v[212:215], a[164:167], v[64:79]\n"
    "ds_read_b64_tr_b16 v[200:201], v240 offset:9216\n"
    "s_mov_b32 m0, 101248\n"
    "s_nop 0\n"
    "buffer_load_dwordx4 v248, s[44:47], s67 offen offset:128 lds\n"
    "v_mfma_f32_32x32x16_bf16 v[80:95], v[212:215], v[228:231], v[80:95]\n"
    "ds_read_b64_tr_b16 v[202:203], v240 offset:9472\n"
    "s_mov_b32 m0, 102400\n"
    "s_add_u32 s67, s75, 2228224\n"
    "buffer_load_dwordx4 v248, s[44:47], s67 offen lds\n"
    "v_mfma_f32_32x32x16_bf16 v[96:111], v[216:219], a[36:39], v[96:111]\n"
    "ds_read_b64_tr_b16 v[204:205], v240 offset:9728\n"
    "s_mov_b32 m0, 103296\n"
    "s_nop 0\n"
    "buffer_load_dwordx4 v248, s[44:47], s67 offen offset:128 lds\n"
    "v_mfma_f32_32x32x16_bf16 v[112:127], v[216:219], a[164:167], v[112:127]\n"
    "ds_read_b64_tr_b16 v[206:207], v240 offset:9984\n"
    "s_mov_b32 m0, 104448\n"
    "s_add_u32 s67, s75, 2293760\n"
    "buffer_load_dwordx4 v248, s[44:47], s67 offen lds\n"
    "v_mfma_f32_32x32x16_bf16 v[128:143], v[216:219], v[228:231], v[128:143]\n"
    "ds_read_b128 v[224:227], v241 offset:512\n"
    "s_mov_b32 m0, 105344\n"
    "s_nop 0\n"
    "buffer_load_dwordx4 v248, s[44:47], s67 offen offset:128 lds\n"
    "v_mfma_f32_32x32x16_bf16 v[144:159], v[220:223], a[36:39], v[144:159]\n"
    "v_mfma_f32_32x32x16_bf16 v[160:175], v[220:223], a[164:167], v[160:175]\n"
    "v_mfma_f32_32x32x16_bf16 v[176:191], v[220:223], v[228:231], v[176:191]\n"
    "s_waitcnt lgkmcnt(0)\n"
    "v_mfma_f32_32x32x16_bf16 v[0:15], v[192:195], a[40:43], v[0:15]\n"
    "ds_read_b64_tr_b16 v[208:209], v240 offset:12288\n"
    "v_mfma_f32_32x32x16_bf16 v[16:31], v[192:195], a[168:171], v[16:31]\n"
    "ds_read_b64_tr_b16 v[210:211], v240 offset:12544\n"
    "v_mfma_f32_32x32x16_bf16 v[32:47], v[192:195], v[224:227], v[32:47]\n"
    "ds_read_b64_tr_b16 v[212:213], v240 offset:12800\n"
    "v_mfma_f32_32x32x16_bf16 v[48:63], v[196:199], a[40:43], v[48:63]\n"
    "ds_read_b64_tr_b16 v[214:215], v240 offset:13056\n"
    "v_mfma_f32_32x32x16_bf16 v[64:79], v[196:199], a[168:171], v[64:79]\n"
    "ds_read_b64_tr_b16 v[216:217], v240 offset:13312\n"
    "v_mfma_f32_32x32x16_bf16 v[80:95], v[196:199], v[224:227], v[80:95]\n"
    "ds_read_b64_tr_b16 v[218:219], v240 offset:13568\n"
    "v_mfma_f32_32x32x16_bf16 v[96:111], v[200:203], a[40:43], v[96:111]\n"
    "s_barrier\n"
    "ds_read_b64_tr_b16 v[220:221], v240 offset:13824\n"
    "v_mfma_f32_32x32x16_bf16 v[112:127], v[200:203], a[168:171], v[112:127]\n"
    "ds_read_b64_tr_b16 v[222:223], v240 offset:14080\n"
    "s_add_u32 m0, s61, 0\n"
    "s_add_u32 s66, s62, 65536\n"
    "buffer_load_dwordx4 v244, s[36:39], s66 offen nt lds\n"
    "buffer_load_dwordx4 v245, s[36:39], s66 offen offset:2048 nt lds\n"
    "v_mfma_f32_32x32x16_bf16 v[128:143], v[200:203], v[224:227], v[128:143]\n"
    "ds_read_b128 v[228:231], v242 offset:512\n"
    "s_add_u32 m0, s61, 896\n"
    "s_nop 0\n"
    "buffer_load_dwordx4 v244, s[36:39], s66 offen offset:128 nt lds\n"
    "buffer_load_dwordx4 v245, s[36:39], s66 offen offset:2176 nt lds\n"
    "v_mfma_f32_32x32x16_bf16 v[144:159], v[204:207], a[40:43], v[144:159]\n"
    "v_mfma_f32_32x32x16_bf16 v[160:175], v[204:207], a[168:171], v[160:175]\n"
    "v_mfma_f32_32x32x16_bf16 v[176:191], v[204:207], v[224:227], v[176:191]\n"
    "s_waitcnt lgkmcnt(0)\n"
    "v_mfma_f32_32x32x16_bf16 v[0:15], v[208:211], a[44:47], v[0:15]\n"
    "s_waitcnt vmcnt(32)\n"
    "s_waitcnt vmcnt(12)\n"
    "ds_read_b64_tr_b16 v[192:193], v240 offset:16384\n"
    "v_mfma_f32_32x32x16_bf16 v[16:31], v[208:211], a[172:175], v[16:31]\n"
    "ds_read_b64_tr_b16 v[194:195], v240 offset:16640\n"
    "s_mov_b32 m0, 106496\n"
    "s_add_u32 s67, s75, 2359296\n"
    "buffer_load_dwordx4 v248, s[44:47], s67 offen lds\n"
    "v_mfma_f32_32x32x16_bf16 v[32:47], v[208:211], v[228:231], v[32:47]\n"
    "ds_read_b64_tr_b16 v[196:197], v240 offset:16896\n"
    "s_mov_b32 m0, 107392\n"
    "s_nop 0\n"
    "buffer_load_dwordx4 v248, s[44:47], s67 offen offset:128 lds\n"
    "v_mfma_f32_32x32x16_bf16 v[48:63], v[212:215], a[44:47], v[48:63]\n"
    "ds_read_b64_tr_b16 v[198:199], v240 offset:17152\n"
    "s_mov_b32 m0, 108544\n"
    "s_add_u32 s67, s75, 2424832\n"
    "buffer_load_dwordx4 v248, s[44:47], s67 offen lds\n"
    "v_mfma_f32_32x32x16_bf16 v[64:79], v[212:215], a[172:175], v[64:79]\n"
    "ds_read_b64_tr_b16 v[200:201], v240 offset:17408\n"
    "s_mov_b32 m0, 109440\n"
    "s_nop 0\n"
    "buffer_load_dwordx4 v248, s[44:47], s67 offen offset:128 lds\n"
    "v_mfma_f32_32x32x16_bf16 v[80:95], v[212:215], v[228:231], v[80:95]\n"
    "ds_read_b64_tr_b16 v[202:203], v240 offset:17664\n"
    "s_mov_b32 m0, 110592\n"
    "s_add_u32 s67, s75, 2490368\n"
    "buffer_load_dwordx4 v248, s[44:47], s67 offen lds\n"
    "v_mfma_f32_32x32x16_bf16 v[96:111], v[216:219], a[44:47], v[96:111]\n"
    "ds_read_b64_tr_b16 v[204:205], v240 offset:17920\n"
    "s_mov_b32 m0, 111488\n"
    "s_nop 0\n"
    "buffer_load_dwordx4 v248, s[44:47], s67 offen offset:128 lds\n"
    "v_mfma_f32_32x32x16_bf16 v[112:127], v[216:219], a[172:175], v[112:127]\n"
    "ds_read_b64_tr_b16 v[206:207], v240 offset:18176\n"
    "s_mov_b32 m0, 112640\n"
    "s_add_u32 s67, s75, 2555904\n"
    "buffer_load_dwordx4 v248, s[44:47], s67 offen lds\n"
    "v_mfma_f32_32x32x16_bf16 v[128:143], v[216:219], v[228:231], v[128:143]\n"
    "s_barrier\n"
    "ds_read_b128 v[224:227], v241 offset:4096\n"
    "s_mov_b32 m0, 113536\n"
    "s_nop 0\n"
    "buffer_load_dwordx4 v248, s[44:47], s67 offen offset:128 lds\n"
    "v_mfma_f32_32x32x16_bf16 v[144:159], v[220:223], a[44:47], v[144:159]\n"
    "s_mov_b32 m0, 155648\n"
    "s_mov_b32 s67, 66048\n"
    "buffer_load_dwordx4 v249, s[52:55], s67 offen lds\n"
    "v_mfma_f32_32x32x16_bf16 v[160:175], v[220:223], a[172:175], v[160:175]\n"
    "s_mov_b32 m0, 156672\n"
    "s_mov_b32 s67, 74240\n"
    "buffer_load_dwordx4 v250, s[52:55], s67 offen lds\n"
    "v_mfma_f32_32x32x16_bf16 v[176:191], v[220:223], v[228:231], v[176:191]\n"
    "s_mov_b32 m0, 157696\n"
    "s_mov_b32 s67, 82432\n"
    "buffer_load_dwordx4 v249, s[52:55], s67 offen lds\n"
    "s_mov_b32 m0, 158720\n"
    "s_mov_b32 s67, 90624\n"
    "buffer_load_dwordx4 v250, s[52:55], s67 offen lds\n"
    "s_waitcnt lgkmcnt(0)\n"
    "v_mfma_f32_32x32x16_bf16 v[0:15], v[192:195], a[48:51], v[0:15]\n"
    "ds_read_b64_tr_b16 v[208:209], v240 offset:20480\n"
    "s_add_u32 m0, s61, 49152\n"
    "s_add_u32 s66, s62, 49152\n"
    "buffer_load_dwordx4 v246, s[40:43], s66 offen nt lds\n"
    "buffer_load_dwordx4 v246, s[40:43], s66 offen offset:2048 nt lds\n"
    "v_mfma_f32_32x32x16_bf16 v[16:31], v[192:195], a[176:179], v[16:31]\n"
    "ds_read_b64_tr_b16 v[210:211], v240 offset:20736\n"
    "s_add_u32 m0, s61, 50048\n"
    "s_nop 0\n"
    "buffer_load_dwordx4 v246, s[40:43], s66 offen offset:128 nt lds\n"
    "buffer_load_dwordx4 v246, s[40:43], s66 offen offset:2176 nt lds\n"
    "v_mfma_f32_32x32x16_bf16 v[32:47], v[192:195], v[224:227], v[32:47]\n"
    "ds_read_b64_tr_b16 v[212:213], v240 offset:20992\n"
    "v_mfma_f32_32x32x16_bf16 v[48:63], v[196:199], a[48:51], v[48:63]\n"
    "ds_read_b64_tr_b16 v[214:215], v240 offset:21248\n"
    "v_mfma_f32_32x32x16_bf16 v[64:79], v[196:199], a[176:179], v[64:79]\n"
    "ds_read_b64_tr_b16 v[216:217], v240 offset:21504\n"
    "v_mfma_f32_32x32x16_bf16 v[80:95], v[196:199], v[224:227], v[80:95]\n"
    "ds_read_b64_tr_b16 v[218:219], v240 offset:21760\n"
    "v_mfma_f32_32x32x16_bf16 v[96:111], v[200:203], a[48:51], v[96:111]\n"
    "ds_read_b64_tr_b16 v[220:221], v240 offset:22016\n"
    "v_mfma_f32_32x32x16_bf16 v[112:127], v[200:203], a[176:179], v[112:127]\n"
    "ds_read_b64_tr_b16 v[222:223], v240 offset:22272\n"
    "v_mfma_f32_32x32x16_bf16 v[128:143], v[200:203], v[224:227], v[128:143]\n"
    "ds_read_b128 v[228:231], v242 offset:4096\n"
    "v_mfma_f32_32x32x16_bf16 v[144:159], v[204:207], a[48:51], v[144:159]\n"
    "v_mfma_f32_32x32x16_bf16 v[160:175], v[204:207], a[176:179], v[160:175]\n"
    "v_mfma_f32_32x32x16_bf16 v[176:191], v[204:207], v[224:227], v[176:191]\n"
    "s_waitcnt lgkmcnt(0)\n"
    "v_mfma_f32_32x32x16_bf16 v[0:15], v[208:211], a[52:55], v[0:15]\n"
    "ds_read_b64_tr_b16 v[192:193], v240 offset:24576\n"
    "v_mfma_f32_32x32x16_bf16 v[16:31], v[208:211], a[180:183], v[16:31]\n"
    "ds_read_b64_tr_b16 v[194:195], v240 offset:24832\n"
    "s_mov_b32 m0, 114688\n"
    "s_add_u32 s67, s75, 2621440\n"
    "buffer_load_dwordx4 v248, s[44:47], s67 offen lds\n"
    "v_mfma_f32_32x32x16_bf16 v[32:47], v[208:211], v[228:231], v[32:47]\n"
    "ds_read_b64_tr_b16 v[196:197], v240 offset:25088\n"
    "s_mov_b32 m0, 115584\n"
    "s_nop 0\n"
    "buffer_load_dwordx4 v248, s[44:47], s67 offen offset:128 lds\n"
    "v_mfma_f32_32x32x16_bf16 v[48:63], v[212:215], a[52:55], v[48:63]\n"
    "ds_read_b64_tr_b16 v[198:199], v240 offset:25344\n"
    "s_mov_b32 m0, 116736\n"
    "s_add_u32 s67, s75, 2686976\n"
    "buffer_load_dwordx4 v248, s[44:47], s67 offen lds\n"
    "v_mfma_f32_32x32x16_bf16 v[64:79], v[212:215], a[180:183], v[64:79]\n"
    "ds_read_b64_tr_b16 v[200:201], v240 offset:25600\n"
    "s_mov_b32 m0, 117632\n"
    "s_nop 0\n"
    "buffer_load_dwordx4 v248, s[44:47], s67 offen offset:128 lds\n"
    "v_mfma_f32_32x32x16_bf16 v[80:95], v[212:215], v[228:231], v[80:95]\n"
    "ds_read_b64_tr_b16 v[202:203], v240 offset:25856\n"
    "s_mov_b32 m0, 118784\n"
    "s_add_u32 s67, s75, 2752512\n"
    "buffer_load_dwordx4 v248, s[44:47], s67 offen lds\n"
    "v_mfma_f32_32x32x16_bf16 v[96:111], v[216:219], a[52:55], v[96:111]\n"
    "s_barrier\n"
    "ds_read_b64_tr_b16 v[204:205], v240 offset:26112\n"
    "s_mov_b32 m0, 119680\n"
    "s_nop 0\n"
    "buffer_load_dwordx4 v248, s[44:47], s67 offen offset:128 lds\n"
    "v_mfma_f32_32x32x16_bf16 v[112:127], v[216:219], a[180:183], v[112:127]\n"
    "ds_read_b64_tr_b16 v[206:207], v240 offset:26368\n"
    "s_mov_b32 m0, 120832\n"
    "s_add_u32 s67, s75, 2818048\n"
    "buffer_load_dwordx4 v248, s[44:47], s67 offen lds\n"
    "v_mfma_f32_32x32x16_bf16 v[128:143], v[216:219], v[228:231], v[128:143]\n"
    "ds_read_b128 v[224:227], v241 offset:4608\n"
    "s_mov_b32 m0, 121728\n"
    "s_nop 0\n"
    "buffer_load_dwordx4 v248, s[44:47], s67 offen offset:128 lds\n"
    "v_mfma_f32_32x32x16_bf16 v[144:159], v[220:223], a[52:55], v[144:159]\n"
    "v_mfma_f32_32x32x16_bf16 v[160:175], v[220:223], a[180:183], v[160:175]\n"
    "v_mfma_f32_32x32x16_bf16 v[176:191], v[220:223], v[228:231], v[176:191]\n"
    "s_waitcnt lgkmcnt(0)\n"
    "v_mfma_f32_32x32x16_bf16 v[0:15], v[192:195], a[56:59], v[0:15]\n"
    "ds_read_b64_tr_b16 v[208:209], v240 offset:28672\n"
    "v_mfma_f32_32x32x16_bf16 v[16:31], v[192:195], a[184:187], v[16:31]\n"
    "ds_read_b64_tr_b16 v[210:211], v240 offset:28928\n"
    "v_mfma_f32_32x32x16_bf16 v[32:47], v[192:195], v[224:227], v[32:47]\n"
    "ds_read_b64_tr_b16 v[212:213], v240 offset:29184\n"
    "v_mfma_f32_32x32x16_bf16 v[48:63], v[196:199], a[56:59], v[48:63]\n"
    "ds_read_b64_tr_b16 v[214:215], v240 offset:29440\n"
    "v_mfma_f32_32x32x16_bf16 v[64:79], v[196:199], a[184:187], v[64:79]\n"
    "ds_read_b64_tr_b16 v[216:217], v240 offset:29696\n"
    "v_mfma_f32_32x32x16_bf16 v[80:95], v[196:199], v[224:227], v[80:95]\n"
    "ds_read_b64_tr_b16 v[218:219], v240 offset:29952\n"
    "v_mfma_f32_32x32x16_bf16 v[96:111], v[200:203], a[56:59], v[96:111]\n"
    "ds_read_b64_tr_b16 v[220:221], v240 offset:30208\n"
    "v_mfma_f32_32x32x16_bf16 v[112:127], v[200:203], a[184:187], v[112:127]\n"
    "ds_read_b64_tr_b16 v[222:223], v240 offset:30464\n"
    "v_mfma_f32_32x32x16_bf16 v[128:143], v[200:203], v[224:227], v[128:143]\n"
    "ds_read_b128 v[228:231], v242 offset:4608\n"
    "v_mfma_f32_32x32x16_bf16 v[144:159], v[204:207], a[56:59], v[144:159]\n"
    "v_mfma_f32_32x32x16_bf16 v[160:175], v[204:207], a[184:187], v[160:175]\n"
    "v_mfma_f32_32x32x16_bf16 v[176:191], v[204:207], v[224:227], v[176:191]\n"
    "s_waitcnt lgkmcnt(0)\n"
    "v_mfma_f32_32x32x16_bf16 v[0:15], v[208:211], a[60:63], v[0:15]\n"
    "s_waitcnt vmcnt(28)\n"
    "s_waitcnt vmcnt(12)\n"
    "ds_read_b64_tr_b16 v[192:193], v240 offset:0\n"
    "v_mfma_f32_32x32x16_bf16 v[16:31], v[208:211], a[188:191], v[16:31]\n"
    "ds_read_b64_tr_b16 v[194:195], v240 offset:256\n"
    "s_mov_b32 m0, 122880\n"
    "s_add_u32 s67, s75, 2883584\n"
    "buffer_load_dwordx4 v248, s[44:47], s67 offen lds\n"
    "v_mfma_f32_32x32x16_bf16 v[32:47], v[208:211], v[228:231], v[32:47]\n"
    "ds_read_b64_tr_b16 v[196:197], v240 offset:512\n"
    "s_mov_b32 m0, 123776\n"
    "s_nop 0\n"
    "buffer_load_dwordx4 v248, s[44:47], s67 offen offset:128 lds\n"
    "v_mfma_f32_32x32x16_bf16 v[48:63], v[212:215], a[60:63], v[48:63]\n"
    "ds_read_b64_tr_b16 v[198:199], v240 offset:768\n"
    "s_mov_b32 m0, 124928\n"
    "s_add_u32 s67, s75, 2949120\n"
    "buffer_load_dwordx4 v248, s[44:47], s67 offen lds\n"
    "v_mfma_f32_32x32x16_bf16 v[64:79], v[212:215], a[188:191], v[64:79]\n"
    "s_barrier\n"
    "ds_read_b64_tr_b16 v[200:201], v240 offset:1024\n"
    "s_mov_b32 m0, 125824\n"
    "s_nop 0\n"
    "buffer_load_dwordx4 v248, s[44:47], s67 offen offset:128 lds\n"
    "v_mfma_f32_32x32x16_bf16 v[80:95], v[212:215], v[228:231], v[80:95]\n"
    "ds_read_b64_tr_b16 v[202:203], v240 offset:1280\n"
    "s_mov_b32 m0, 126976\n"
    "s_add_u32 s67, s75, 3014656\n"
    "buffer_load_dwordx4 v248, s[44:47], s67 offen lds\n"
    "v_mfma_f32_32x32x16_bf16 v[96:111], v[216:219], a[60:63], v[96:111]\n"
    "ds_read_b64_tr_b16 v[204:205], v240 offset:1536\n"
    "s_mov_b32 m0, 127872\n"
    "s_nop 0\n"
    "buffer_load_dwordx4 v248, s[44:47], s67 offen offset:128 lds\n"
    "v_mfma_f32_32x32x16_bf16 v[112:127], v[216:219], a[188:191], v[112:127]\n"
    "ds_read_b64_tr_b16 v[206:207], v240 offset:1792\n"
    "s_mov_b32 m0, 129024\n"
    "s_add_u32 s67, s75, 3080192\n"
    "buffer_load_dwordx4 v248, s[44:47], s67 offen lds\n"
    "v_mfma_f32_32x32x16_bf16 v[128:143], v[216:219], v[228:231], v[128:143]\n"
    "ds_read_b128 v[224:227], v241 offset:0\n"
    "s_mov_b32 m0, 129920\n"
    "s_nop 0\n"
    "buffer_load_dwordx4 v248, s[44:47], s67 offen offset:128 lds\n"
    "v_mfma_f32_32x32x16_bf16 v[144:159], v[220:223], a[60:63], v[144:159]\n"
    "s_mov_b32 m0, 159744\n"
    "s_mov_b32 s67, 66176\n"
    "buffer_load_dwordx4 v249, s[52:55], s67 offen lds\n"
    "v_mfma_f32_32x32x16_bf16 v[160:175], v[220:223], a[188:191], v[160:175]\n"
    "s_mov_b32 m0, 160768\n"
    "s_mov_b32 s67, 74368\n"
    "buffer_load_dwordx4 v250, s[52:55], s67 offen lds\n"
    "s_add_u32 m0, s61, 16384\n"
    "s_add_u32 s66, s62, 81920\n"
    "buffer_load_dwordx4 v244, s[36:39], s66 offen nt lds\n"
    "buffer_load_dwordx4 v245, s[36:39], s66 offen offset:2048 nt lds\n"
    "v_mfma_f32_32x32x16_bf16 v[176:191], v[220:223], v[228:231], v[176:191]\n"
    "s_mov_b32 m0, 161792\n"
    "s_mov_b32 s67, 82560\n"
    "buffer_load_dwordx4 v249, s[52:55], s67 offen lds\n"
    "s_mov_b32 m0, 162816\n"
    "s_mov_b32 s67, 90752\n"
    "buffer_load_dwordx4 v250, s[52:55], s67 offen lds\n"
    "s_waitcnt lgkmcnt(0)\n"
    "v_mfma_f32_32x32x16_bf16 v[0:15], v[192:195], a[64:67], v[0:15]\n"
    "ds_read_b64_tr_b16 v[208:209], v240 offset:4096\n"
    "s_add_u32 m0, s61, 17280\n"
    "s_nop 0\n"
    "buffer_load_dwordx4 v244, s[36:39], s66 offen offset:128 nt lds\n"
    "buffer_load_dwordx4 v245, s[36:39], s66 offen offset:2176 nt lds\n"
    "v_mfma_f32_32x32x16_bf16 v[16:31], v[192:195], a[192:195], v[16:31]\n"
    "ds_read_b64_tr_b16 v[210:211], v240 offset:4352\n"
    "v_mfma_f32_32x32x16_bf16 v[32:47], v[192:195], v[224:227], v[32:47]\n"
    "ds_read_b64_tr_b16 v[212:213], v240 offset:4608\n"
    "v_mfma_f32_32x32x16_bf16 v[48:63], v[196:199], a[64:67], v[48:63]\n"
    "ds_read_b64_tr_b16 v[214:215], v240 offset:4864\n"
    "v_mfma_f32_32x32x16_bf16 v[64:79], v[196:199], a[192:195], v[64:79]\n"
    "ds_read_b64_tr_b16 v[216:217], v240 offset:5120\n"
    "v_mfma_f32_32x32x16_bf16 v[80:95], v[196:199], v[224:227], v[80:95]\n"
    "ds_read_b64_tr_b16 v[218:219], v240 offset:5376\n"
    "v_mfma_f32_32x32x16_bf16 v[96:111], v[200:203], a[64:67], v[96:111]\n"
    "s_barrier\n"
    "ds_read_b64_tr_b16 v[220:221], v240 offset:5632\n"
    "v_mfma_f32_32x32x16_bf16 v[112:127], v[200:203], a[192:195], v[112:127]\n"
    "ds_read_b64_tr_b16 v[222:223], v240 offset:5888\n"
    "s_add_u32 m0, s61, 32768\n"
    "s_add_u32 s66, s62, 65536\n"
    "buffer_load_dwordx4 v246, s[40:43], s66 offen nt lds\n"
    "buffer_load_dwordx4 v246, s[40:43], s66 offen offset:2048 nt lds\n"
    "v_mfma_f32_32x32x16_bf16 v[128:143], v[200:203], v[224:227], v[128:143]\n"
    "ds_read_b128 v[228:231], v242 offset:0\n"
    "s_add_u32 m0, s61, 33664\n"
    "s_nop 0\n"
    "buffer_load_dwordx4 v246, s[40:43], s66 offen offset:128 nt lds\n"
    "buffer_load_dwordx4 v246, s[40:43], s66 offen offset:2176 nt lds\n"
    "v_mfma_f32_32x32x16_bf16 v[144:159], v[204:207], a[64:67], v[144:159]\n"
    "v_mfma_f32_32x32x16_bf16 v[160:175], v[204:207], a[192:195], v[160:175]\n"
    "v_mfma_f32_32x32x16_bf16 v[176:191], v[204:207], v[224:227], v[176:191]\n"
    "s_waitcnt lgkmcnt(0)\n"
    "v_mfma_f32_32x32x16_bf16 v[0:15], v[208:211], a[68:71], v[0:15]\n"
    "ds_read_b64_tr_b16 v[192:193], v240 offset:8192\n"
    "v_mfma_f32_32x32x16_bf16 v[16:31], v[208:211], a[196:199], v[16:31]\n"
    "ds_read_b64_tr_b16 v[194:195], v240 offset:8448\n"
    "s_mov_b32 m0, 98304\n"
    "s_add_u32 s67, s75, 3145728\n"
    "buffer_load_dwordx4 v248, s[44:47], s67 offen lds\n"
    "v_mfma_f32_32x32x16_bf16 v[32:47], v[208:211], v[228:231], v[32:47]\n"
    "ds_read_b64_tr_b16 v[196:197], v240 offset:8704\n"
    "s_mov_b32 m0, 99200\n"
    "s_nop 0\n"
    "buffer_load_dwordx4 v248, s[44:47], s67 offen offset:128 lds\n"
    "v_mfma_f32_32x32x16_bf16 v[48:63], v[212:215], a[68:71], v[48:63]\n"
    "ds_read_b64_tr_b16 v[198:199], v240 offset:8960\n"
    "s_mov_b32 m0, 100352\n"
    "s_add_u32 s67, s75, 3211264\n"
    "buffer_load_dwordx4 v248, s[44:47], s67 offen lds\n"
    "v_mfma_f32_32x32x16_bf16 v[64:79], v[212:215], a[196:199], v[64:79]\n"
    "ds_read_b64_tr_b16 v[200:201], v240 offset:9216\n"
    "s_mov_b32 m0, 101248\n"
    "s_nop 0\n"
    "buffer_load_dwordx4 v248, s[44:47], s67 offen offset:128 lds\n"
    "v_mfma_f32_32x32x16_bf16 v[80:95], v[212:215], v[228:231], v[80:95]\n"
    "ds_read_b64_tr_b16 v[202:203], v240 offset:9472\n"
    "s_mov_b32 m0, 102400\n"
    "s_add_u32 s67, s75, 3276800\n"
    "buffer_load_dwordx4 v248, s[44:47], s67 offen lds\n"
    "v_mfma_f32_32x32x16_bf16 v[96:111], v[216:219], a[68:71], v[96:111]\n"
    "ds_read_b64_tr_b16 v[204:205], v240 offset:9728\n"
    "s_mov_b32 m0, 103296\n"
    "s_nop 0\n"
    "buffer_load_dwordx4 v248, s[44:47], s67 offen offset:128 lds\n"
    "v_mfma_f32_32x32x16_bf16 v[112:127], v[216:219], a[196:199], v[112:127]\n"
    "ds_read_b64_tr_b16 v[206:207], v240 offset:9984\n"
    "s_mov_b32 m0, 104448\n"
    "s_add_u32 s67, s75, 3342336\n"
    "buffer_load_dwordx4 v248, s[44:47], s67 offen lds\n"
    "v_mfma_f32_32x32x16_bf16 v[128:143], v[216:219], v[228:231], v[128:143]\n"
    "ds_read_b128 v[224:227], v241 offset:512\n"
    "s_mov_b32 m0, 105344\n"
    "s_nop 0\n"
    "buffer_load_dwordx4 v248, s[44:47], s67 offen offset:128 lds\n"
    "v_mfma_f32_32x32x16_bf16 v[144:159], v[220:223], a[68:71], v[144:159]\n"
    "v_mfma_f32_32x32x16_bf16 v[160:175], v[220:223], a[196:199], v[160:175]\n"
    "v_mfma_f32_32x32x16_bf16 v[176:191], v[220:223], v[228:231], v[176:191]\n"
    "s_waitcnt lgkmcnt(0)\n"
    "v_mfma_f32_32x32x16_bf16 v[0:15], v[192:195], a[72:75], v[0:15]\n"
    "ds_read_b64_tr_b16 v[208:209], v240 offset:12288\n"
    "v_mfma_f32_32x32x16_bf16 v[16:31], v[192:195], a[200:203], v[16:31]\n"
    "ds_read_b64_tr_b16 v[210:211], v240 offset:12544\n"
    "v_mfma_f32_32x32x16_bf16 v[32:47], v[192:195], v[224:227], v[32:47]\n"
    "ds_read_b64_tr_b16 v[212:213], v240 offset:12800\n"
    "v_mfma_f32_32x32x16_bf16 v[48:63], v[196:199], a[72:75], v[48:63]\n"
    "ds_read_b64_tr_b16 v[214:215], v240 offset:13056\n"
    "v_mfma_f32_32x32x16_bf16 v[64:79], v[196:199], a[200:203], v[64:79]\n"
    "ds_read_b64_tr_b16 v[216:217], v240 offset:13312\n"
    "v_mfma_f32_32x32x16_bf16 v[80:95], v[196:199], v[224:227], v[80:95]\n"
    "s_waitcnt vmcnt(36)\n"
    "s_barrier\n"
    "ds_read_b64_tr_b16 v[218:219], v240 offset:13568\n"
    "v_mfma_f32_32x32x16_bf16 v[96:111], v[200:203], a[72:75], v[96:111]\n"
    "ds_read_b64_tr_b16 v[220:221], v240 offset:13824\n"
    "v_mfma_f32_32x32x16_bf16 v[112:127], v[200:203], a[200:203], v[112:127]\n"
    "ds_read_b64_tr_b16 v[222:223], v240 offset:14080\n"
    "v_mfma_f32_32x32x16_bf16 v[128:143], v[200:203], v[224:227], v[128:143]\n"
    "ds_read_b128 v[228:231], v242 offset:512\n"
    "v_mfma_f32_32x32x16_bf16 v[144:159], v[204:207], a[72:75], v[144:159]\n"
    "v_mfma_f32_32x32x16_bf16 v[160:175], v[204:207], a[200:203], v[160:175]\n"
    "v_mfma_f32_32x32x16_bf16 v[176:191], v[204:207], v[224:227], v[176:191]\n"
    "s_waitcnt lgkmcnt(0)\n"
    "v_mfma_f32_32x32x16_bf16 v[0:15], v[208:211], a[76:79], v[0:15]\n"
    "s_waitcnt vmcnt(28)\n"
    "s_waitcnt vmcnt(14)\n"
    "ds_read_b64_tr_b16 v[192:193], v240 offset:16384\n"
    "v_mfma_f32_32x32x16_bf16 v[16:31], v[208:211], a[204:207], v[16:31]\n"
    "ds_read_b64_tr_b16 v[194:195], v240 offset:16640\n"
    "s_mov_b32 m0, 106496\n"
    "s_add_u32 s67, s75, 3407872\n"
    "buffer_load_dwordx4 v248, s[44:47], s67 offen lds\n"
    "v_mfma_f32_32x32x16_bf16 v[32:47], v[208:211], v[228:231], v[32:47]\n"
    "ds_read_b64_tr_b16 v[196:197], v240 offset:16896\n"
    "s_mov_b32 m0, 107392\n"
    "s_nop 0\n"
    "buffer_load_dwordx4 v248, s[44:47], s67 offen offset:128 lds\n"
    "v_mfma_f32_32x32x16_bf16 v[48:63], v[212:215], a[76:79], v[48:63]\n"
    "ds_read_b64_tr_b16 v[198:199], v240 offset:17152\n"
    "s_mov_b32 m0, 108544\n"
    "s_add_u32 s67, s75, 3473408\n"
    "buffer_load_dwordx4 v248, s[44:47], s67 offen lds\n"
    "v_mfma_f32_32x32x16_bf16 v[64:79], v[212:215], a[204:207], v[64:79]\n"
    "ds_read_b64_tr_b16 v[200:201], v240 offset:17408\n"
    "s_mov_b32 m0, 109440\n"
    "s_nop 0\n"
    "buffer_load_dwordx4 v248, s[44:47], s67 offen offset:128 lds\n"
    "v_mfma_f32_32x32x16_bf16 v[80:95], v[212:215], v[228:231], v[80:95]\n"
    "ds_read_b64_tr_b16 v[202:203], v240 offset:17664\n"
    "s_mov_b32 m0, 110592\n"
    "s_add_u32 s67, s75, 3538944\n"
    "buffer_load_dwordx4 v248, s[44:47], s67 offen lds\n"
    "v_mfma_f32_32x32x16_bf16 v[96:111], v[216:219], a[76:79], v[96:111]\n"
    "ds_read_b64_tr_b16 v[204:205], v240 offset:17920\n"
    "s_mov_b32 m0, 111488\n"
    "s_nop 0\n"
    "buffer_load_dwordx4 v248, s[44:47], s67 offen offset:128 lds\n"
    "v_mfma_f32_32x32x16_bf16 v[112:127], v[216:219], a[204:207], v[112:127]\n"
    "ds_read_b64_tr_b16 v[206:207], v240 offset:18176\n"
    "s_mov_b32 m0, 112640\n"
    "s_add_u32 s67, s75, 3604480\n"
    "buffer_load_dwordx4 v248, s[44:47], s67 offen lds\n"
    "v_mfma_f32_32x32x16_bf16 v[128:143], v[216:219], v[228:231], v[128:143]\n"
    "ds_read_b128 v[224:227], v241 offset:4096\n"
    "s_mov_b32 m0, 113536\n"
    "s_nop 0\n"
    "buffer_load_dwordx4 v248, s[44:47], s67 offen offset:128 lds\n"
    "v_mfma_f32_32x32x16_bf16 v[144:159], v[220:223], a[76:79], v[144:159]\n"
    "s_mov_b32 m0, 155648\n"
    "s_mov_b32 s67, 66304\n"
    "buffer_load_dwordx4 v249, s[52:55], s67 offen lds\n"
    "v_mfma_f32_32x32x16_bf16 v[160:175], v[220:223], a[204:207], v[160:175]\n"
    "s_mov_b32 m0, 156672\n"
    "s_mov_b32 s67, 74496\n"
    "buffer_load_dwordx4 v250, s[52:55], s67 offen lds\n"
    "v_mfma_f32_32x32x16_bf16 v[176:191], v[220:223], v[228:231], v[176:191]\n"
    "s_mov_b32 m0, 157696\n"
    "s_mov_b32 s67, 82688\n"
    "buffer_load_dwordx4 v249, s[52:55], s67 offen lds\n"
    "s_mov_b32 m0, 158720\n"
    "s_mov_b32 s67, 90880\n"
    "buffer_load_dwordx4 v250, s[52:55], s67 offen lds\n"
    "s_waitcnt lgkmcnt(0)\n"
    "v_mfma_f32_32x32x16_bf16 v[0:15], v[192:195], a[80:83], v[0:15]\n"
    "ds_read_b64_tr_b16 v[208:209], v240 offset:20480\n"
    "v_mfma_f32_32x32x16_bf16 v[16:31], v[192:195], a[208:211], v[16:31]\n"
    "ds_read_b64_tr_b16 v[210:211], v240 offset:20736\n"
    "v_mfma_f32_32x32x16_bf16 v[32:47], v[192:195], v[224:227], v[32:47]\n"
    "ds_read_b64_tr_b16 v[212:213], v240 offset:20992\n"
    "v_mfma_f32_32x32x16_bf16 v[48:63], v[196:199], a[80:83], v[48:63]\n"
    "s_barrier\n"
    "ds_read_b64_tr_b16 v[214:215], v240 offset:21248\n"
    "v_mfma_f32_32x32x16_bf16 v[64:79], v[196:199], a[208:211], v[64:79]\n"
    "ds_read_b64_tr_b16 v[216:217], v240 offset:21504\n"
    "s_add_u32 m0, s61, 0\n"
    "s_add_u32 s66, s62, 98304\n"
    "buffer_load_dwordx4 v244, s[36:39], s66 offen nt lds\n"
    "buffer_load_dwordx4 v245, s[36:39], s66 offen offset:2048 nt lds\n"
    "v_mfma_f32_32x32x16_bf16 v[80:95], v[196:199], v[224:227], v[80:95]\n"
    "ds_read_b64_tr_b16 v[218:219], v240 offset:21760\n"
    "s_add_u32 m0, s61, 896\n"
    "s_nop 0\n"
    "buffer_load_dwordx4 v244, s[36:39], s66 offen offset:128 nt lds\n"
    "buffer_load_dwordx4 v245, s[36:39], s66 offen offset:2176 nt lds\n"
    "v_mfma_f32_32x32x16_bf16 v[96:111], v[200:203], a[80:83], v[96:111]\n"
    "ds_read_b64_tr_b16 v[220:221], v240 offset:22016\n"
    "v_mfma_f32_32x32x16_bf16 v[112:127], v[200:203], a[208:211], v[112:127]\n"
    "ds_read_b64_tr_b16 v[222:223], v240 offset:22272\n"
    "v_mfma_f32_32x32x16_bf16 v[128:143], v[200:203], v[224:227], v[128:143]\n"
    "ds_read_b128 v[228:231], v242 offset:4096\n"
    "v_mfma_f32_32x32x16_bf16 v[144:159], v[204:207], a[80:83], v[144:159]\n"
    "v_mfma_f32_32x32x16_bf16 v[160:175], v[204:207], a[208:211], v[160:175]\n"
    "v_mfma_f32_32x32x16_bf16 v[176:191], v[204:207], v[224:227], v[176:191]\n"
    "s_waitcnt lgkmcnt(0)\n"
    "v_mfma_f32_32x32x16_bf16 v[0:15], v[208:211], a[84:87], v[0:15]\n"
    "ds_read_b64_tr_b16 v[192:193], v240 offset:24576\n"
    "v_mfma_f32_32x32x16_bf16 v[16:31], v[208:211], a[212:215], v[16:31]\n"
    "ds_read_b64_tr_b16 v[194:195], v240 offset:24832\n"
    "s_mov_b32 m0, 114688\n"
    "s_add_u32 s67, s75, 3670016\n"
    "buffer_load_dwordx4 v248, s[44:47], s67 offen lds\n"
    "v_mfma_f32_32x32x16_bf16 v[32:47], v[208:211], v[228:231], v[32:47]\n"
    "ds_read_b64_tr_b16 v[196:197], v240 offset:25088\n"
    "s_mov_b32 m0, 115584\n"
    "s_nop 0\n"
    "buffer_load_dwordx4 v248, s[44:47], s67 offen offset:128 lds\n"
    "v_mfma_f32_32x32x16_bf16 v[48:63], v[212:215], a[84:87], v[48:63]\n"
    "ds_read_b64_tr_b16 v[198:199], v240 offset:25344\n"
    "s_mov_b32 m0, 116736\n"
    "s_add_u32 s67, s75, 3735552\n"
    "buffer_load_dwordx4 v248, s[44:47], s67 offen lds\n"
    "v_mfma_f32_32x32x16_bf16 v[64:79], v[212:215], a[212:215], v[64:79]\n"
    "ds_read_b64_tr_b16 v[200:201], v240 offset:25600\n"
    "s_mov_b32 m0, 117632\n"
    "s_nop 0\n"
    "buffer_load_dwordx4 v248, s[44:47], s67 offen offset:128 lds\n"
    "v_mfma_f32_32x32x16_bf16 v[80:95], v[212:215], v[228:231], v[80:95]\n"
    "s_waitcnt vmcnt(32)\n"
    "s_barrier\n"
    "ds_read_b64_tr_b16 v[202:203], v240 offset:25856\n"
    "s_mov_b32 m0, 118784\n"
    "s_add_u32 s67, s75, 3801088\n"
    "buffer_load_dwordx4 v248, s[44:47], s67 offen lds\n"
    "v_mfma_f32_32x32x16_bf16 v[96:111], v[216:219], a[84:87], v[96:111]\n"
    "ds_read_b64_tr_b16 v[204:205], v240 offset:26112\n"
    "s_mov_b32 m0, 119680\n"
    "s_nop 0\n"
    "buffer_load_dwordx4 v248, s[44:47], s67 offen offset:128 lds\n"
    "v_mfma_f32_32x32x16_bf16 v[112:127], v[216:219], a[212:215], v[112:127]\n"
    "ds_read_b64_tr_b16 v[206:207], v240 offset:26368\n"
    "s_mov_b32 m0, 120832\n"
    "s_add_u32 s67, s75, 3866624\n"
    "buffer_load_dwordx4 v248, s[44:47], s67 offen lds\n"
    "v_mfma_f32_32x32x16_bf16 v[128:143], v[216:219], v[228:231], v[128:143]\n"
    "ds_read_b128 v[224:227], v241 offset:4608\n"
    "s_mov_b32 m0, 121728\n"
    "s_nop 0\n"
    "buffer_load_dwordx4 v248, s[44:47], s67 offen offset:128 lds\n"
    "v_mfma_f32_32x32x16_bf16 v[144:159], v[220:223], a[84:87], v[144:159]\n"
    "s_add_u32 m0, s61, 49152\n"
    "s_add_u32 s66, s62, 81920\n"
    "buffer_load_dwordx4 v246, s[40:43], s66 offen nt lds\n"
    "buffer_load_dwordx4 v246, s[40:43], s66 offen offset:2048 nt lds\n"
    "v_mfma_f32_32x32x16_bf16 v[160:175], v[220:223], a[212:215], v[160:175]\n"
    "s_add_u32 m0, s61, 50048\n"
    "s_nop 0\n"
    "buffer_load_dwordx4 v246, s[40:43], s66 offen offset:128 nt lds\n"
    "buffer_load_dwordx4 v246, s[40:43], s66 offen offset:2176 nt lds\n"
    "v_mfma_f32_32x32x16_bf16 v[176:191], v[220:223], v[228:231], v[176:191]\n"
    "s_waitcnt lgkmcnt(0)\n"
    "v_mfma_f32_32x32x16_bf16 v[0:15], v[192:195], a[88:91], v[0:15]\n"
    "ds_read_b64_tr_b16 v[208:209], v240 offset:28672\n"
    "v_mfma_f32_32x32x16_bf16 v[16:31], v[192:195], a[216:219], v[16:31]\n"
    "ds_read_b64_tr_b16 v[210:211], v240 offset:28928\n"
    "v_mfma_f32_32x32x16_bf16 v[32:47], v[192:195], v[224:227], v[32:47]\n"
    "ds_read_b64_tr_b16 v[212:213], v240 offset:29184\n"
    "v_mfma_f32_32x32x16_bf16 v[48:63], v[196:199], a[88:91], v[48:63]\n"
    "ds_read_b64_tr_b16 v[214:215], v240 offset:29440\n"
    "v_mfma_f32_32x32x16_bf16 v[64:79], v[196:199], a[216:219], v[64:79]\n"
    "ds_read_b64_tr_b16 v[216:217], v240 offset:29696\n"
    "v_mfma_f32_32x32x16_bf16 v[80:95], v[196:199], v[224:227], v[80:95]\n"
    "ds_read_b64_tr_b16 v[218:219], v240 offset:29952\n"
    "v_mfma_f32_32x32x16_bf16 v[96:111], v[200:203], a[88:91], v[96:111]\n"
    "ds_read_b64_tr_b16 v[220:221], v240 offset:30208\n"
    "v_mfma_f32_32x32x16_bf16 v[112:127], v[200:203], a[216:219], v[112:127]\n"
    "ds_read_b64_tr_b16 v[222:223], v240 offset:30464\n"
    "v_mfma_f32_32x32x16_bf16 v[128:143], v[200:203], v[224:227], v[128:143]\n"
    "ds_read_b128 v[228:231], v242 offset:4608\n"
    "v_mfma_f32_32x32x16_bf16 v[144:159], v[204:207], a[88:91], v[144:159]\n"
    "v_mfma_f32_32x32x16_bf16 v[160:175], v[204:207], a[216:219], v[160:175]\n"
    "v_mfma_f32_32x32x16_bf16 v[176:191], v[204:207], v[224:227], v[176:191]\n"
    "s_waitcnt lgkmcnt(0)\n"
    "v_mfma_f32_32x32x16_bf16 v[0:15], v[208:211], a[92:95], v[0:15]\n"
    "s_waitcnt vmcnt(28)\n"
    "s_waitcnt vmcnt(16)\n"
    "ds_read_b64_tr_b16 v[192:193], v240 offset:0\n"
    "v_mfma_f32_32x32x16_bf16 v[16:31], v[208:211], a[220:223], v[16:31]\n"
    "ds_read_b64_tr_b16 v[194:195], v240 offset:256\n"
    "s_mov_b32 m0, 122880\n"
    "s_add_u32 s67, s75, 3932160\n"
    "buffer_load_dwordx4 v248, s[44:47], s67 offen lds\n"
    "v_mfma_f32_32x32x16_bf16 v[32:47], v[208:211], v[228:231], v[32:47]\n"
    "ds_read_b64_tr_b16 v[196:197], v240 offset:512\n"
    "s_mov_b32 m0, 123776\n"
    "s_nop 0\n"
    "buffer_load_dwordx4 v248, s[44:47], s67 offen offset:128 lds\n"
    "v_mfma_f32_32x32x16_bf16 v[48:63], v[212:215], a[92:95], v[48:63]\n"
    "ds_read_b64_tr_b16 v[198:199], v240 offset:768\n"
    "s_mov_b32 m0, 124928\n"
    "s_add_u32 s67, s75, 3997696\n"
    "buffer_load_dwordx4 v248, s[44:47], s67 offen lds\n"
    "v_mfma_f32_32x32x16_bf16 v[64:79], v[212:215], a[220:223], v[64:79]\n"
    "s_barrier\n"
    "ds_read_b64_tr_b16 v[200:201], v240 offset:1024\n"
    "s_mov_b32 m0, 125824\n"
    "s_nop 0\n"
    "buffer_load_dwordx4 v248, s[44:47], s67 offen offset:128 lds\n"
    "v_mfma_f32_32x32x16_bf16 v[80:95], v[212:215], v[228:231], v[80:95]\n"
    "ds_read_b64_tr_b16 v[202:203], v240 offset:1280\n"
    "s_mov_b32 m0, 126976\n"
    "s_add_u32 s67, s75, 4063232\n"
    "buffer_load_dwordx4 v248, s[44:47], s67 offen lds\n"
    "v_mfma_f32_32x32x16_bf16 v[96:111], v[216:219], a[92:95], v[96:111]\n"
    "ds_read_b64_tr_b16 v[204:205], v240 offset:1536\n"
    "s_mov_b32 m0, 127872\n"
    "s_nop 0\n"
    "buffer_load_dwordx4 v248, s[44:47], s67 offen offset:128 lds\n"
    "v_mfma_f32_32x32x16_bf16 v[112:127], v[216:219], a[220:223], v[112:127]\n"
    "ds_read_b64_tr_b16 v[206:207], v240 offset:1792\n"
    "s_mov_b32 m0, 129024\n"
    "s_add_u32 s67, s75, 4128768\n"
    "buffer_load_dwordx4 v248, s[44:47], s67 offen lds\n"
    "v_mfma_f32_32x32x16_bf16 v[128:143], v[216:219], v[228:231], v[128:143]\n"
    "ds_read_b128 v[224:227], v241 offset:0\n"
    "s_mov_b32 m0, 129920\n"
    "s_nop 0\n"
    "buffer_load_dwordx4 v248, s[44:47], s67 offen offset:128 lds\n"
    "v_mfma_f32_32x32x16_bf16 v[144:159], v[220:223], a[92:95], v[144:159]\n"
    "s_mov_b32 m0, 159744\n"
    "s_mov_b32 s67, 66432\n"
    "buffer_load_dwordx4 v249, s[52:55], s67 offen lds\n"
    "v_mfma_f32_32x32x16_bf16 v[160:175], v[220:223], a[220:223], v[160:175]\n"
    "s_mov_b32 m0, 160768\n"
    "s_mov_b32 s67, 74624\n"
    "buffer_load_dwordx4 v250, s[52:55], s67 offen lds\n"
    "v_mfma_f32_32x32x16_bf16 v[176:191], v[220:223], v[228:231], v[176:191]\n"
    "s_mov_b32 m0, 161792\n"
    "s_mov_b32 s67, 82816\n"
    "buffer_load_dwordx4 v249, s[52:55], s67 offen lds\n"
    "s_mov_b32 m0, 162816\n"
    "s_mov_b32 s67, 91008\n"
    "buffer_load_dwordx4 v250, s[52:55], s67 offen lds\n"
    "s_waitcnt lgkmcnt(0)\n"
    "v_mfma_f32_32x32x16_bf16 v[0:15], v[192:195], a[96:99], v[0:15]\n"
    "ds_read_b64_tr_b16 v[208:209], v240 offset:4096\n"
    "v_mfma_f32_32x32x16_bf16 v[16:31], v[192:195], a[224:227], v[16:31]\n"
    "ds_read_b64_tr_b16 v[210:211], v240 offset:4352\n"
    "v_mfma_f32_32x32x16_bf16 v[32:47], v[192:195], v[224:227], v[32:47]\n"
    "ds_read_b64_tr_b16 v[212:213], v240 offset:4608\n"
    "v_mfma_f32_32x32x16_bf16 v[48:63], v[196:199], a[96:99], v[48:63]\n"
    "ds_read_b64_tr_b16 v[214:215], v240 offset:4864\n"
    "v_mfma_f32_32x32x16_bf16 v[64:79], v[196:199], a[224:227], v[64:79]\n"
    "ds_read_b64_tr_b16 v[216:217], v240 offset:5120\n"
    "v_mfma_f32_32x32x16_bf16 v[80:95], v[196:199], v[224:227], v[80:95]\n"
    "ds_read_b64_tr_b16 v[218:219], v240 offset:5376\n"
    "v_mfma_f32_32x32x16_bf16 v[96:111], v[200:203], a[96:99], v[96:111]\n"
    "ds_read_b64_tr_b16 v[220:221], v240 offset:5632\n"
    "v_mfma_f32_32x32x16_bf16 v[112:127], v[200:203], a[224:227], v[112:127]\n"
    "ds_read_b64_tr_b16 v[222:223], v240 offset:5888\n"
    "v_mfma_f32_32x32x16_bf16 v[128:143], v[200:203], v[224:227], v[128:143]\n"
    "ds_read_b128 v[228:231], v242 offset:0\n"
    "v_mfma_f32_32x32x16_bf16 v[144:159], v[204:207], a[96:99], v[144:159]\n"
    "v_mfma_f32_32x32x16_bf16 v[160:175], v[204:207], a[224:227], v[160:175]\n"
    "v_mfma_f32_32x32x16_bf16 v[176:191], v[204:207], v[224:227], v[176:191]\n"
    "s_waitcnt lgkmcnt(0)\n"
    "v_mfma_f32_32x32x16_bf16 v[0:15], v[208:211], a[100:103], v[0:15]\n"
    "ds_read_b64_tr_b16 v[192:193], v240 offset:8192\n"
    "v_mfma_f32_32x32x16_bf16 v[16:31], v[208:211], a[228:231], v[16:31]\n"
    "ds_read_b64_tr_b16 v[194:195], v240 offset:8448\n"
    "s_mov_b32 m0, 98304\n"
    "s_add_u32 s67, s76, 0\n"
    "buffer_load_dwordx4 v248, s[44:47], s67 offen lds\n"
    "v_mfma_f32_32x32x16_bf16 v[32:47], v[208:211], v[228:231], v[32:47]\n"
    "s_barrier\n"
    "ds_read_b64_tr_b16 v[196:197], v240 offset:8704\n"
    "s_mov_b32 m0, 99200\n"
    "s_nop 0\n"
    "buffer_load_dwordx4 v248, s[44:47], s67 offen offset:128 lds\n"
    "v_mfma_f32_32x32x16_bf16 v[48:63], v[212:215], a[100:103], v[48:63]\n"
    "ds_read_b64_tr_b16 v[198:199], v240 offset:8960\n"
    "s_mov_b32 m0, 100352\n"
    "s_add_u32 s67, s76, 65536\n"
    "buffer_load_dwordx4 v248, s[44:47], s67 offen lds\n"
    "v_mfma_f32_32x32x16_bf16 v[64:79], v[212:215], a[228:231], v[64:79]\n"
    "ds_read_b64_tr_b16 v[200:201], v240 offset:9216\n"
    "s_mov_b32 m0, 101248\n"
    "s_nop 0\n"
    "buffer_load_dwordx4 v248, s[44:47], s67 offen offset:128 lds\n"
    "v_mfma_f32_32x32x16_bf16 v[80:95], v[212:215], v[228:231], v[80:95]\n"
    "ds_read_b64_tr_b16 v[202:203], v240 offset:9472\n"
    "s_mov_b32 m0, 102400\n"
    "s_add_u32 s67, s76, 131072\n"
    "buffer_load_dwordx4 v248, s[44:47], s67 offen lds\n"
    "v_mfma_f32_32x32x16_bf16 v[96:111], v[216:219], a[100:103], v[96:111]\n"
    "ds_read_b64_tr_b16 v[204:205], v240 offset:9728\n"
    "s_mov_b32 m0, 103296\n"
    "s_nop 0\n"
    "buffer_load_dwordx4 v248, s[44:47], s67 offen offset:128 lds\n"
    "v_mfma_f32_32x32x16_bf16 v[112:127], v[216:219], a[228:231], v[112:127]\n"
    "ds_read_b64_tr_b16 v[206:207], v240 offset:9984\n"
    "s_mov_b32 m0, 104448\n"
    "s_add_u32 s67, s76, 196608\n"
    "buffer_load_dwordx4 v248, s[44:47], s67 offen lds\n"
    "v_mfma_f32_32x32x16_bf16 v[128:143], v[216:219], v[228:231], v[128:143]\n"
    "ds_read_b128 v[224:227], v241 offset:512\n"
    "s_mov_b32 m0, 105344\n"
    "s_nop 0\n"
    "buffer_load_dwordx4 v248, s[44:47], s67 offen offset:128 lds\n"
    "s_add_u32 m0, s61, 16384\n"
    "s_add_u32 s66, s62, 114688\n"
    "buffer_load_dwordx4 v244, s[36:39], s66 offen nt lds\n"
    "buffer_load_dwordx4 v245, s[36:39], s66 offen offset:2048 nt lds\n"
    "v_mfma_f32_32x32x16_bf16 v[144:159], v[220:223], a[100:103], v[144:159]\n"
    "s_add_u32 m0, s61, 17280\n"
    "s_nop 0\n"
    "buffer_load_dwordx4 v244, s[36:39], s66 offen offset:128 nt lds\n"
    "buffer_load_dwordx4 v245, s[36:39], s66 offen offset:2176 nt lds\n"
    "v_mfma_f32_32x32x16_bf16 v[160:175], v[220:223], a[228:231], v[160:175]\n"
    "v_mfma_f32_32x32x16_bf16 v[176:191], v[220:223], v[228:231], v[176:191]\n"
    "s_waitcnt lgkmcnt(0)\n"
    "v_mfma_f32_32x32x16_bf16 v[0:15], v[192:195], a[104:107], v[0:15]\n"
    "ds_read_b64_tr_b16 v[208:209], v240 offset:12288\n"
    "v_mfma_f32_32x32x16_bf16 v[16:31], v[192:195], a[232:235], v[16:31]\n"
    "ds_read_b64_tr_b16 v[210:211], v240 offset:12544\n"
    "v_mfma_f32_32x32x16_bf16 v[32:47], v[192:195], v[224:227], v[32:47]\n"
    "ds_read_b64_tr_b16 v[212:213], v240 offset:12800\n"
    "v_mfma_f32_32x32x16_bf16 v[48:63], v[196:199], a[104:107], v[48:63]\n"
    "ds_read_b64_tr_b16 v[214:215], v240 offset:13056\n"
    "v_mfma_f32_32x32x16_bf16 v[64:79], v[196:199], a[232:235], v[64:79]\n"
    "s_waitcnt vmcnt(36)\n"
    "s_barrier\n"
    "ds_read_b64_tr_b16 v[216:217], v240 offset:13312\n"
    "v_mfma_f32_32x32x16_bf16 v[80:95], v[196:199], v[224:227], v[80:95]\n"
    "ds_read_b64_tr_b16 v[218:219], v240 offset:13568\n"
    "s_add_u32 m0, s61, 32768\n"
    "s_add_u32 s66, s62, 98304\n"
    "buffer_load_dwordx4 v246, s[40:43], s66 offen nt lds\n"
    "buffer_load_dwordx4 v246, s[40:43], s66 offen offset:2048 nt lds\n"
    "v_mfma_f32_32x32x16_bf16 v[96:111], v[200:203], a[104:107], v[96:111]\n"
    "ds_read_b64_tr_b16 v[220:221], v240 offset:13824\n"
    "s_add_u32 m0, s61, 33664\n"
    "s_nop 0\n"
    "buffer_load_dwordx4 v246, s[40:43], s66 offen offset:128 nt lds\n"
    "buffer_load_dwordx4 v246, s[40:43], s66 offen offset:2176 nt lds\n"
    "v_mfma_f32_32x32x16_bf16 v[112:127], v[200:203], a[232:235], v[112:127]\n"
    "ds_read_b64_tr_b16 v[222:223], v240 offset:14080\n"
    "v_mfma_f32_32x32x16_bf16 v[128:143], v[200:203], v[224:227], v[128:143]\n"
    "ds_read_b128 v[228:231], v242 offset:512\n"
    "v_mfma_f32_32x32x16_bf16 v[144:159], v[204:207], a[104:107], v[144:159]\n"
    "v_mfma_f32_32x32x16_bf16 v[160:175], v[204:207], a[232:235], v[160:175]\n"
    "v_mfma_f32_32x32x16_bf16 v[176:191], v[204:207], v[224:227], v[176:191]\n"
    "s_waitcnt lgkmcnt(0)\n"
    "v_mfma_f32_32x32x16_bf16 v[0:15], v[208:211], a[108:111], v[0:15]\n"
    "s_waitcnt vmcnt(32)\n"
    "s_waitcnt vmcnt(16)\n"
    "ds_read_b64_tr_b16 v[192:193], v240 offset:16384\n"
    "v_mfma_f32_32x32x16_bf16 v[16:31], v[208:211], a[236:239], v[16:31]\n"
    "ds_read_b64_tr_b16 v[194:195], v240 offset:16640\n"
    "s_mov_b32 m0, 106496\n"
    "s_add_u32 s67, s76, 262144\n"
    "buffer_load_dwordx4 v248, s[44:47], s67 offen lds\n"
    "v_mfma_f32_32x32x16_bf16 v[32:47], v[208:211], v[228:231], v[32:47]\n"
    "ds_read_b64_tr_b16 v[196:197], v240 offset:16896\n"
    "s_mov_b32 m0, 107392\n"
    "s_nop 0\n"
    "buffer_load_dwordx4 v248, s[44:47], s67 offen offset:128 lds\n"
    "v_mfma_f32_32x32x16_bf16 v[48:63], v[212:215], a[108:111], v[48:63]\n"
    "ds_read_b64_tr_b16 v[198:199], v240 offset:17152\n"
    "s_mov_b32 m0, 108544\n"
    "s_add_u32 s67, s76, 327680\n"
    "buffer_load_dwordx4 v248, s[44:47], s67 offen lds\n"
    "v_mfma_f32_32x32x16_bf16 v[64:79], v[212:215], a[236:239], v[64:79]\n"
    "ds_read_b64_tr_b16 v[200:201], v240 offset:17408\n"
    "s_mov_b32 m0, 109440\n"
    "s_nop 0\n"
    "buffer_load_dwordx4 v248, s[44:47], s67 offen offset:128 lds\n"
    "v_mfma_f32_32x32x16_bf16 v[80:95], v[212:215], v[228:231], v[80:95]\n"
    "ds_read_b64_tr_b16 v[202:203], v240 offset:17664\n"
    "s_mov_b32 m0, 110592\n"
    "s_add_u32 s67, s76, 393216\n"
    "buffer_load_dwordx4 v248, s[44:47], s67 offen lds\n"
    "v_mfma_f32_32x32x16_bf16 v[96:111], v[216:219], a[108:111], v[96:111]\n"
    "ds_read_b64_tr_b16 v[204:205], v240 offset:17920\n"
    "s_mov_b32 m0, 111488\n"
    "s_nop 0\n"
    "buffer_load_dwordx4 v248, s[44:47], s67 offen offset:128 lds\n"
    "v_mfma_f32_32x32x16_bf16 v[112:127], v[216:219], a[236:239], v[112:127]\n"
    "ds_read_b64_tr_b16 v[206:207], v240 offset:18176\n"
    "s_mov_b32 m0, 112640\n"
    "s_add_u32 s67, s76, 458752\n"
    "buffer_load_dwordx4 v248, s[44:47], s67 offen lds\n"
    "v_mfma_f32_32x32x16_bf16 v[128:143], v[216:219], v[228:231], v[128:143]\n"
    "ds_read_b128 v[224:227], v241 offset:4096\n"
    "s_mov_b32 m0, 113536\n"
    "s_nop 0\n"
    "buffer_load_dwordx4 v248, s[44:47], s67 offen offset:128 lds\n"
    "v_mfma_f32_32x32x16_bf16 v[144:159], v[220:223], a[108:111], v[144:159]\n"
    "s_mov_b32 m0, 155648\n"
    "s_mov_b32 s67, 65536\n"
    "buffer_load_dwordx4 v249, s[52:55], s67 offen lds\n"
    "v_mfma_f32_32x32x16_bf16 v[160:175], v[220:223], a[236:239], v[160:175]\n"
    "s_mov_b32 m0, 156672\n"
    "s_mov_b32 s67, 73728\n"
    "buffer_load_dwordx4 v250, s[52:55], s67 offen lds\n"
    "v_mfma_f32_32x32x16_bf16 v[176:191], v[220:223], v[228:231], v[176:191]\n"
    "s_mov_b32 m0, 157696\n"
    "s_mov_b32 s67, 81920\n"
    "buffer_load_dwordx4 v249, s[52:55], s67 offen lds\n"
    "s_mov_b32 m0, 158720\n"
    "s_mov_b32 s67, 90112\n"
    "buffer_load_dwordx4 v250, s[52:55], s67 offen lds\n"
    "s_waitcnt lgkmcnt(0)\n"
    "v_mfma_f32_32x32x16_bf16 v[0:15], v[192:195], a[112:115], v[0:15]\n"
    "ds_read_b64_tr_b16 v[208:209], v240 offset:20480\n"
    "v_mfma_f32_32x32x16_bf16 v[16:31], v[192:195], a[240:243], v[16:31]\n"
    "ds_read_b64_tr_b16 v[210:211], v240 offset:20736\n"
    "v_mfma_f32_32x32x16_bf16 v[32:47], v[192:195], v[224:227], v[32:47]\n"
    "s_barrier\n"
    "ds_read_b64_tr_b16 v[212:213], v240 offset:20992\n"
    "v_mfma_f32_32x32x16_bf16 v[48:63], v[196:199], a[112:115], v[48:63]\n"
    "ds_read_b64_tr_b16 v[214:215], v240 offset:21248\n"
    "v_mfma_f32_32x32x16_bf16 v[64:79], v[196:199], a[240:243], v[64:79]\n"
    "ds_read_b64_tr_b16 v[216:217], v240 offset:21504\n"
    "v_mfma_f32_32x32x16_bf16 v[80:95], v[196:199], v[224:227], v[80:95]\n"
    "ds_read_b64_tr_b16 v[218:219], v240 offset:21760\n"
    "v_mfma_f32_32x32x16_bf16 v[96:111], v[200:203], a[112:115], v[96:111]\n"
    "ds_read_b64_tr_b16 v[220:221], v240 offset:22016\n"
    "v_mfma_f32_32x32x16_bf16 v[112:127], v[200:203], a[240:243], v[112:127]\n"
    "ds_read_b64_tr_b16 v[222:223], v240 offset:22272\n"
    "v_mfma_f32_32x32x16_bf16 v[128:143], v[200:203], v[224:227], v[128:143]\n"
    "ds_read_b128 v[228:231], v242 offset:4096\n"
    "v_mfma_f32_32x32x16_bf16 v[144:159], v[204:207], a[112:115], v[144:159]\n"
    "v_mfma_f32_32x32x16_bf16 v[160:175], v[204:207], a[240:243], v[160:175]\n"
    "v_mfma_f32_32x32x16_bf16 v[176:191], v[204:207], v[224:227], v[176:191]\n"
    "s_waitcnt lgkmcnt(0)\n"
    "v_mfma_f32_32x32x16_bf16 v[0:15], v[208:211], a[116:119], v[0:15]\n"
    "ds_read_b64_tr_b16 v[192:193], v240 offset:24576\n"
    "v_mfma_f32_32x32x16_bf16 v[16:31], v[208:211], a[244:247], v[16:31]\n"
    "ds_read_b64_tr_b16 v[194:195], v240 offset:24832\n"
    "s_mov_b32 m0, 114688\n"
    "s_add_u32 s67, s76, 524288\n"
    "buffer_load_dwordx4 v248, s[44:47], s67 offen lds\n"
    "v_mfma_f32_32x32x16_bf16 v[32:47], v[208:211], v[228:231], v[32:47]\n"
    "ds_read_b64_tr_b16 v[196:197], v240 offset:25088\n"
    "s_mov_b32 m0, 115584\n"
    "s_nop 0\n"
    "buffer_load_dwordx4 v248, s[44:47], s67 offen offset:128 lds\n"
    "v_mfma_f32_32x32x16_bf16 v[48:63], v[212:215], a[116:119], v[48:63]\n"
    "ds_read_b64_tr_b16 v[198:199], v240 offset:25344\n"
    "s_mov_b32 m0, 116736\n"
    "s_add_u32 s67, s76, 589824\n"
    "buffer_load_dwordx4 v248, s[44:47], s67 offen lds\n"
    "v_mfma_f32_32x32x16_bf16 v[64:79], v[212:215], a[244:247], v[64:79]\n"
    "ds_read_b64_tr_b16 v[200:201], v240 offset:25600\n"
    "s_mov_b32 m0, 117632\n"
    "s_nop 0\n"
    "buffer_load_dwordx4 v248, s[44:47], s67 offen offset:128 lds\n"
    "v_mfma_f32_32x32x16_bf16 v[80:95], v[212:215], v[228:231], v[80:95]\n"
    "ds_read_b64_tr_b16 v[202:203], v240 offset:25856\n"
    "s_mov_b32 m0, 118784\n"
    "s_add_u32 s67, s76, 655360\n"
    "buffer_load_dwordx4 v248, s[44:47], s67 offen lds\n"
    "v_mfma_f32_32x32x16_bf16 v[96:111], v[216:219], a[116:119], v[96:111]\n"
    "ds_read_b64_tr_b16 v[204:205], v240 offset:26112\n"
    "s_mov_b32 m0, 119680\n"
    "s_nop 0\n"
    "buffer_load_dwordx4 v248, s[44:47], s67 offen offset:128 lds\n"
    "v_mfma_f32_32x32x16_bf16 v[112:127], v[216:219], a[244:247], v[112:127]\n"
    "ds_read_b64_tr_b16 v[206:207], v240 offset:26368\n"
    "s_mov_b32 m0, 120832\n"
    "s_add_u32 s67, s76, 720896\n"
    "buffer_load_dwordx4 v248, s[44:47], s67 offen lds\n"
    "v_mfma_f32_32x32x16_bf16 v[128:143], v[216:219], v[228:231], v[128:143]\n"
    "ds_read_b128 v[224:227], v241 offset:4608\n"
    "s_mov_b32 m0, 121728\n"
    "s_nop 0\n"
    "buffer_load_dwordx4 v248, s[44:47], s67 offen offset:128 lds\n"
    "v_mfma_f32_32x32x16_bf16 v[144:159], v[220:223], a[116:119], v[144:159]\n"
    "v_mfma_f32_32x32x16_bf16 v[160:175], v[220:223], a[244:247], v[160:175]\n"
    "v_mfma_f32_32x32x16_bf16 v[176:191], v[220:223], v[228:231], v[176:191]\n"
    "ds_read_b64_tr_b16 v[208:209], v240 offset:28672\n"
    "ds_read_b64_tr_b16 v[210:211], v240 offset:28928\n"
    "ds_read_b64_tr_b16 v[212:213], v240 offset:29184\n"
    "ds_read_b64_tr_b16 v[214:215], v240 offset:29440\n"
    "ds_read_b64_tr_b16 v[216:217], v240 offset:29696\n"
    "ds_read_b64_tr_b16 v[218:219], v240 offset:29952\n"
    "s_waitcnt lgkmcnt(14)\n"
    "ds_read_b64_tr_b16 v[220:221], v240 offset:30208\n"
    "s_waitcnt lgkmcnt(14)\n"
    "ds_read_b64_tr_b16 v[222:223], v240 offset:30464\n"
    "s_waitcnt lgkmcnt(14)\n"
    "ds_read_b128 v[228:231], v242 offset:4608\n"
    "v_mfma_f32_32x32x16_bf16 v[0:15], v[192:195], a[120:123], v[0:15]\n"
    "s_barrier\n"
    "s_add_u32 m0, s61, 0\n"
    "s_add_u32 s66, s63, 0\n"
    "buffer_load_dwordx4 v244, s[36:39], s66 offen nt lds\n"
    "buffer_load_dwordx4 v245, s[36:39], s66 offen offset:2048 nt lds\n"
    "v_mfma_f32_32x32x16_bf16 v[16:31], v[192:195], a[248:251], v[16:31]\n"
    "s_add_u32 m0, s61, 896\n"
    "s_nop 0\n"
    "buffer_load_dwordx4 v244, s[36:39], s66 offen offset:128 nt lds\n"
    "buffer_load_dwordx4 v245, s[36:39], s66 offen offset:2176 nt lds\n"
    "s_waitcnt lgkmcnt(9)\n"
    "v_mfma_f32_32x32x16_bf16 v[32:47], v[192:195], v[224:227], v[32:47]\n"
    "s_waitcnt lgkmcnt(7)\n"
    "v_mfma_f32_32x32x16_bf16 v[0:15], v[208:211], a[124:127], v[0:15]\n"
    "s_waitcnt vmcnt(32)\n"
    "s_waitcnt vmcnt(12)\n"
    "ds_read_b64_tr_b16 v[192:193], v240 offset:0\n"
    "ds_read_b64_tr_b16 v[194:195], v240 offset:256\n"
    "v_mfma_f32_32x32x16_bf16 v[16:31], v[208:211], a[252:255], v[16:31]\n"
    "s_mov_b32 m0, 122880\n"
    "s_add_u32 s67, s76, 786432\n"
    "buffer_load_dwordx4 v248, s[44:47], s67 offen lds\n"
    "s_mov_b32 m0, 123776\n"
    "s_nop 0\n"
    "buffer_load_dwordx4 v248, s[44:47], s67 offen offset:128 lds\n"
    "s_waitcnt lgkmcnt(2)\n"
    "v_mfma_f32_32x32x16_bf16 v[32:47], v[208:211], v[228:231], v[32:47]\n"
    "s_mov_b32 m0, 124928\n"
    "s_add_u32 s67, s76, 851968\n"
    "buffer_load_dwordx4 v248, s[44:47], s67 offen lds\n"
    "s_mov_b32 m0, 125824\n"
    "s_nop 0\n"
    "buffer_load_dwordx4 v248, s[44:47], s67 offen offset:128 lds\n"
    "v_mfma_f32_32x32x16_bf16 v[48:63], v[196:199], a[120:123], v[48:63]\n"
    "s_mov_b32 m0, 126976\n"
    "s_add_u32 s67, s76, 917504\n"
    "buffer_load_dwordx4 v248, s[44:47], s67 offen lds\n"
    "s_mov_b32 m0, 127872\n"
    "s_nop 0\n"
    "buffer_load_dwordx4 v248, s[44:47], s67 offen offset:128 lds\n"
    "v_mfma_f32_32x32x16_bf16 v[64:79], v[196:199], a[248:251], v[64:79]\n"
    "s_mov_b32 m0, 129024\n"
    "s_add_u32 s67, s76, 983040\n"
    "buffer_load_dwordx4 v248, s[44:47], s67 offen lds\n"
    "s_mov_b32 m0, 129920\n"
    "s_nop 0\n"
    "buffer_load_dwordx4 v248, s[44:47], s67 offen offset:128 lds\n"
    "v_mfma_f32_32x32x16_bf16 v[80:95], v[196:199], v[224:227], v[80:95]\n"
    "s_mov_b32 m0, 159744\n"
    "s_mov_b32 s67, 65664\n"
    "buffer_load_dwordx4 v249, s[52:55], s67 offen lds\n"
    "s_mov_b32 m0, 160768\n"
    "s_mov_b32 s67, 73856\n"
    "buffer_load_dwordx4 v250, s[52:55], s67 offen lds\n"
    "v_mfma_f32_32x32x16_bf16 v[48:63], v[212:215], a[124:127], v[48:63]\n"
    "ds_read_b64_tr_b16 v[196:197], v240 offset:512\n"
    "ds_read_b64_tr_b16 v[198:199], v240 offset:768\n"
    "s_mov_b32 m0, 161792\n"
    "s_mov_b32 s67, 82048\n"
    "buffer_load_dwordx4 v249, s[52:55], s67 offen lds\n"
    "v_mfma_f32_32x32x16_bf16 v[64:79], v[212:215], a[252:255], v[64:79]\n"
    "s_mov_b32 m0, 162816\n"
    "s_mov_b32 s67, 90240\n"
    "buffer_load_dwordx4 v250, s[52:55], s67 offen lds\n"
    "v_pk_mul_f32 v[0:1], v[0:1], s[72:73]\n"
    "v_mfma_f32_32x32x16_bf16 v[80:95], v[212:215], v[228:231], v[80:95]\n"
    "v_pk_mul_f32 v[2:3], v[2:3], s[72:73]\n"
    "v_pk_mul_f32 v[4:5], v[4:5], s[72:73]\n"
    "v_pk_mul_f32 v[6:7], v[6:7], s[72:73]\n"
    "v_mfma_f32_32x32x16_bf16 v[96:111], v[200:203], a[120:123], v[96:111]\n"
    "s_barrier\n"
    "v_pk_mul_f32 v[8:9], v[8:9], s[72:73]\n"
    "v_pk_mul_f32 v[10:11], v[10:11], s[72:73]\n"
    "v_pk_mul_f32 v[12:13], v[12:13], s[72:73]\n"
    "v_mfma_f32_32x32x16_bf16 v[112:127], v[200:203], a[248:251], v[112:127]\n"
    "v_pk_mul_f32 v[14:15], v[14:15], s[72:73]\n"
    "v_cvt_pk_bf16_f32 v232, v0, v1\n"
    "v_cvt_pk_bf16_f32 v233, v2, v3\n"
    "v_cvt_pk_bf16_f32 v234, v4, v5\n"
    "v_cvt_pk_bf16_f32 v235, v6, v7\n"
    "v_mfma_f32_32x32x16_bf16 v[128:143], v[200:203], v[224:227], v[128:143]\n"
    "v_cvt_pk_bf16_f32 v236, v8, v9\n"
    "v_cvt_pk_bf16_f32 v237, v10, v11\n"
    "v_cvt_pk_bf16_f32 v238, v12, v13\n"
    "v_cvt_pk_bf16_f32 v239, v14, v15\n"
    "v_mfma_f32_32x32x16_bf16 v[96:111], v[216:219], a[124:127], v[96:111]\n"
    "ds_read_b64_tr_b16 v[200:201], v240 offset:1024\n"
    "ds_read_b64_tr_b16 v[202:203], v240 offset:1280\n"
    "ds_write_b128 v243, v[232:235] offset:0\n"
    "v_mfma_f32_32x32x16_bf16 v[112:127], v[216:219], a[252:255], v[112:127]\n"
    "ds_write_b128 v243, v[236:239] offset:1024\n"
    "v_pk_mul_f32 v[16:17], v[16:17], s[72:73]\n"
    "v_mfma_f32_32x32x16_bf16 v[128:143], v[216:219], v[228:231], v[128:143]\n"
    "v_pk_mul_f32 v[18:19], v[18:19], s[72:73]\n"
    "v_pk_mul_f32 v[20:21], v[20:21], s[72:73]\n"
    "v_pk_mul_f32 v[22:23], v[22:23], s[72:73]\n"
    "v_mfma_f32_32x32x16_bf16 v[144:159], v[204:207], a[120:123], v[144:159]\n"
    "v_pk_mul_f32 v[24:25], v[24:25], s[72:73]\n"
    "v_pk_mul_f32 v[26:27], v[26:27], s[72:73]\n"
    "v_pk_mul_f32 v[28:29], v[28:29], s[72:73]\n"
    "v_mfma_f32_32x32x16_bf16 v[160:175], v[204:207], a[248:251], v[160:175]\n"
    "v_pk_mul_f32 v[30:31], v[30:31], s[72:73]\n"
    "v_cvt_pk_bf16_f32 v232, v16, v17\n"
    "v_cvt_pk_bf16_f32 v233, v18, v19\n"
    "v_cvt_pk_bf16_f32 v234, v20, v21\n"
    "v_cvt_pk_bf16_f32 v235, v22, v23\n"
    "s_add_u32 m0, s61, 49152\n"
    "s_add_u32 s66, s62, 114688\n"
    "buffer_load_dwordx4 v246, s[40:43], s66 offen nt lds\n"
    "buffer_load_dwordx4 v246, s[40:43], s66 offen offset:2048 nt lds\n"
    "v_mfma_f32_32x32x16_bf16 v[176:191], v[204:207], v[224:227], v[176:191]\n"
    "v_cvt_pk_bf16_f32 v236, v24, v25\n"
    "v_cvt_pk_bf16_f32 v237, v26, v27\n"
    "v_cvt_pk_bf16_f32 v238, v28, v29\n"
    "v_cvt_pk_bf16_f32 v239, v30, v31\n"
    "v_mfma_f32_32x32x16_bf16 v[144:159], v[220:223], a[124:127], v[144:159]\n"
    "ds_read_b64_tr_b16 v[204:205], v240 offset:1536\n"
    "ds_read_b64_tr_b16 v[206:207], v240 offset:1792\n"
    "ds_read_b128 v[224:227], v241 offset:0\n"
    "ds_write_b128 v243, v[232:235] offset:8192\n"
    "v_mfma_f32_32x32x16_bf16 v[160:175], v[220:223], a[252:255], v[160:175]\n"
    "ds_write_b128 v243, v[236:239] offset:9216\n"
    "v_pk_mul_f32 v[32:33], v[32:33], s[72:73]\n"
    "v_mfma_f32_32x32x16_bf16 v[176:191], v[220:223], v[228:231], v[176:191]\n"
    "v_pk_mul_f32 v[34:35], v[34:35], s[72:73]\n"
    "v_pk_mul_f32 v[36:37], v[36:37], s[72:73]\n"
    "v_pk_mul_f32 v[38:39], v[38:39], s[72:73]\n"
    "s_waitcnt lgkmcnt(11)\n"
    "v_mfma_f32_32x32x16_bf16 v[0:15], v[192:195], a[0:3], 0\n"
    "ds_read_b64_tr_b16 v[208:209], v240 offset:4096\n"
    "v_pk_mul_f32 v[40:41], v[40:41], s[72:73]\n"
    "v_pk_mul_f32 v[42:43], v[42:43], s[72:73]\n"
    "v_pk_mul_f32 v[44:45], v[44:45], s[72:73]\n"
    "v_mfma_f32_32x32x16_bf16 v[16:31], v[192:195], a[128:131], 0\n"
    "ds_read_b64_tr_b16 v[210:211], v240 offset:4352\n"
    "v_pk_mul_f32 v[46:47], v[46:47], s[72:73]\n"
    "v_cvt_pk_bf16_f32 v232, v32, v33\n"
    "v_cvt_pk_bf16_f32 v233, v34, v35\n"
    "v_cvt_pk_bf16_f32 v234, v36, v37\n"
    "v_cvt_pk_bf16_f32 v235, v38, v39\n"
    "v_cvt_pk_bf16_f32 v236, v40, v41\n"
    "v_cvt_pk_bf16_f32 v237, v42, v43\n"
    "v_cvt_pk_bf16_f32 v238, v44, v45\n"
    "v_cvt_pk_bf16_f32 v239, v46, v47\n"
    "ds_write_b128 v243, v[232:235] offset:16384\n"
    "ds_write_b128 v243, v[236:239] offset:17408\n"
    "s_waitcnt lgkmcnt(6)\n"
    "v_mfma_f32_32x32x16_bf16 v[32:47], v[192:195], v[224:227], 0\n"
    "ds_read_b64_tr_b16 v[212:213], v240 offset:4608\n"
    "v_pk_mul_f32 v[48:49], v[48:49], s[72:73]\n"
    "v_pk_mul_f32 v[50:51], v[50:51], s[72:73]\n"
    "v_pk_mul_f32 v[52:53], v[52:53], s[72:73]\n"
    "v_pk_mul_f32 v[54:55], v[54:55], s[72:73]\n"
    "v_pk_mul_f32 v[56:57], v[56:57], s[72:73]\n"
    "v_pk_mul_f32 v[58:59], v[58:59], s[72:73]\n"
    "v_pk_mul_f32 v[60:61], v[60:61], s[72:73]\n"
    "v_pk_mul_f32 v[62:63], v[62:63], s[72:73]\n"
    "v_cvt_pk_bf16_f32 v232, v48, v49\n"
    "v_cvt_pk_bf16_f32 v233, v50, v51\n"
    "v_cvt_pk_bf16_f32 v234, v52, v53\n"
    "v_cvt_pk_bf16_f32 v235, v54, v55\n"
    "v_cvt_pk_bf16_f32 v236, v56, v57\n"
    "v_cvt_pk_bf16_f32 v237, v58, v59\n"
    "v_cvt_pk_bf16_f32 v238, v60, v61\n"
    "v_cvt_pk_bf16_f32 v239, v62, v63\n"
    "ds_write_b128 v243, v[232:235] offset:2048\n"
    "ds_write_b128 v243, v[236:239] offset:3072\n"
    "s_add_u32 m0, s61, 50048\n"
    "s_nop 0\n"
    "buffer_load_dwordx4 v246, s[40:43], s66 offen offset:128 nt lds\n"
    "buffer_load_dwordx4 v246, s[40:43], s66 offen offset:2176 nt lds\n"
    "v_mfma_f32_32x32x16_bf16 v[48:63], v[196:199], a[0:3], 0\n"
    "s_barrier\n"
    "ds_read_b64_tr_b16 v[214:215], v240 offset:4864\n"
    "v_pk_mul_f32 v[64:65], v[64:65], s[72:73]\n"
    "v_pk_mul_f32 v[66:67], v[66:67], s[72:73]\n"
    "v_pk_mul_f32 v[68:69], v[68:69], s[72:73]\n"
    "v_pk_mul_f32 v[70:71], v[70:71], s[72:73]\n"
    "v_pk_mul_f32 v[72:73], v[72:73], s[72:73]\n"
    "v_pk_mul_f32 v[74:75], v[74:75], s[72:73]\n"
    "v_pk_mul_f32 v[76:77], v[76:77], s[72:73]\n"
    "v_pk_mul_f32 v[78:79], v[78:79], s[72:73]\n"
    "v_cvt_pk_bf16_f32 v232, v64, v65\n"
    "v_cvt_pk_bf16_f32 v233, v66, v67\n"
    "v_cvt_pk_bf16_f32 v234, v68, v69\n"
    "v_cvt_pk_bf16_f32 v235, v70, v71\n"
    "v_cvt_pk_bf16_f32 v236, v72, v73\n"
    "v_cvt_pk_bf16_f32 v237, v74, v75\n"
    "v_cvt_pk_bf16_f32 v238, v76, v77\n"
    "v_cvt_pk_bf16_f32 v239, v78, v79\n"
    "ds_write_b128 v243, v[232:235] offset:10240\n"
    "ds_write_b128 v243, v[236:239] offset:11264\n"
    "s_mov_b32 m0, 65536\n"
    "s_add_u32 s66, s65, 0\n"
    "buffer_load_dwordx4 v247, s[48:51], s66 offen lds\n"
    "s_mov_b32 m0, 66432\n"
    "s_nop 0\n"
    "buffer_load_dwordx4 v247, s[48:51], s66 offen offset:128 lds\n"
    "s_mov_b32 m0, 67328\n"
    "s_nop 0\n"
    "buffer_load_dwordx4 v247, s[48:51], s66 offen offset:256 lds\n"
    "v_mfma_f32_32x32x16_bf16 v[64:79], v[196:199], a[128:131], 0\n"
    "ds_read_b64_tr_b16 v[216:217], v240 offset:5120\n"
    "v_pk_mul_f32 v[80:81], v[80:81], s[72:73]\n"
    "v_pk_mul_f32 v[82:83], v[82:83], s[72:73]\n"
    "v_pk_mul_f32 v[84:85], v[84:85], s[72:73]\n"
    "v_pk_mul_f32 v[86:87], v[86:87], s[72:73]\n"
    "v_pk_mul_f32 v[88:89], v[88:89], s[72:73]\n"
    "v_pk_mul_f32 v[90:91], v[90:91], s[72:73]\n"
    "v_pk_mul_f32 v[92:93], v[92:93], s[72:73]\n"
    "v_pk_mul_f32 v[94:95], v[94:95], s[72:73]\n"
    "v_cvt_pk_bf16_f32 v232, v80, v81\n"
    "v_cvt_pk_bf16_f32 v233, v82, v83\n"
    "v_cvt_pk_bf16_f32 v234, v84, v85\n"
    "v_cvt_pk_bf16_f32 v235, v86, v87\n"
    "v_cvt_pk_bf16_f32 v236, v88, v89\n"
    "v_cvt_pk_bf16_f32 v237, v90, v91\n"
    "v_cvt_pk_bf16_f32 v238, v92, v93\n"
    "v_cvt_pk_bf16_f32 v239, v94, v95\n"
    "ds_write_b128 v243, v[232:235] offset:18432\n"
    "ds_write_b128 v243, v[236:239] offset:19456\n"
    "s_mov_b32 m0, 68224\n"
    "s_nop 0\n"
    "buffer_load_dwordx4 v247, s[48:51], s66 offen offset:384 lds\n"
    "s_mov_b32 m0, 69120\n"
    "s_nop 0\n"
    "buffer_load_dwordx4 v247, s[48:51], s66 offen offset:512 lds\n"
    "s_mov_b32 m0, 70016\n"
    "s_nop 0\n"
    "buffer_load_dwordx4 v247, s[48:51], s66 offen offset:640 lds\n"
    "s_mov_b32 m0, 70912\n"
    "s_nop 0\n"
    "buffer_load_dwordx4 v247, s[48:51], s66 offen offset:768 lds\n"
    "v_mfma_f32_32x32x16_bf16 v[80:95], v[196:199], v[224:227], 0\n"
    "s_waitcnt lgkmcnt(14)\n"
    "ds_read_b64_tr_b16 v[218:219], v240 offset:5376\n"
    "v_pk_mul_f32 v[96:97], v[96:97], s[72:73]\n"
    "v_pk_mul_f32 v[98:99], v[98:99], s[72:73]\n"
    "v_pk_mul_f32 v[100:101], v[100:101], s[72:73]\n"
    "v_pk_mul_f32 v[102:103], v[102:103], s[72:73]\n"
    "v_pk_mul_f32 v[104:105], v[104:105], s[72:73]\n"
    "v_pk_mul_f32 v[106:107], v[106:107], s[72:73]\n"
    "v_pk_mul_f32 v[108:109], v[108:109], s[72:73]\n"
    "v_pk_mul_f32 v[110:111], v[110:111], s[72:73]\n"
    "v_cvt_pk_bf16_f32 v232, v96, v97\n"
    "v_cvt_pk_bf16_f32 v233, v98, v99\n"
    "v_cvt_pk_bf16_f32 v234, v100, v101\n"
    "v_cvt_pk_bf16_f32 v235, v102, v103\n"
    "v_cvt_pk_bf16_f32 v236, v104, v105\n"
    "v_cvt_pk_bf16_f32 v237, v106, v107\n"
    "v_cvt_pk_bf16_f32 v238, v108, v109\n"
    "v_cvt_pk_bf16_f32 v239, v110, v111\n"
    "s_waitcnt lgkmcnt(14)\n"
    "ds_write_b128 v243, v[232:235] offset:4096\n"
    "s_waitcnt lgkmcnt(14)\n"
    "ds_write_b128 v243, v[236:239] offset:5120\n"
    "s_mov_b32 m0, 71808\n"
    "s_nop 0\n"
    "buffer_load_dwordx4 v247, s[48:51], s66 offen offset:896 lds\n"
    "s_mov_b32 m0, 73728\n"
    "s_add_u32 s66, s65, 8192\n"
    "buffer_load_dwordx4 v247, s[48:51], s66 offen lds\n"
    "s_mov_b32 m0, 74624\n"
    "s_nop 0\n"
    "buffer_load_dwordx4 v247, s[48:51], s66 offen offset:128 lds\n"
    "s_mov_b32 m0, 75520\n"
    "s_nop 0\n"
    "buffer_load_dwordx4 v247, s[48:51], s66 offen offset:256 lds\n"
    "v_mfma_f32_32x32x16_bf16 v[96:111], v[200:203], a[0:3], 0\n"
    "s_waitcnt lgkmcnt(14)\n"
    "ds_read_b64_tr_b16 v[220:221], v240 offset:5632\n"
    "v_pk_mul_f32 v[112:113], v[112:113], s[72:73]\n"
    "v_pk_mul_f32 v[114:115], v[114:115], s[72:73]\n"
    "v_pk_mul_f32 v[116:117], v[116:117], s[72:73]\n"
    "v_pk_mul_f32 v[118:119], v[118:119], s[72:73]\n"
    "v_pk_mul_f32 v[120:121], v[120:121], s[72:73]\n"
    "v_pk_mul_f32 v[122:123], v[122:123], s[72:73]\n"
    "v_pk_mul_f32 v[124:125], v[124:125], s[72:73]\n"
    "v_pk_mul_f32 v[126:127], v[126:127], s[72:73]\n"
    "v_cvt_pk_bf16_f32 v232, v112, v113\n"
    "v_cvt_pk_bf16_f32 v233, v114, v115\n"
    "v_cvt_pk_bf16_f32 v234, v116, v117\n"
    "v_cvt_pk_bf16_f32 v235, v118, v119\n"
    "v_cvt_pk_bf16_f32 v236, v120, v121\n"
    "v_cvt_pk_bf16_f32 v237, v122, v123\n"
    "v_cvt_pk_bf16_f32 v238, v124, v125\n"
    "v_cvt_pk_bf16_f32 v239, v126, v127\n"
    "s_waitcnt lgkmcnt(14)\n"
    "ds_write_b128 v243, v[232:235] offset:12288\n"
    "s_waitcnt lgkmcnt(14)\n"
    "ds_write_b128 v243, v[236:239] offset:13312\n"
    "s_mov_b32 m0, 76416\n"
    "s_nop 0\n"
    "buffer_load_dwordx4 v247, s[48:51], s66 offen offset:384 lds\n"
    "s_mov_b32 m0, 77312\n"
    "s_nop 0\n"
    "buffer_load_dwordx4 v247, s[48:51], s66 offen offset:512 lds\n"
    "s_mov_b32 m0, 78208\n"
    "s_nop 0\n"
    "buffer_load_dwordx4 v247, s[48:51], s66 offen offset:640 lds\n"
    "s_mov_b32 m0, 79104\n"
    "s_nop 0\n"
    "buffer_load_dwordx4 v247, s[48:51], s66 offen offset:768 lds\n"
    "v_mfma_f32_32x32x16_bf16 v[112:127], v[200:203], a[128:131], 0\n"
    "s_waitcnt lgkmcnt(14)\n"
    "ds_read_b64_tr_b16 v[222:223], v240 offset:5888\n"
    "v_pk_mul_f32 v[128:129], v[128:129], s[72:73]\n"
    "v_pk_mul_f32 v[130:131], v[130:131], s[72:73]\n"
    "v_pk_mul_f32 v[132:133], v[132:133], s[72:73]\n"
    "v_pk_mul_f32 v[134:135], v[134:135], s[72:73]\n"
    "v_pk_mul_f32 v[136:137], v[136:137], s[72:73]\n"
    "v_pk_mul_f32 v[138:139], v[138:139], s[72:73]\n"
    "v_pk_mul_f32 v[140:141], v[140:141], s[72:73]\n"
    "v_pk_mul_f32 v[142:143], v[142:143], s[72:73]\n"
    "v_cvt_pk_bf16_f32 v232, v128, v129\n"
    "v_cvt_pk_bf16_f32 v233, v130, v131\n"
    "v_cvt_pk_bf16_f32 v234, v132, v133\n"
    "v_cvt_pk_bf16_f32 v235, v134, v135\n"
    "v_cvt_pk_bf16_f32 v236, v136, v137\n"
    "v_cvt_pk_bf16_f32 v237, v138, v139\n"
    "v_cvt_pk_bf16_f32 v238, v140, v141\n"
    "v_cvt_pk_bf16_f32 v239, v142, v143\n"
    "s_waitcnt lgkmcnt(14)\n"
    "ds_write_b128 v243, v[232:235] offset:20480\n"
    "s_waitcnt lgkmcnt(14)\n"
    "ds_write_b128 v243, v[236:239] offset:21504\n"
    "s_mov_b32 m0, 80000\n"
    "s_nop 0\n"
    "buffer_load_dwordx4 v247, s[48:51], s66 offen offset:896 lds\n"
    "v_mfma_f32_32x32x16_bf16 v[128:143], v[200:203], v[224:227], 0\n"
    "s_barrier\n"
    "s_waitcnt lgkmcnt(14)\n"
    "ds_read_b128 v[228:231], v242 offset:0\n"
    "v_pk_mul_f32 v[144:145], v[144:145], s[72:73]\n"
    "v_pk_mul_f32 v[146:147], v[146:147], s[72:73]\n"
    "v_pk_mul_f32 v[148:149], v[148:149], s[72:73]\n"
    "v_pk_mul_f32 v[150:151], v[150:151], s[72:73]\n"
    "v_pk_mul_f32 v[152:153], v[152:153], s[72:73]\n"
    "v_pk_mul_f32 v[154:155], v[154:155], s[72:73]\n"
    "v_pk_mul_f32 v[156:157], v[156:157], s[72:73]\n"
    "v_pk_mul_f32 v[158:159], v[158:159], s[72:73]\n"
    "v_cvt_pk_bf16_f32 v232, v144, v145\n"
    "v_cvt_pk_bf16_f32 v233, v146, v147\n"
    "v_cvt_pk_bf16_f32 v234, v148, v149\n"
    "v_cvt_pk_bf16_f32 v235, v150, v151\n"
    "v_cvt_pk_bf16_f32 v236, v152, v153\n"
    "v_cvt_pk_bf16_f32 v237, v154, v155\n"
    "v_cvt_pk_bf16_f32 v238, v156, v157\n"
    "v_cvt_pk_bf16_f32 v239, v158, v159\n"
    "s_waitcnt lgkmcnt(14)\n"
    "ds_write_b128 v243, v[232:235] offset:6144\n"
    "s_waitcnt lgkmcnt(14)\n"
    "ds_write_b128 v243, v[236:239] offset:7168\n"
    "v_mfma_f32_32x32x16_bf16 v[144:159], v[204:207], a[0:3], 0\n"
    "v_pk_mul_f32 v[160:161], v[160:161], s[72:73]\n"
    "v_pk_mul_f32 v[162:163], v[162:163], s[72:73]\n"
    "v_pk_mul_f32 v[164:165], v[164:165], s[72:73]\n"
    "v_pk_mul_f32 v[166:167], v[166:167], s[72:73]\n"
    "v_pk_mul_f32 v[168:169], v[168:169], s[72:73]\n"
    "v_pk_mul_f32 v[170:171], v[170:171], s[72:73]\n"
    "v_pk_mul_f32 v[172:173], v[172:173], s[72:73]\n"
    "v_pk_mul_f32 v[174:175], v[174:175], s[72:73]\n"
    "v_cvt_pk_bf16_f32 v232, v160, v161\n"
    "v_cvt_pk_bf16_f32 v233, v162, v163\n"
    "v_cvt_pk_bf16_f32 v234, v164, v165\n"
    "v_cvt_pk_bf16_f32 v235, v166, v167\n"
    "v_cvt_pk_bf16_f32 v236, v168, v169\n"
    "v_cvt_pk_bf16_f32 v237, v170, v171\n"
    "v_cvt_pk_bf16_f32 v238, v172, v173\n"
    "v_cvt_pk_bf16_f32 v239, v174, v175\n"
    "s_waitcnt lgkmcnt(14)\n"
    "ds_write_b128 v243, v[232:235] offset:14336\n"
    "s_waitcnt lgkmcnt(14)\n"
    "ds_write_b128 v243, v[236:239] offset:15360\n"
    "s_add_u32 m0, s61, 16384\n"
    "s_add_u32 s66, s63, 16384\n"
    "buffer_load_dwordx4 v244, s[36:39], s66 offen nt lds\n"
    "buffer_load_dwordx4 v245, s[36:39], s66 offen offset:2048 nt lds\n"
    "v_mfma_f32_32x32x16_bf16 v[160:175], v[204:207], a[128:131], 0\n"
    "v_pk_mul_f32 v[176:177], v[176:177], s[72:73]\n"
    "v_pk_mul_f32 v[178:179], v[178:179], s[72:73]\n"
    "v_pk_mul_f32 v[180:181], v[180:181], s[72:73]\n"
    "v_pk_mul_f32 v[182:183], v[182:183], s[72:73]\n"
    "v_pk_mul_f32 v[184:185], v[184:185], s[72:73]\n"
    "v_pk_mul_f32 v[186:187], v[186:187], s[72:73]\n"
    "v_pk_mul_f32 v[188:189], v[188:189], s[72:73]\n"
    "v_pk_mul_f32 v[190:191], v[190:191], s[72:73]\n"
    "v_cvt_pk_bf16_f32 v232, v176, v177\n"
    "v_cvt_pk_bf16_f32 v233, v178, v179\n"
    "v_cvt_pk_bf16_f32 v234, v180, v181\n"
    "v_cvt_pk_bf16_f32 v235, v182, v183\n"
    "v_cvt_pk_bf16_f32 v236, v184, v185\n"
    "v_cvt_pk_bf16_f32 v237, v186, v187\n"
    "v_cvt_pk_bf16_f32 v238, v188, v189\n"
    "v_cvt_pk_bf16_f32 v239, v190, v191\n"
    "s_waitcnt lgkmcnt(14)\n"
    "ds_write_b128 v243, v[232:235] offset:22528\n"
    "s_waitcnt lgkmcnt(14)\n"
    "ds_write_b128 v243, v[236:239] offset:23552\n"
    "v_mfma_f32_32x32x16_bf16 v[176:191], v[204:207], v[224:227], 0\n"
    "s_add_u32 m0, s61, 17280\n"
    "s_nop 0\n"
    "buffer_load_dwordx4 v244, s[36:39], s66 offen offset:128 nt lds\n"
    "buffer_load_dwordx4 v245, s[36:39], s66 offen offset:2176 nt lds\n"
    "s_waitcnt lgkmcnt(0)\n"
    "s_waitcnt vmcnt(36)\n"
    "s_barrier\n"
    "s_add_u32 m0, s61, 32768\n"
    "s_add_u32 s66, s63, 0\n"
    "buffer_load_dwordx4 v246, s[40:43], s66 offen nt lds\n"
    "buffer_load_dwordx4 v246, s[40:43], s66 offen offset:2048 nt lds\n"
    "s_add_u32 m0, s61, 33664\n"
    "s_nop 0\n"
    "buffer_load_dwordx4 v246, s[40:43], s66 offen offset:128 nt lds\n"
    "buffer_load_dwordx4 v246, s[40:43], s66 offen offset:2176 nt lds\n"
    "s_mov_b32 s64, s65\n"
    "s_mov_b32 s62, s63\n"
    "s_add_u32 s63, s63, 131072\n"
    "s_add_u32 s65, s65, 131072\n"
    "s_sub_u32 s68, s68, 1\n"
    "s_cmp_eq_u32 s68, 1\n"
    "s_cselect_b32 s63, 268435456, s63\n"
    "s_cselect_b32 s65, 268435456, s65\n"
    "s_mov_b32 s75, s76\n"
    "s_add_u32 s76, s76, 256\n"
    "s_cmp_eq_u32 s68, 2\n"
    "s_cselect_b32 s76, 268435456, s76\n"
    "L_w3_loop:\n"
    "v_mfma_f32_32x32x16_bf16 v[0:15], v[208:211], a[4:7], v[0:15]\n"
    "ds_read_b64_tr_b16 v[192:193], v240 offset:8192\n"
    "v_mfma_f32_32x32x16_bf16 v[16:31], v[208:211], a[132:135], v[16:31]\n"
    "ds_read_b64_tr_b16 v[194:195], v240 offset:8448\n"
    "s_mov_b32 m0, 98304\n"
    "s_add_u32 s67, s75, 1048576\n"
    "buffer_load_dwordx4 v248, s[44:47], s67 offen lds\n"
    "v_mfma_f32_32x32x16_bf16 v[32:47], v[208:211], v[228:231], v[32:47]\n"
    "ds_read_b64_tr_b16 v[196:197], v240 offset:8704\n"
    "s_mov_b32 m0, 99200\n"
    "s_nop 0\n"
    "buffer_load_dwordx4 v248, s[44:47], s67 offen offset:128 lds\n"
    "v_mfma_f32_32x32x16_bf16 v[48:63], v[212:215], a[4:7], v[48:63]\n"
    "ds_read_b64_tr_b16 v[198:199], v240 offset:8960\n"
    "s_mov_b32 m0, 100352\n"
    "s_add_u32 s67, s75, 1114112\n"
    "buffer_load_dwordx4 v248, s[44:47], s67 offen lds\n"
    "v_mfma_f32_32x32x16_bf16 v[64:79], v[212:215], a[132:135], v[64:79]\n"
    "ds_read_b64_tr_b16 v[200:201], v240 offset:9216\n"
    "s_mov_b32 m0, 101248\n"
    "s_nop 0\n"
    "buffer_load_dwordx4 v248, s[44:47], s67 offen offset:128 lds\n"
    "v_mfma_f32_32x32x16_bf16 v[80:95], v[212:215], v[228:231], v[80:95]\n"
    "ds_read_b64_tr_b16 v[202:203], v240 offset:9472\n"
    "s_mov_b32 m0, 102400\n"
    "s_add_u32 s67, s75, 1179648\n"
    "buffer_load_dwordx4 v248, s[44:47], s67 offen lds\n"
    "v_mfma_f32_32x32x16_bf16 v[96:111], v[216:219], a[4:7], v[96:111]\n"
    "ds_read_b64_tr_b16 v[204:205], v240 offset:9728\n"
    "s_mov_b32 m0, 103296\n"
    "s_nop 0\n"
    "buffer_load_dwordx4 v248, s[44:47], s67 offen offset:128 lds\n"
    "v_mfma_f32_32x32x16_bf16 v[112:127], v[216:219], a[132:135], v[112:127]\n"
    "ds_read_b64_tr_b16 v[206:207], v240 offset:9984\n"
    "s_mov_b32 m0, 104448\n"
    "s_add_u32 s67, s75, 1245184\n"
    "buffer_load_dwordx4 v248, s[44:47], s67 offen lds\n"
    "v_mfma_f32_32x32x16_bf16 v[128:143], v[216:219], v[228:231], v[128:143]\n"
    "ds_read_b128 v[224:227], v241 offset:512\n"
    "s_mov_b32 m0, 105344\n"
    "s_nop 0\n"
    "buffer_load_dwordx4 v248, s[44:47], s67 offen offset:128 lds\n"
    "v_mfma_f32_32x32x16_bf16 v[144:159], v[220:223], a[4:7], v[144:159]\n"
    "v_mfma_f32_32x32x16_bf16 v[160:175], v[220:223], a[132:135], v[160:175]\n"
    "v_mfma_f32_32x32x16_bf16 v[176:191], v[220:223], v[228:231], v[176:191]\n"
    "s_waitcnt lgkmcnt(0)\n"
    "v_mfma_f32_32x32x16_bf16 v[0:15], v[192:195], a[8:11], v[0:15]\n"
    "ds_read_b64_tr_b16 v[208:209], v240 offset:12288\n"
    "v_mfma_f32_32x32x16_bf16 v[16:31], v[192:195], a[136:139], v[16:31]\n"
    "ds_read_b64_tr_b16 v[210:211], v240 offset:12544\n"
    "v_mfma_f32_32x32x16_bf16 v[32:47], v[192:195], v[224:227], v[32:47]\n"
    "ds_read_b64_tr_b16 v[212:213], v240 offset:12800\n"
    "v_mfma_f32_32x32x16_bf16 v[48:63], v[196:199], a[8:11], v[48:63]\n"
    "ds_read_b64_tr_b16 v[214:215], v240 offset:13056\n"
    "v_mfma_f32_32x32x16_bf16 v[64:79], v[196:199], a[136:139], v[64:79]\n"
    "ds_read_b64_tr_b16 v[216:217], v240 offset:13312\n"
    "v_mfma_f32_32x32x16_bf16 v[80:95], v[196:199], v[224:227], v[80:95]\n"
    "ds_read_b64_tr_b16 v[218:219], v240 offset:13568\n"
    "v_mfma_f32_32x32x16_bf16 v[96:111], v[200:203], a[8:11], v[96:111]\n"
    "s_waitcnt vmcnt(32)\n"
    "s_barrier\n"
    "ds_read_b64_tr_b16 v[220:221], v240 offset:13824\n"
    "s_mov_b32 m0, 81920\n"
    "s_add_u32 s66, s64, 16384\n"
    "buffer_load_dwordx4 v247, s[48:51], s66 offen lds\n"
    "v_mfma_f32_32x32x16_bf16 v[112:127], v[200:203], a[136:139], v[112:127]\n"
    "ds_read_b64_tr_b16 v[222:223], v240 offset:14080\n"
    "s_mov_b32 m0, 82816\n"
    "s_nop 0\n"
    "buffer_load_dwordx4 v247, s[48:51], s66 offen offset:128 lds\n"
    "v_mfma_f32_32x32x16_bf16 v[128:143], v[200:203], v[224:227], v[128:143]\n"
    "ds_read_b128 v[228:231], v242 offset:512\n"
    "s_mov_b32 m0, 83712\n"
    "s_nop 0\n"
    "buffer_load_dwordx4 v247, s[48:51], s66 offen offset:256 lds\n"
    "v_mfma_f32_32x32x16_bf16 v[144:159], v[204:207], a[8:11], v[144:159]\n"
    "s_mov_b32 m0, 84608\n"
    "s_nop 0\n"
    "buffer_load_dwordx4 v247, s[48:51], s66 offen offset:384 lds\n"
    "v_mfma_f32_32x32x16_bf16 v[160:175], v[204:207], a[136:139], v[160:175]\n"
    "s_mov_b32 m0, 85504\n"
    "s_nop 0\n"
    "buffer_load_dwordx4 v247, s[48:51], s66 offen offset:512 lds\n"
    "v_mfma_f32_32x32x16_bf16 v[176:191], v[204:207], v[224:227], v[176:191]\n"
    "s_mov_b32 m0, 86400\n"
    "s_nop 0\n"
    "buffer_load_dwordx4 v247, s[48:51], s66 offen offset:640 lds\n"
    "s_waitcnt lgkmcnt(0)\n"
    "v_mfma_f32_32x32x16_bf16 v[0:15], v[208:211], a[12:15], v[0:15]\n"
    "ds_read_b64_tr_b16 v[192:193], v240 offset:16384\n"
    "s_mov_b32 m0, 87296\n"
    "s_nop 0\n"
    "buffer_load_dwordx4 v247, s[48:51], s66 offen offset:768 lds\n"
    "v_mfma_f32_32x32x16_bf16 v[16:31], v[208:211], a[140:143], v[16:31]\n"
    "ds_read_b64_tr_b16 v[194:195], v240 offset:16640\n"
    "s_mov_b32 m0, 106496\n"
    "s_add_u32 s67, s75, 1310720\n"
    "buffer_load_dwordx4 v248, s[44:47], s67 offen lds\n"
    "v_mfma_f32_32x32x16_bf16 v[32:47], v[208:211], v[228:231], v[32:47]\n"
    "ds_read_b64_tr_b16 v[196:197], v240 offset:16896\n"
    "s_mov_b32 m0, 107392\n"
    "s_nop 0\n"
    "buffer_load_dwordx4 v248, s[44:47], s67 offen offset:128 lds\n"
    "v_mfma_f32_32x32x16_bf16 v[48:63], v[212:215], a[12:15], v[48:63]\n"
    "ds_read_b64_tr_b16 v[198:199], v240 offset:17152\n"
    "s_mov_b32 m0, 108544\n"
    "s_add_u32 s67, s75, 1376256\n"
    "buffer_load_dwordx4 v248, s[44:47], s67 offen lds\n"
    "s_mov_b32 m0, 88192\n"
    "s_nop 0\n"
    "buffer_load_dwordx4 v247, s[48:51], s66 offen offset:896 lds\n"
    "v_mfma_f32_32x32x16_bf16 v[64:79], v[212:215], a[140:143], v[64:79]\n"
    "ds_read_b64_tr_b16 v[200:201], v240 offset:17408\n"
    "s_mov_b32 m0, 109440\n"
    "s_nop 0\n"
    "buffer_load_dwordx4 v248, s[44:47], s67 offen offset:128 lds\n"
    "s_mov_b32 m0, 90112\n"
    "s_add_u32 s66, s64, 24576\n"
    "buffer_load_dwordx4 v247, s[48:51], s66 offen lds\n"
    "v_mfma_f32_32x32x16_bf16 v[80:95], v[212:215], v[228:231], v[80:95]\n"
    "ds_read_b64_tr_b16 v[202:203], v240 offset:17664\n"
    "s_mov_b32 m0, 110592\n"
    "s_add_u32 s67, s75, 1441792\n"
    "buffer_load_dwordx4 v248, s[44:47], s67 offen lds\n"
    "s_mov_b32 m0, 91008\n"
    "s_nop 0\n"
    "buffer_load_dwordx4 v247, s[48:51], s66 offen offset:128 lds\n"
    "v_mfma_f32_32x32x16_bf16 v[96:111], v[216:219], a[12:15], v[96:111]\n"
    "ds_read_b64_tr_b16 v[204:205], v240 offset:17920\n"
    "s_mov_b32 m0, 111488\n"
    "s_nop 0\n"
    "buffer_load_dwordx4 v248, s[44:47], s67 offen offset:128 lds\n"
    "v_mfma_f32_32x32x16_bf16 v[112:127], v[216:219], a[140:143], v[112:127]\n"
    "ds_read_b64_tr_b16 v[206:207], v240 offset:18176\n"
    "s_mov_b32 m0, 112640\n"
    "s_add_u32 s67, s75, 1507328\n"
    "buffer_load_dwordx4 v248, s[44:47], s67 offen lds\n"
    "s_mov_b32 m0, 91904\n"
    "s_nop 0\n"
    "buffer_load_dwordx4 v247, s[48:51], s66 offen offset:256 lds\n"
    "v_mfma_f32_32x32x16_bf16 v[128:143], v[216:219], v[228:231], v[128:143]\n"
    "ds_read_b128 v[224:227], v241 offset:4096\n"
    "s_mov_b32 m0, 113536\n"
    "s_nop 0\n"
    "buffer_load_dwordx4 v248, s[44:47], s67 offen offset:128 lds\n"
    "s_mov_b32 m0, 92800\n"
    "s_nop 0\n"
    "buffer_load_dwordx4 v247, s[48:51], s66 offen offset:384 lds\n"
    "v_mfma_f32_32x32x16_bf16 v[144:159], v[220:223], a[12:15], v[144:159]\n"
    "s_mov_b32 m0, 155648\n"
    "s_mov_b32 s67, 65792\n"
    "buffer_load_dwordx4 v249, s[52:55], s67 offen lds\n"
    "s_mov_b32 m0, 93696\n"
    "s_nop 0\n"
    "buffer_load_dwordx4 v247, s[48:51], s66 offen offset:512 lds\n"
    "v_mfma_f32_32x32x16_bf16 v[160:175], v[220:223], a[140:143], v[160:175]\n"
    "s_mov_b32 m0, 156672\n"
    "s_mov_b32 s67, 73984\n"
    "buffer_load_dwordx4 v250, s[52:55], s67 offen lds\n"
    "s_mov_b32 m0, 94592\n"
    "s_nop 0\n"
    "buffer_load_dwordx4 v247, s[48:51], s66 offen offset:640 lds\n"
    "v_mfma_f32_32x32x16_bf16 v[176:191], v[220:223], v[228:231], v[176:191]\n"
    "s_mov_b32 m0, 157696\n"
    "s_mov_b32 s67, 82176\n"
    "buffer_load_dwordx4 v249, s[52:55], s67 offen lds\n"
    "s_mov_b32 m0, 158720\n"
    "s_mov_b32 s67, 90368\n"
    "buffer_load_dwordx4 v250, s[52:55], s67 offen lds\n"
    "s_mov_b32 m0, 95488\n"
    "s_nop 0\n"
    "buffer_load_dwordx4 v247, s[48:51], s66 offen offset:768 lds\n"
    "s_waitcnt lgkmcnt(0)\n"
    "v_mfma_f32_32x32x16_bf16 v[0:15], v[192:195], a[16:19], v[0:15]\n"
    "ds_read_b64_tr_b16 v[208:209], v240 offset:20480\n"
    "s_mov_b32 m0, 96384\n"
    "s_nop 0\n"
    "buffer_load_dwordx4 v247, s[48:51], s66 offen offset:896 lds\n"
    "v_mfma_f32_32x32x16_bf16 v[16:31], v[192:195], a[144:147], v[16:31]\n"
    "ds_read_b64_tr_b16 v[210:211], v240 offset:20736\n"
    "v_mfma_f32_32x32x16_bf16 v[32:47], v[192:195], v[224:227], v[32:47]\n"
    "s_waitcnt vmcnt(44)\n"
    "s_barrier\n"
    "ds_read_b64_tr_b16 v[212:213], v240 offset:20992\n"
    "s_add_u32 s85, s76, s83\n"
    "buffer_load_dword v253, v251, s[44:47], s85 offen\n"
    "v_mfma_f32_32x32x16_bf16 v[48:63], v[196:199], a[16:19], v[48:63]\n"
    "ds_read_b64_tr_b16 v[214:215], v240 offset:21248\n"
    "s_add_u32 s85, s65, s84\n"
    "buffer_load_dword v253, v252, s[48:51], s85 offen\n"
    "v_mfma_f32_32x32x16_bf16 v[64:79], v[196:199], a[144:147], v[64:79]\n"
    "ds_read_b64_tr_b16 v[216:217], v240 offset:21504\n"
    "s_add_u32 m0, s61, 0\n"
    "s_add_u32 s66, s62, 32768\n"
    "buffer_load_dwordx4 v244, s[36:39], s66 offen nt lds\n"
    "buffer_load_dwordx4 v245, s[36:39], s66 offen offset:2048 nt lds\n"
    "v_mfma_f32_32x32x16_bf16 v[80:95], v[196:199], v[224:227], v[80:95]\n"
    "ds_read_b64_tr_b16 v[218:219], v240 offset:21760\n"
    "s_add_u32 m0, s61, 896\n"
    "s_nop 0\n"
    "buffer_load_dwordx4 v244, s[36:39], s66 offen offset:128 nt lds\n"
    "buffer_load_dwordx4 v245, s[36:39], s66 offen offset:2176 nt lds\n"
    "v_mfma_f32_32x32x16_bf16 v[96:111], v[200:203], a[16:19], v[96:111]\n"
    "ds_read_b64_tr_b16 v[220:221], v240 offset:22016\n"
    "v_mfma_f32_32x32x16_bf16 v[112:127], v[200:203], a[144:147], v[112:127]\n"
    "ds_read_b64_tr_b16 v[222:223], v240 offset:22272\n"
    "v_mfma_f32_32x32x16_bf16 v[128:143], v[200:203], v[224:227], v[128:143]\n"
    "ds_read_b128 v[228:231], v242 offset:4096\n"
    "v_mfma_f32_32x32x16_bf16 v[144:159], v[204:207], a[16:19], v[144:159]\n"
    "v_mfma_f32_32x32x16_bf16 v[160:175], v[204:207], a[144:147], v[160:175]\n"
    "v_mfma_f32_32x32x16_bf16 v[176:191], v[204:207], v[224:227], v[176:191]\n"
    "s_waitcnt lgkmcnt(0)\n"
    "v_mfma_f32_32x32x16_bf16 v[0:15], v[208:211], a[20:23], v[0:15]\n"
    "ds_read_b64_tr_b16 v[192:193], v240 offset:24576\n"
    "v_mfma_f32_32x32x16_bf16 v[16:31], v[208:211], a[148:151], v[16:31]\n"
    "ds_read_b64_tr_b16 v[194:195], v240 offset:24832\n"
    "s_mov_b32 m0, 114688\n"
    "s_add_u32 s67, s75, 1572864\n"
    "buffer_load_dwordx4 v248, s[44:47], s67 offen lds\n"
    "v_mfma_f32_32x32x16_bf16 v[32:47], v[208:211], v[228:231], v[32:47]\n"
    "ds_read_b64_tr_b16 v[196:197], v240 offset:25088\n"
    "s_mov_b32 m0, 115584\n"
    "s_nop 0\n"
    "buffer_load_dwordx4 v248, s[44:47], s67 offen offset:128 lds\n"
    "v_mfma_f32_32x32x16_bf16 v[48:63], v[212:215], a[20:23], v[48:63]\n"
    "ds_read_b64_tr_b16 v[198:199], v240 offset:25344\n"
    "s_mov_b32 m0, 116736\n"
    "s_add_u32 s67, s75, 1638400\n"
    "buffer_load_dwordx4 v248, s[44:47], s67 offen lds\n"
    "v_mfma_f32_32x32x16_bf16 v[64:79], v[212:215], a[148:151], v[64:79]\n"
    "ds_read_b64_tr_b16 v[200:201], v240 offset:25600\n"
    "s_mov_b32 m0, 117632\n"
    "s_nop 0\n"
    "buffer_load_dwordx4 v248, s[44:47], s67 offen offset:128 lds\n"
    "v_mfma_f32_32x32x16_bf16 v[80:95], v[212:215], v[228:231], v[80:95]\n"
    "ds_read_b64_tr_b16 v[202:203], v240 offset:25856\n"
    "s_mov_b32 m0, 118784\n"
    "s_add_u32 s67, s75, 1703936\n"
    "buffer_load_dwordx4 v248, s[44:47], s67 offen lds\n"
    "v_mfma_f32_32x32x16_bf16 v[96:111], v[216:219], a[20:23], v[96:111]\n"
    "ds_read_b64_tr_b16 v[204:205], v240 offset:26112\n"
    "s_mov_b32 m0, 119680\n"
    "s_nop 0\n"
    "buffer_load_dwordx4 v248, s[44:47], s67 offen offset:128 lds\n"
    "v_mfma_f32_32x32x16_bf16 v[112:127], v[216:219], a[148:151], v[112:127]\n"
    "ds_read_b64_tr_b16 v[206:207], v240 offset:26368\n"
    "s_mov_b32 m0, 120832\n"
    "s_add_u32 s67, s75, 1769472\n"
    "buffer_load_dwordx4 v248, s[44:47], s67 offen lds\n"
    "v_mfma_f32_32x32x16_bf16 v[128:143], v[216:219], v[228:231], v[128:143]\n"
    "ds_read_b128 v[224:227], v241 offset:4608\n"
    "s_mov_b32 m0, 121728\n"
    "s_nop 0\n"
    "buffer_load_dwordx4 v248, s[44:47], s67 offen offset:128 lds\n"
    "v_mfma_f32_32x32x16_bf16 v[144:159], v[220:223], a[20:23], v[144:159]\n"
    "v_mfma_f32_32x32x16_bf16 v[160:175], v[220:223], a[148:151], v[160:175]\n"
    "v_mfma_f32_32x32x16_bf16 v[176:191], v[220:223], v[228:231], v[176:191]\n"
    "s_waitcnt lgkmcnt(0)\n"
    "v_mfma_f32_32x32x16_bf16 v[0:15], v[192:195], a[24:27], v[0:15]\n"
    "ds_read_b64_tr_b16 v[208:209], v240 offset:28672\n"
    "v_mfma_f32_32x32x16_bf16 v[16:31], v[192:195], a[152:155], v[16:31]\n"
    "s_waitcnt vmcnt(54)\n"
    "s_barrier\n"
    "ds_read_b64_tr_b16 v[210:211], v240 offset:28928\n"
    "v_mfma_f32_32x32x16_bf16 v[32:47], v[192:195], v[224:227], v[32:47]\n"
    "ds_read_b64_tr_b16 v[212:213], v240 offset:29184\n"
    "s_add_u32 m0, s61, 49152\n"
    "s_add_u32 s66, s62, 16384\n"
    "buffer_load_dwordx4 v246, s[40:43], s66 offen nt lds\n"
    "buffer_load_dwordx4 v246, s[40:43], s66 offen offset:2048 nt lds\n"
    "v_mfma_f32_32x32x16_bf16 v[48:63], v[196:199], a[24:27], v[48:63]\n"
    "ds_read_b64_tr_b16 v[214:215], v240 offset:29440\n"
    "s_add_u32 m0, s61, 50048\n"
    "s_nop 0\n"
    "buffer_load_dwordx4 v246, s[40:43], s66 offen offset:128 nt lds\n"
    "buffer_load_dwordx4 v246, s[40:43], s66 offen offset:2176 nt lds\n"
    "v_mfma_f32_32x32x16_bf16 v[64:79], v[196:199], a[152:155], v[64:79]\n"
    "ds_read_b64_tr_b16 v[216:217], v240 offset:29696\n"
    "v_mfma_f32_32x32x16_bf16 v[80:95], v[196:199], v[224:227], v[80:95]\n"
    "ds_read_b64_tr_b16 v[218:219], v240 offset:29952\n"
    "v_mfma_f32_32x32x16_bf16 v[96:111], v[200:203], a[24:27], v[96:111]\n"
    "ds_read_b64_tr_b16 v[220:221], v240 offset:30208\n"
    "v_mfma_f32_32x32x16_bf16 v[112:127], v[200:203], a[152:155], v[112:127]\n"
    "ds_read_b64_tr_b16 v[222:223], v240 offset:30464\n"
    "v_mfma_f32_32x32x16_bf16 v[128:143], v[200:203], v[224:227], v[128:143]\n"
    "ds_read_b128 v[228:231], v242 offset:4608\n"
    "v_mfma_f32_32x32x16_bf16 v[144:159], v[204:207], a[24:27], v[144:159]\n"
    "v_mfma_f32_32x32x16_bf16 v[160:175], v[204:207], a[152:155], v[160:175]\n"
    "v_mfma_f32_32x32x16_bf16 v[176:191], v[204:207], v[224:227], v[176:191]\n"
    "s_waitcnt lgkmcnt(0)\n"
    "v_mfma_f32_32x32x16_bf16 v[0:15], v[208:211], a[28:31], v[0:15]\n"
    "s_waitcnt vmcnt(46)\n"
    "s_waitcnt vmcnt(20)\n"
    "ds_read_b64_tr_b16 v[192:193], v240 offset:0\n"
    "v_mfma_f32_32x32x16_bf16 v[16:31], v[208:211], a[156:159], v[16:31]\n"
    "ds_read_b64_tr_b16 v[194:195], v240 offset:256\n"
    "s_mov_b32 m0, 122880\n"
    "s_add_u32 s67, s75, 1835008\n"
    "buffer_load_dwordx4 v248, s[44:47], s67 offen lds\n"
    "v_mfma_f32_32x32x16_bf16 v[32:47], v[208:211], v[228:231], v[32:47]\n"
    "ds_read_b64_tr_b16 v[196:197], v240 offset:512\n"
    "s_mov_b32 m0, 123776\n"
    "s_nop 0\n"
    "buffer_load_dwordx4 v248, s[44:47], s67 offen offset:128 lds\n"
    "v_mfma_f32_32x32x16_bf16 v[48:63], v[212:215], a[28:31], v[48:63]\n"
    "ds_read_b64_tr_b16 v[198:199], v240 offset:768\n"
    "s_mov_b32 m0, 124928\n"
    "s_add_u32 s67, s75, 1900544\n"
    "buffer_load_dwordx4 v248, s[44:47], s67 offen lds\n"
    "v_mfma_f32_32x32x16_bf16 v[64:79], v[212:215], a[156:159], v[64:79]\n"
    "ds_read_b64_tr_b16 v[200:201], v240 offset:1024\n"
    "s_mov_b32 m0, 125824\n"
    "s_nop 0\n"
    "buffer_load_dwordx4 v248, s[44:47], s67 offen offset:128 lds\n"
    "v_mfma_f32_32x32x16_bf16 v[80:95], v[212:215], v[228:231], v[80:95]\n"
    "ds_read_b64_tr_b16 v[202:203], v240 offset:1280\n"
    "s_mov_b32 m0, 126976\n"
    "s_add_u32 s67, s75, 1966080\n"
    "buffer_load_dwordx4 v248, s[44:47], s67 offen lds\n"
    "v_mfma_f32_32x32x16_bf16 v[96:111], v[216:219], a[28:31], v[96:111]\n"
    "ds_read_b64_tr_b16 v[204:205], v240 offset:1536\n"
    "s_mov_b32 m0, 127872\n"
    "s_nop 0\n"
    "buffer_load_dwordx4 v248, s[44:47], s67 offen offset:128 lds\n"
    "v_mfma_f32_32x32x16_bf16 v[112:127], v[216:219], a[156:159], v[112:127]\n"
    "ds_read_b64_tr_b16 v[206:207], v240 offset:1792\n"
    "s_mov_b32 m0, 129024\n"
    "s_add_u32 s67, s75, 2031616\n"
    "buffer_load_dwordx4 v248, s[44:47], s67 offen lds\n"
    "v_mfma_f32_32x32x16_bf16 v[128:143], v[216:219], v[228:231], v[128:143]\n"
    "ds_read_b128 v[224:227], v241 offset:0\n"
    "s_mov_b32 m0, 129920\n"
    "s_nop 0\n"
    "buffer_load_dwordx4 v248, s[44:47], s67 offen offset:128 lds\n"
    "v_mfma_f32_32x32x16_bf16 v[144:159], v[220:223], a[28:31], v[144:159]\n"
    "s_mov_b32 m0, 159744\n"
    "s_mov_b32 s67, 65920\n"
    "buffer_load_dwordx4 v249, s[52:55], s67 offen lds\n"
    "v_mfma_f32_32x32x16_bf16 v[160:175], v[220:223], a[156:159], v[160:175]\n"
    "s_mov_b32 m0, 160768\n"
    "s_mov_b32 s67, 74112\n"
    "buffer_load_dwordx4 v250, s[52:55], s67 offen lds\n"
    "v_mfma_f32_32x32x16_bf16 v[176:191], v[220:223], v[228:231], v[176:191]\n"
    "s_barrier\n"
    "s_mov_b32 m0, 161792\n"
    "s_mov_b32 s67, 82304\n"
    "buffer_load_dwordx4 v249, s[52:55], s67 offen lds\n"
    "s_mov_b32 m0, 162816\n"
    "s_mov_b32 s67, 90496\n"
    "buffer_load_dwordx4 v250, s[52:55], s67 offen lds\n"
    "s_waitcnt lgkmcnt(0)\n"
    "v_mfma_f32_32x32x16_bf16 v[0:15], v[192:195], a[32:35], v[0:15]\n"
    "ds_read_b64_tr_b16 v[208:209], v240 offset:4096\n"
    "s_mov_b32 m0, 65536\n"
    "s_add_u32 s66, s64, 32768\n"
    "buffer_load_dwordx4 v247, s[48:51], s66 offen lds\n"
    "v_mfma_f32_32x32x16_bf16 v[16:31], v[192:195], a[160:163], v[16:31]\n"
    "ds_read_b64_tr_b16 v[210:211], v240 offset:4352\n"
    "s_mov_b32 m0, 66432\n"
    "s_nop 0\n"
    "buffer_load_dwordx4 v247, s[48:51], s66 offen offset:128 lds\n"
    "v_mfma_f32_32x32x16_bf16 v[32:47], v[192:195], v[224:227], v[32:47]\n"
    "ds_read_b64_tr_b16 v[212:213], v240 offset:4608\n"
    "s_mov_b32 m0, 67328\n"
    "s_nop 0\n"
    "buffer_load_dwordx4 v247, s[48:51], s66 offen offset:256 lds\n"
    "v_mfma_f32_32x32x16_bf16 v[48:63], v[196:199], a[32:35], v[48:63]\n"
    "ds_read_b64_tr_b16 v[214:215], v240 offset:4864\n"
    "s_mov_b32 m0, 68224\n"
    "s_nop 0\n"
    "buffer_load_dwordx4 v247, s[48:51], s66 offen offset:384 lds\n"
    "v_mfma_f32_32x32x16_bf16 v[64:79], v[196:199], a[160:163], v[64:79]\n"
    "ds_read_b64_tr_b16 v[216:217], v240 offset:5120\n"
    "s_mov_b32 m0, 69120\n"
    "s_nop 0\n"
    "buffer_load_dwordx4 v247, s[48:51], s66 offen offset:512 lds\n"
    "v_mfma_f32_32x32x16_bf16 v[80:95], v[196:199], v[224:227], v[80:95]\n"
    "ds_read_b64_tr_b16 v[218:219], v240 offset:5376\n"
    "s_mov_b32 m0, 70016\n"
    "s_nop 0\n"
    "buffer_load_dwordx4 v247, s[48:51], s66 offen offset:640 lds\n"
    "v_mfma_f32_32x32x16_bf16 v[96:111], v[200:203], a[32:35], v[96:111]\n"
    "ds_read_b64_tr_b16 v[220:221], v240 offset:5632\n"
    "s_mov_b32 m0, 70912\n"
    "s_nop 0\n"
    "buffer_load_dwordx4 v247, s[48:51], s66 offen offset:768 lds\n"
    "v_mfma_f32_32x32x16_bf16 v[112:127], v[200:203], a[160:163], v[112:127]\n"
    "ds_read_b64_tr_b16 v[222:223], v240 offset:5888\n"
    "s_mov_b32 m0, 71808\n"
    "s_nop 0\n"
    "buffer_load_dwordx4 v247, s[48:51], s66 offen offset:896 lds\n"
    "v_mfma_f32_32x32x16_bf16 v[128:143], v[200:203], v[224:227], v[128:143]\n"
    "ds_read_b128 v[228:231], v242 offset:0\n"
    "s_mov_b32 m0, 73728\n"
    "s_add_u32 s66, s64, 40960\n"
    "buffer_load_dwordx4 v247, s[48:51], s66 offen lds\n"
    "v_mfma_f32_32x32x16_bf16 v[144:159], v[204:207], a[32:35], v[144:159]\n"
    "s_mov_b32 m0, 74624\n"
    "s_nop 0\n"
    "buffer_load_dwordx4 v247, s[48:51], s66 offen offset:128 lds\n"
    "v_mfma_f32_32x32x16_bf16 v[160:175], v[204:207], a[160:163], v[160:175]\n"
    "s_mov_b32 m0, 75520\n"
    "s_nop 0\n"
    "buffer_load_dwordx4 v247, s[48:51], s66 offen offset:256 lds\n"
    "v_mfma_f32_32x32x16_bf16 v[176:191], v[204:207], v[224:227], v[176:191]\n"
    "s_mov_b32 m0, 76416\n"
    "s_nop 0\n"
    "buffer_load_dwordx4 v247, s[48:51], s66 offen offset:384 lds\n"
    "s_waitcnt lgkmcnt(0)\n"
    "v_mfma_f32_32x32x16_bf16 v[0:15], v[208:211], a[36:39], v[0:15]\n"
    "ds_read_b64_tr_b16 v[192:193], v240 offset:8192\n"
    "s_mov_b32 m0, 77312\n"
    "s_nop 0\n"
    "buffer_load_dwordx4 v247, s[48:51], s66 offen offset:512 lds\n"
    "v_mfma_f32_32x32x16_bf16 v[16:31], v[208:211], a[164:167], v[16:31]\n"
    "ds_read_b64_tr_b16 v[194:195], v240 offset:8448\n"
    "s_mov_b32 m0, 98304\n"
    "s_add_u32 s67, s75, 2097152\n"
    "buffer_load_dwordx4 v248, s[44:47], s67 offen lds\n"
    "s_mov_b32 m0, 78208\n"
    "s_nop 0\n"
    "buffer_load_dwordx4 v247, s[48:51], s66 offen offset:640 lds\n"
    "v_mfma_f32_32x32x16_bf16 v[32:47], v[208:211], v[228:231], v[32:47]\n"
    "ds_read_b64_tr_b16 v[196:197], v240 offset:8704\n"
    "s_mov_b32 m0, 99200\n"
    "s_nop 0\n"
    "buffer_load_dwordx4 v248, s[44:47], s67 offen offset:128 lds\n"
    "s_mov_b32 m0, 79104\n"
    "s_nop 0\n"
    "buffer_load_dwordx4 v247, s[48:51], s66 offen offset:768 lds\n"
    "v_mfma_f32_32x32x16_bf16 v[48:63], v[212:215], a[36:39], v[48:63]\n"
    "ds_read_b64_tr_b16 v[198:199], v240 offset:8960\n"
    "s_mov_b32 m0, 100352\n"
    "s_add_u32 s67, s75, 2162688\n"
    "buffer_load_dwordx4 v248, s[44:47], s67 offen lds\n"
    "v_mfma_f32_32x32x16_bf16 v[64:79], v[212:215], a[164:167], v[64:79]\n"
    "ds_read_b64_tr_b16 v[200:201], v240 offset:9216\n"
    "s_mov_b32 m0, 101248\n"
    "s_nop 0\n"
    "buffer_load_dwordx4 v248, s[44:47], s67 offen offset:128 lds\n"
    "s_mov_b32 m0, 80000\n"
    "s_nop 0\n"
    "buffer_load_dwordx4 v247, s[48:51], s66 offen offset:896 lds\n"
    "v_mfma_f32_32x32x16_bf16 v[80:95], v[212:215], v[228:231], v[80:95]\n"
    "s_waitcnt vmcnt(50)\n"
    "s_barrier\n"
    "ds_read_b64_tr_b16 v[202:203], v240 offset:9472\n"
    "s_mov_b32 m0, 102400\n"
    "s_add_u32 s67, s75, 2228224\n"
    "buffer_load_dwordx4 v248, s[44:47], s67 offen lds\n"
    "v_mfma_f32_32x32x16_bf16 v[96:111], v[216:219], a[36:39], v[96:111]\n"
    "ds_read_b64_tr_b16 v[204:205], v240 offset:9728\n"
    "s_mov_b32 m0, 103296\n"
    "s_nop 0\n"
    "buffer_load_dwordx4 v248, s[44:47], s67 offen offset:128 lds\n"
    "v_mfma_f32_32x32x16_bf16 v[112:127], v[216:219], a[164:167], v[112:127]\n"
    "ds_read_b64_tr_b16 v[206:207], v240 offset:9984\n"
    "s_mov_b32 m0, 104448\n"
    "s_add_u32 s67, s75, 2293760\n"
    "buffer_load_dwordx4 v248, s[44:47], s67 offen lds\n"
    "v_mfma_f32_32x32x16_bf16 v[128:143], v[216:219], v[228:231], v[128:143]\n"
    "ds_read_b128 v[224:227], v241 offset:512\n"
    "s_mov_b32 m0, 105344\n"
    "s_nop 0\n"
    "buffer_load_dwordx4 v248, s[44:47], s67 offen offset:128 lds\n"
    "v_mfma_f32_32x32x16_bf16 v[144:159], v[220:223], a[36:39], v[144:159]\n"
    "s_add_u32 m0, s61, 16384\n"
    "s_add_u32 s66, s62, 49152\n"
    "buffer_load_dwordx4 v244, s[36:39], s66 offen nt lds\n"
    "buffer_load_dwordx4 v245, s[36:39], s66 offen offset:2048 nt lds\n"
    "v_mfma_f32_32x32x16_bf16 v[160:175], v[220:223], a[164:167], v[160:175]\n"
    "s_add_u32 m0, s61, 17280\n"
    "s_nop 0\n"
    "buffer_load_dwordx4 v244, s[36:39], s66 offen offset:128 nt lds\n"
    "buffer_load_dwordx4 v245, s[36:39], s66 offen offset:2176 nt lds\n"
    "v_mfma_f32_32x32x16_bf16 v[176:191], v[220:223], v[228:231], v[176:191]\n"
    "s_waitcnt lgkmcnt(0)\n"
    "v_mfma_f32_32x32x16_bf16 v[0:15], v[192:195], a[40:43], v[0:15]\n"
    "ds_read_b64_tr_b16 v[208:209], v240 offset:12288\n"
    "v_mfma_f32_32x32x16_bf16 v[16:31], v[192:195], a[168:171], v[16:31]\n"
    "ds_read_b64_tr_b16 v[210:211], v240 offset:12544\n"
    "v_mfma_f32_32x32x16_bf16 v[32:47], v[192:195], v[224:227], v[32:47]\n"
    "ds_read_b64_tr_b16 v[212:213], v240 offset:12800\n"
    "v_mfma_f32_32x32x16_bf16 v[48:63], v[196:199], a[40:43], v[48:63]\n"
    "ds_read_b64_tr_b16 v[214:215], v240 offset:13056\n"
    "v_mfma_f32_32x32x16_bf16 v[64:79], v[196:199], a[168:171], v[64:79]\n"
    "ds_read_b64_tr_b16 v[216:217], v240 offset:13312\n"
    "v_mfma_f32_32x32x16_bf16 v[80:95], v[196:199], v[224:227], v[80:95]\n"
    "ds_read_b64_tr_b16 v[218:219], v240 offset:13568\n"
    "v_mfma_f32_32x32x16_bf16 v[96:111], v[200:203], a[40:43], v[96:111]\n"
    "ds_read_b64_tr_b16 v[220:221], v240 offset:13824\n"
    "v_mfma_f32_32x32x16_bf16 v[112:127], v[200:203], a[168:171], v[112:127]\n"
    "ds_read_b64_tr_b16 v[222:223], v240 offset:14080\n"
    "v_mfma_f32_32x32x16_bf16 v[128:143], v[200:203], v[224:227], v[128:143]\n"
    "ds_read_b128 v[228:231], v242 offset:512\n"
    "v_mfma_f32_32x32x16_bf16 v[144:159], v[204:207], a[40:43], v[144:159]\n"
    "v_mfma_f32_32x32x16_bf16 v[160:175], v[204:207], a[168:171], v[160:175]\n"
    "s_waitcnt vmcnt(52)\n"
    "s_barrier\n"
    "s_add_u32 m0, s61, 32768\n"
    "s_add_u32 s66, s62, 32768\n"
    "buffer_load_dwordx4 v246, s[40:43], s66 offen nt lds\n"
    "buffer_load_dwordx4 v246, s[40:43], s66 offen offset:2048 nt lds\n"
    "v_mfma_f32_32x32x16_bf16 v[176:191], v[204:207], v[224:227], v[176:191]\n"
    "s_add_u32 m0, s61, 33664\n"
    "s_nop 0\n"
    "buffer_load_dwordx4 v246, s[40:43], s66 offen offset:128 nt lds\n"
    "buffer_load_dwordx4 v246, s[40:43], s66 offen offset:2176 nt lds\n"
    "s_waitcnt lgkmcnt(0)\n"
    "v_mfma_f32_32x32x16_bf16 v[0:15], v[208:211], a[44:47], v[0:15]\n"
    "s_waitcnt vmcnt(48)\n"
    "s_waitcnt vmcnt(32)\n"
    "ds_read_b64_tr_b16 v[192:193], v240 offset:16384\n"
    "v_mfma_f32_32x32x16_bf16 v[16:31], v[208:211], a[172:175], v[16:31]\n"
    "ds_read_b64_tr_b16 v[194:195], v240 offset:16640\n"
    "s_mov_b32 m0, 106496\n"
    "s_add_u32 s67, s75, 2359296\n"
    "buffer_load_dwordx4 v248, s[44:47], s67 offen lds\n"
    "v_mfma_f32_32x32x16_bf16 v[32:47], v[208:211], v[228:231], v[32:47]\n"
    "ds_read_b64_tr_b16 v[196:197], v240 offset:16896\n"
    "s_mov_b32 m0, 107392\n"
    "s_nop 0\n"
    "buffer_load_dwordx4 v248, s[44:47], s67 offen offset:128 lds\n"
    "v_mfma_f32_32x32x16_bf16 v[48:63], v[212:215], a[44:47], v[48:63]\n"
    "ds_read_b64_tr_b16 v[198:199], v240 offset:17152\n"
    "s_mov_b32 m0, 108544\n"
    "s_add_u32 s67, s75, 2424832\n"
    "buffer_load_dwordx4 v248, s[44:47], s67 offen lds\n"
    "v_mfma_f32_32x32x16_bf16 v[64:79], v[212:215], a[172:175], v[64:79]\n"
    "ds_read_b64_tr_b16 v[200:201], v240 offset:17408\n"
    "s_mov_b32 m0, 109440\n"
    "s_nop 0\n"
    "buffer_load_dwordx4 v248, s[44:47], s67 offen offset:128 lds\n"
    "v_mfma_f32_32x32x16_bf16 v[80:95], v[212:215], v[228:231], v[80:95]\n"
    "ds_read_b64_tr_b16 v[202:203], v240 offset:17664\n"
    "s_mov_b32 m0, 110592\n"
    "s_add_u32 s67, s75, 2490368\n"
    "buffer_load_dwordx4 v248, s[44:47], s67 offen lds\n"
    "v_mfma_f32_32x32x16_bf16 v[96:111], v[216:219], a[44:47], v[96:111]\n"
    "ds_read_b64_tr_b16 v[204:205], v240 offset:17920\n"
    "s_mov_b32 m0, 111488\n"
    "s_nop 0\n"
    "buffer_load_dwordx4 v248, s[44:47], s67 offen offset:128 lds\n"
    "v_mfma_f32_32x32x16_bf16 v[112:127], v[216:219], a[172:175], v[112:127]\n"
    "ds_read_b64_tr_b16 v[206:207], v240 offset:18176\n"
    "s_mov_b32 m0, 112640\n"
    "s_add_u32 s67, s75, 2555904\n"
    "buffer_load_dwordx4 v248, s[44:47], s67 offen lds\n"
    "v_mfma_f32_32x32x16_bf16 v[128:143], v[216:219], v[228:231], v[128:143]\n"
    "ds_read_b128 v[224:227], v241 offset:4096\n"
    "s_mov_b32 m0, 113536\n"
    "s_nop 0\n"
    "buffer_load_dwordx4 v248, s[44:47], s67 offen offset:128 lds\n"
    "v_mfma_f32_32x32x16_bf16 v[144:159], v[220:223], a[44:47], v[144:159]\n"
    "s_mov_b32 m0, 155648\n"
    "s_mov_b32 s67, 66048\n"
    "buffer_load_dwordx4 v249, s[52:55], s67 offen lds\n"
    "v_mfma_f32_32x32x16_bf16 v[160:175], v[220:223], a[172:175], v[160:175]\n"
    "s_mov_b32 m0, 156672\n"
    "s_mov_b32 s67, 74240\n"
    "buffer_load_dwordx4 v250, s[52:55], s67 offen lds\n"
    "v_mfma_f32_32x32x16_bf16 v[176:191], v[220:223], v[228:231], v[176:191]\n"
    "s_mov_b32 m0, 157696\n"
    "s_mov_b32 s67, 82432\n"
    "buffer_load_dwordx4 v249, s[52:55], s67 offen lds\n"
    "s_mov_b32 m0, 158720\n"
    "s_mov_b32 s67, 90624\n"
    "buffer_load_dwordx4 v250, s[52:55], s67 offen lds\n"
    "s_waitcnt lgkmcnt(0)\n"
    "v_mfma_f32_32x32x16_bf16 v[0:15], v[192:195], a[48:51], v[0:15]\n"
    "ds_read_b64_tr_b16 v[208:209], v240 offset:20480\n"
    "v_mfma_f32_32x32x16_bf16 v[16:31], v[192:195], a[176:179], v[16:31]\n"
    "ds_read_b64_tr_b16 v[210:211], v240 offset:20736\n"
    "v_mfma_f32_32x32x16_bf16 v[32:47], v[192:195], v[224:227], v[32:47]\n"
    "ds_read_b64_tr_b16 v[212:213], v240 offset:20992\n"
    "v_mfma_f32_32x32x16_bf16 v[48:63], v[196:199], a[48:51], v[48:63]\n"
    "s_barrier\n"
    "ds_read_b64_tr_b16 v[214:215], v240 offset:21248\n"
    "s_mov_b32 m0, 81920\n"
    "s_add_u32 s66, s64, 49152\n"
    "buffer_load_dwordx4 v247, s[48:51], s66 offen lds\n"
    "v_mfma_f32_32x32x16_bf16 v[64:79], v[196:199], a[176:179], v[64:79]\n"
    "ds_read_b64_tr_b16 v[216:217], v240 offset:21504\n"
    "s_mov_b32 m0, 82816\n"
    "s_nop 0\n"
    "buffer_load_dwordx4 v247, s[48:51], s66 offen offset:128 lds\n"
    "v_mfma_f32_32x32x16_bf16 v[80:95], v[196:199], v[224:227], v[80:95]\n"
    "ds_read_b64_tr_b16 v[218:219], v240 offset:21760\n"
    "s_mov_b32 m0, 83712\n"
    "s_nop 0\n"
    "buffer_load_dwordx4 v247, s[48:51], s66 offen offset:256 lds\n"
    "v_mfma_f32_32x32x16_bf16 v[96:111], v[200:203], a[48:51], v[96:111]\n"
    "ds_read_b64_tr_b16 v[220:221], v240 offset:22016\n"
    "s_mov_b32 m0, 84608\n"
    "s_nop 0\n"
    "buffer_load_dwordx4 v247, s[48:51], s66 offen offset:384 lds\n"
    "v_mfma_f32_32x32x16_bf16 v[112:127], v[200:203], a[176:179], v[112:127]\n"
    "ds_read_b64_tr_b16 v[222:223], v240 offset:22272\n"
    "s_mov_b32 m0, 85504\n"
    "s_nop 0\n"
    "buffer_load_dwordx4 v247, s[48:51], s66 offen offset:512 lds\n"
    "v_mfma_f32_32x32x16_bf16 v[128:143], v[200:203], v[224:227], v[128:143]\n"
    "ds_read_b128 v[228:231], v242 offset:4096\n"
    "s_mov_b32 m0, 86400\n"
    "s_nop 0\n"
    "buffer_load_dwordx4 v247, s[48:51], s66 offen offset:640 lds\n"
    "v_mfma_f32_32x32x16_bf16 v[144:159], v[204:207], a[48:51], v[144:159]\n"
    "s_mov_b32 m0, 87296\n"
    "s_nop 0\n"
    "buffer_load_dwordx4 v247, s[48:51], s66 offen offset:768 lds\n"
    "v_mfma_f32_32x32x16_bf16 v[160:175], v[204:207], a[176:179], v[160:175]\n"
    "s_mov_b32 m0, 88192\n"
    "s_nop 0\n"
    "buffer_load_dwordx4 v247, s[48:51], s66 offen offset:896 lds\n"
    "v_mfma_f32_32x32x16_bf16 v[176:191], v[204:207], v[224:227], v[176:191]\n"
    "s_mov_b32 m0, 90112\n"
    "s_add_u32 s66, s64, 57344\n"
    "buffer_load_dwordx4 v247, s[48:51], s66 offen lds\n"
    "s_waitcnt lgkmcnt(0)\n"
    "v_mfma_f32_32x32x16_bf16 v[0:15], v[208:211], a[52:55], v[0:15]\n"
    "ds_read_b64_tr_b16 v[192:193], v240 offset:24576\n"
    "s_mov_b32 m0, 91008\n"
    "s_nop 0\n"
    "buffer_load_dwordx4 v247, s[48:51], s66 offen offset:128 lds\n"
    "v_mfma_f32_32x32x16_bf16 v[16:31], v[208:211], a[180:183], v[16:31]\n"
    "ds_read_b64_tr_b16 v[194:195], v240 offset:24832\n"
    "s_mov_b32 m0, 114688\n"
    "s_add_u32 s67, s75, 2621440\n"
    "buffer_load_dwordx4 v248, s[44:47], s67 offen lds\n"
    "v_mfma_f32_32x32x16_bf16 v[32:47], v[208:211], v[228:231], v[32:47]\n"
    "ds_read_b64_tr_b16 v[196:197], v240 offset:25088\n"
    "s_mov_b32 m0, 115584\n"
    "s_nop 0\n"
    "buffer_load_dwordx4 v248, s[44:47], s67 offen offset:128 lds\n"
    "s_mov_b32 m0, 91904\n"
    "s_nop 0\n"
    "buffer_load_dwordx4 v247, s[48:51], s66 offen offset:256 lds\n"
    "v_mfma_f32_32x32x16_bf16 v[48:63], v[212:215], a[52:55], v[48:63]\n"
    "ds_read_b64_tr_b16 v[198:199], v240 offset:25344\n"
    "s_mov_b32 m0, 116736\n"
    "s_add_u32 s67, s75, 2686976\n"
    "buffer_load_dwordx4 v248, s[44:47], s67 offen lds\n"
    "s_mov_b32 m0, 92800\n"
    "s_nop 0\n"
    "buffer_load_dwordx4 v247, s[48:51], s66 offen offset:384 lds\n"
    "v_mfma_f32_32x32x16_bf16 v[64:79], v[212:215], a[180:183], v[64:79]\n"
    "ds_read_b64_tr_b16 v[200:201], v240 offset:25600\n"
    "s_mov_b32 m0, 117632\n"
    "s_nop 0\n"
    "buffer_load_dwordx4 v248, s[44:47], s67 offen offset:128 lds\n"
    "s_mov_b32 m0, 93696\n"
    "s_nop 0\n"
    "buffer_load_dwordx4 v247, s[48:51], s66 offen offset:512 lds\n"
    "v_mfma_f32_32x32x16_bf16 v[80:95], v[212:215], v[228:231], v[80:95]\n"
    "ds_read_b64_tr_b16 v[202:203], v240 offset:25856\n"
    "s_mov_b32 m0, 118784\n"
    "s_add_u32 s67, s75, 2752512\n"
    "buffer_load_dwordx4 v248, s[44:47], s67 offen lds\n"
    "s_mov_b32 m0, 94592\n"
    "s_nop 0\n"
    "buffer_load_dwordx4 v247, s[48:51], s66 offen offset:640 lds\n"
    "v_mfma_f32_32x32x16_bf16 v[96:111], v[216:219], a[52:55], v[96:111]\n"
    "ds_read_b64_tr_b16 v[204:205], v240 offset:26112\n"
    "s_mov_b32 m0, 119680\n"
    "s_nop 0\n"
    "s_waitcnt vmcnt(62)\n"
    "buffer_load_dwordx4 v248, s[44:47], s67 offen offset:128 lds\n"
    "s_mov_b32 m0, 95488\n"
    "s_nop 0\n"
    "s_waitcnt vmcnt(62)\n"
    "buffer_load_dwordx4 v247, s[48:51], s66 offen offset:768 lds\n"
    "v_mfma_f32_32x32x16_bf16 v[112:127], v[216:219], a[180:183], v[112:127]\n"
    "ds_read_b64_tr_b16 v[206:207], v240 offset:26368\n"
    "s_mov_b32 m0, 120832\n"
    "s_add_u32 s67, s75, 2818048\n"
    "s_waitcnt vmcnt(62)\n"
    "buffer_load_dwordx4 v248, s[44:47], s67 offen lds\n"
    "s_mov_b32 m0, 96384\n"
    "s_nop 0\n"
    "s_waitcnt vmcnt(62)\n"
    "buffer_load_dwordx4 v247, s[48:51], s66 offen offset:896 lds\n"
    "v_mfma_f32_32x32x16_bf16 v[128:143], v[216:219], v[228:231], v[128:143]\n"
    "s_waitcnt vmcnt(47)\n"
    "s_barrier\n"
    "ds_read_b128 v[224:227], v241 offset:4608\n"
    "s_mov_b32 m0, 121728\n"
    "s_nop 0\n"
    "buffer_load_dwordx4 v248, s[44:47], s67 offen offset:128 lds\n"
    "v_mfma_f32_32x32x16_bf16 v[144:159], v[220:223], a[52:55], v[144:159]\n"
    "s_add_u32 m0, s61, 0\n"
    "s_add_u32 s66, s62, 65536\n"
    "buffer_load_dwordx4 v244, s[36:39], s66 offen nt lds\n"
    "buffer_load_dwordx4 v245, s[36:39], s66 offen offset:2048 nt lds\n"
    "v_mfma_f32_32x32x16_bf16 v[160:175], v[220:223], a[180:183], v[160:175]\n"
    "s_add_u32 m0, s61, 896\n"
    "s_nop 0\n"
    "buffer_load_dwordx4 v244, s[36:39], s66 offen offset:128 nt lds\n"
    "buffer_load_dwordx4 v245, s[36:39], s66 offen offset:2176 nt lds\n"
    "v_mfma_f32_32x32x16_bf16 v[176:191], v[220:223], v[228:231], v[176:191]\n"
    "s_waitcnt lgkmcnt(0)\n"
    "v_mfma_f32_32x32x16_bf16 v[0:15], v[192:195], a[56:59], v[0:15]\n"
    "ds_read_b64_tr_b16 v[208:209], v240 offset:28672\n"
    "v_mfma_f32_32x32x16_bf16 v[16:31], v[192:195], a[184:187], v[16:31]\n"
    "ds_read_b64_tr_b16 v[210:211], v240 offset:28928\n"
    "v_mfma_f32_32x32x16_bf16 v[32:47], v[192:195], v[224:227], v[32:47]\n"
    "ds_read_b64_tr_b16 v[212:213], v240 offset:29184\n"
    "v_mfma_f32_32x32x16_bf16 v[48:63], v[196:199], a[56:59], v[48:63]\n"
    "ds_read_b64_tr_b16 v[214:215], v240 offset:29440\n"
    "v_mfma_f32_32x32x16_bf16 v[64:79], v[196:199], a[184:187], v[64:79]\n"
    "ds_read_b64_tr_b16 v[216:217], v240 offset:29696\n"
    "v_mfma_f32_32x32x16_bf16 v[80:95], v[196:199], v[224:227], v[80:95]\n"
    "ds_read_b64_tr_b16 v[218:219], v240 offset:29952\n"
    "v_mfma_f32_32x32x16_bf16 v[96:111], v[200:203], a[56:59], v[96:111]\n"
    "ds_read_b64_tr_b16 v[220:221], v240 offset:30208\n"
    "v_mfma_f32_32x32x16_bf16 v[112:127], v[200:203], a[184:187], v[112:127]\n"
    "ds_read_b64_tr_b16 v[222:223], v240 offset:30464\n"
    "v_mfma_f32_32x32x16_bf16 v[128:143], v[200:203], v[224:227], v[128:143]\n"
    "ds_read_b128 v[228:231], v242 offset:4608\n"
    "v_mfma_f32_32x32x16_bf16 v[144:159], v[204:207], a[56:59], v[144:159]\n"
    "v_mfma_f32_32x32x16_bf16 v[160:175], v[204:207], a[184:187], v[160:175]\n"
    "v_mfma_f32_32x32x16_bf16 v[176:191], v[204:207], v[224:227], v[176:191]\n"
    "s_waitcnt lgkmcnt(0)\n"
    "v_mfma_f32_32x32x16_bf16 v[0:15], v[208:211], a[60:63], v[0:15]\n"
    "s_waitcnt vmcnt(44)\n"
    "s_barrier\n"
    "s_waitcnt vmcnt(28)\n"
    "ds_read_b64_tr_b16 v[192:193], v240 offset:0\n"
    "v_mfma_f32_32x32x16_bf16 v[16:31], v[208:211], a[188:191], v[16:31]\n"
    "ds_read_b64_tr_b16 v[194:195], v240 offset:256\n"
    "s_mov_b32 m0, 122880\n"
    "s_add_u32 s67, s75, 2883584\n"
    "buffer_load_dwordx4 v248, s[44:47], s67 offen lds\n"
    "v_mfma_f32_32x32x16_bf16 v[32:47], v[208:211], v[228:231], v[32:47]\n"
    "ds_read_b64_tr_b16 v[196:197], v240 offset:512\n"
    "s_mov_b32 m0, 123776\n"
    "s_nop 0\n"
    "buffer_load_dwordx4 v248, s[44:47], s67 offen offset:128 lds\n"
    "v_mfma_f32_32x32x16_bf16 v[48:63], v[212:215], a[60:63], v[48:63]\n"
    "ds_read_b64_tr_b16 v[198:199], v240 offset:768\n"
    "s_mov_b32 m0, 124928\n"
    "s_add_u32 s67, s75, 2949120\n"
    "buffer_load_dwordx4 v248, s[44:47], s67 offen lds\n"
    "v_mfma_f32_32x32x16_bf16 v[64:79], v[212:215], a[188:191], v[64:79]\n"
    "ds_read_b64_tr_b16 v[200:201], v240 offset:1024\n"
    "s_mov_b32 m0, 125824\n"
    "s_nop 0\n"
    "buffer_load_dwordx4 v248, s[44:47], s67 offen offset:128 lds\n"
    "v_mfma_f32_32x32x16_bf16 v[80:95], v[212:215], v[228:231], v[80:95]\n"
    "ds_read_b64_tr_b16 v[202:203], v240 offset:1280\n"
    "s_mov_b32 m0, 126976\n"
    "s_add_u32 s67, s75, 3014656\n"
    "buffer_load_dwordx4 v248, s[44:47], s67 offen lds\n"
    "v_mfma_f32_32x32x16_bf16 v[96:111], v[216:219], a[60:63], v[96:111]\n"
    "ds_read_b64_tr_b16 v[204:205], v240 offset:1536\n"
    "s_mov_b32 m0, 127872\n"
    "s_nop 0\n"
    "buffer_load_dwordx4 v248, s[44:47], s67 offen offset:128 lds\n"
    "v_mfma_f32_32x32x16_bf16 v[112:127], v[216:219], a[188:191], v[112:127]\n"
    "ds_read_b64_tr_b16 v[206:207], v240 offset:1792\n"
    "s_mov_b32 m0, 129024\n"
    "s_add_u32 s67, s75, 3080192\n"
    "buffer_load_dwordx4 v248, s[44:47], s67 offen lds\n"
    "v_mfma_f32_32x32x16_bf16 v[128:143], v[216:219], v[228:231], v[128:143]\n"
    "ds_read_b128 v[224:227], v241 offset:0\n"
    "s_mov_b32 m0, 129920\n"
    "s_nop 0\n"
    "buffer_load_dwordx4 v248, s[44:47], s67 offen offset:128 lds\n"
    "s_add_u32 m0, s61, 49152\n"
    "s_add_u32 s66, s62, 49152\n"
    "buffer_load_dwordx4 v246, s[40:43], s66 offen nt lds\n"
    "buffer_load_dwordx4 v246, s[40:43], s66 offen offset:2048 nt lds\n"
    "v_mfma_f32_32x32x16_bf16 v[144:159], v[220:223], a[60:63], v[144:159]\n"
    "s_mov_b32 m0, 159744\n"
    "s_mov_b32 s67, 66176\n"
    "buffer_load_dwordx4 v249, s[52:55], s67 offen lds\n"
    "v_mfma_f32_32x32x16_bf16 v[160:175], v[220:223], a[188:191], v[160:175]\n"
    "s_mov_b32 m0, 160768\n"
    "s_mov_b32 s67, 74368\n"
    "buffer_load_dwordx4 v250, s[52:55], s67 offen lds\n"
    "v_mfma_f32_32x32x16_bf16 v[176:191], v[220:223], v[228:231], v[176:191]\n"
    "s_mov_b32 m0, 161792\n"
    "s_mov_b32 s67, 82560\n"
    "buffer_load_dwordx4 v249, s[52:55], s67 offen lds\n"
    "s_mov_b32 m0, 162816\n"
    "s_mov_b32 s67, 90752\n"
    "buffer_load_dwordx4 v250, s[52:55], s67 offen lds\n"
    "s_waitcnt lgkmcnt(0)\n"
    "v_mfma_f32_32x32x16_bf16 v[0:15], v[192:195], a[64:67], v[0:15]\n"
    "ds_read_b64_tr_b16 v[208:209], v240 offset:4096\n"
    "s_add_u32 m0, s61, 50048\n"
    "s_nop 0\n"
    "buffer_load_dwordx4 v246, s[40:43], s66 offen offset:128 nt lds\n"
    "buffer_load_dwordx4 v246, s[40:43], s66 offen offset:2176 nt lds\n"
    "v_mfma_f32_32x32x16_bf16 v[16:31], v[192:195], a[192:195], v[16:31]\n"
    "ds_read_b64_tr_b16 v[210:211], v240 offset:4352\n"
    "v_mfma_f32_32x32x16_bf16 v[32:47], v[192:195], v[224:227], v[32:47]\n"
    "ds_read_b64_tr_b16 v[212:213], v240 offset:4608\n"
    "v_mfma_f32_32x32x16_bf16 v[48:63], v[196:199], a[64:67], v[48:63]\n"
    "ds_read_b64_tr_b16 v[214:215], v240 offset:4864\n"
    "v_mfma_f32_32x32x16_bf16 v[64:79], v[196:199], a[192:195], v[64:79]\n"
    "ds_read_b64_tr_b16 v[216:217], v240 offset:5120\n"
    "v_mfma_f32_32x32x16_bf16 v[80:95], v[196:199], v[224:227], v[80:95]\n"
    "s_barrier\n"
    "ds_read_b64_tr_b16 v[218:219], v240 offset:5376\n"
    "s_mov_b32 m0, 65536\n"
    "s_add_u32 s66, s64, 65536\n"
    "buffer_load_dwordx4 v247, s[48:51], s66 offen lds\n"
    "v_mfma_f32_32x32x16_bf16 v[96:111], v[200:203], a[64:67], v[96:111]\n"
    "ds_read_b64_tr_b16 v[220:221], v240 offset:5632\n"
    "s_mov_b32 m0, 66432\n"
    "s_nop 0\n"
    "buffer_load_dwordx4 v247, s[48:51], s66 offen offset:128 lds\n"
    "v_mfma_f32_32x32x16_bf16 v[112:127], v[200:203], a[192:195], v[112:127]\n"
    "ds_read_b64_tr_b16 v[222:223], v240 offset:5888\n"
    "s_mov_b32 m0, 67328\n"
    "s_nop 0\n"
    "buffer_load_dwordx4 v247, s[48:51], s66 offen offset:256 lds\n"
    "v_mfma_f32_32x32x16_bf16 v[128:143], v[200:203], v[224:227], v[128:143]\n"
    "ds_read_b128 v[228:231], v242 offset:0\n"
    "s_mov_b32 m0, 68224\n"
    "s_nop 0\n"
    "buffer_load_dwordx4 v247, s[48:51], s66 offen offset:384 lds\n"
    "v_mfma_f32_32x32x16_bf16 v[144:159], v[204:207], a[64:67], v[144:159]\n"
    "s_mov_b32 m0, 69120\n"
    "s_nop 0\n"
    "buffer_load_dwordx4 v247, s[48:51], s66 offen offset:512 lds\n"
    "v_mfma_f32_32x32x16_bf16 v[160:175], v[204:207], a[192:195], v[160:175]\n"
    "s_mov_b32 m0, 70016\n"
    "s_nop 0\n"
    "buffer_load_dwordx4 v247, s[48:51], s66 offen offset:640 lds\n"
    "v_mfma_f32_32x32x16_bf16 v[176:191], v[204:207], v[224:227], v[176:191]\n"
    "s_mov_b32 m0, 70912\n"
    "s_nop 0\n"
    "buffer_load_dwordx4 v247, s[48:51], s66 offen offset:768 lds\n"
    "s_waitcnt lgkmcnt(0)\n"
    "v_mfma_f32_32x32x16_bf16 v[0:15], v[208:211], a[68:71], v[0:15]\n"
    "ds_read_b64_tr_b16 v[192:193], v240 offset:8192\n"
    "s_mov_b32 m0, 71808\n"
    "s_nop 0\n"
    "buffer_load_dwordx4 v247, s[48:51], s66 offen offset:896 lds\n"
    "v_mfma_f32_32x32x16_bf16 v[16:31], v[208:211], a[196:199], v[16:31]\n"
    "ds_read_b64_tr_b16 v[194:195], v240 offset:8448\n"
    "s_mov_b32 m0, 98304\n"
    "s_add_u32 s67, s75, 3145728\n"
    "buffer_load_dwordx4 v248, s[44:47], s67 offen lds\n"
    "v_mfma_f32_32x32x16_bf16 v[32:47], v[208:211], v[228:231], v[32:47]\n"
    "ds_read_b64_tr_b16 v[196:197], v240 offset:8704\n"
    "s_mov_b32 m0, 99200\n"
    "s_nop 0\n"
    "buffer_load_dwordx4 v248, s[44:47], s67 offen offset:128 lds\n"
    "s_mov_b32 m0, 73728\n"
    "s_add_u32 s66, s64, 73728\n"
    "buffer_load_dwordx4 v247, s[48:51], s66 offen lds\n"
    "v_mfma_f32_32x32x16_bf16 v[48:63], v[212:215], a[68:71], v[48:63]\n"
    "ds_read_b64_tr_b16 v[198:199], v240 offset:8960\n"
    "s_mov_b32 m0, 100352\n"
    "s_add_u32 s67, s75, 3211264\n"
    "buffer_load_dwordx4 v248, s[44:47], s67 offen lds\n"
    "s_mov_b32 m0, 74624\n"
    "s_nop 0\n"
    "buffer_load_dwordx4 v247, s[48:51], s66 offen offset:128 lds\n"
    "v_mfma_f32_32x32x16_bf16 v[64:79], v[212:215], a[196:199], v[64:79]\n"
    "ds_read_b64_tr_b16 v[200:201], v240 offset:9216\n"
    "s_mov_b32 m0, 101248\n"
    "s_nop 0\n"
    "buffer_load_dwordx4 v248, s[44:47], s67 offen offset:128 lds\n"
    "s_mov_b32 m0, 75520\n"
    "s_nop 0\n"
    "buffer_load_dwordx4 v247, s[48:51], s66 offen offset:256 lds\n"
    "v_mfma_f32_32x32x16_bf16 v[80:95], v[212:215], v[228:231], v[80:95]\n"
    "ds_read_b64_tr_b16 v[202:203], v240 offset:9472\n"
    "s_mov_b32 m0, 102400\n"
    "s_add_u32 s67, s75, 3276800\n"
    "buffer_load_dwordx4 v248, s[44:47], s67 offen lds\n"
    "s_mov_b32 m0, 76416\n"
    "s_nop 0\n"
    "buffer_load_dwordx4 v247, s[48:51], s66 offen offset:384 lds\n"
    "v_mfma_f32_32x32x16_bf16 v[96:111], v[216:219], a[68:71], v[96:111]\n"
    "ds_read_b64_tr_b16 v[204:205], v240 offset:9728\n"
    "s_mov_b32 m0, 103296\n"
    "s_nop 0\n"
    "buffer_load_dwordx4 v248, s[44:47], s67 offen offset:128 lds\n"
    "s_mov_b32 m0, 77312\n"
    "s_nop 0\n"
    "buffer_load_dwordx4 v247, s[48:51], s66 offen offset:512 lds\n"
    "v_mfma_f32_32x32x16_bf16 v[112:127], v[216:219], a[196:199], v[112:127]\n"
    "ds_read_b64_tr_b16 v[206:207], v240 offset:9984\n"
    "s_mov_b32 m0, 104448\n"
    "s_add_u32 s67, s75, 3342336\n"
    "s_waitcnt vmcnt(62)\n"
    "buffer_load_dwordx4 v248, s[44:47], s67 offen lds\n"
    "s_mov_b32 m0, 78208\n"
    "s_nop 0\n"
    "s_waitcnt vmcnt(62)\n"
    "buffer_load_dwordx4 v247, s[48:51], s66 offen offset:640 lds\n"
    "v_mfma_f32_32x32x16_bf16 v[128:143], v[216:219], v[228:231], v[128:143]\n"
    "ds_read_b128 v[224:227], v241 offset:512\n"
    "s_mov_b32 m0, 105344\n"
    "s_nop 0\n"
    "s_waitcnt vmcnt(62)\n"
    "buffer_load_dwordx4 v248, s[44:47], s67 offen offset:128 lds\n"
    "s_mov_b32 m0, 79104\n"
    "s_nop 0\n"
    "s_waitcnt vmcnt(62)\n"
    "buffer_load_dwordx4 v247, s[48:51], s66 offen offset:768 lds\n"
    "v_mfma_f32_32x32x16_bf16 v[144:159], v[220:223], a[68:71], v[144:159]\n"
    "s_mov_b32 m0, 80000\n"
    "s_nop 0\n"
    "s_waitcnt vmcnt(62)\n"
    "buffer_load_dwordx4 v247, s[48:51], s66 offen offset:896 lds\n"
    "v_mfma_f32_32x32x16_bf16 v[160:175], v[220:223], a[196:199], v[160:175]\n"
    "s_waitcnt vmcnt(45)\n"
    "s_barrier\n"
    "s_add_u32 m0, s61, 16384\n"
    "s_add_u32 s66, s62, 81920\n"
    "buffer_load_dwordx4 v244, s[36:39], s66 offen nt lds\n"
    "buffer_load_dwordx4 v245, s[36:39], s66 offen offset:2048 nt lds\n"
    "v_mfma_f32_32x32x16_bf16 v[176:191], v[220:223], v[228:231], v[176:191]\n"
    "s_add_u32 m0, s61, 17280\n"
    "s_nop 0\n"
    "buffer_load_dwordx4 v244, s[36:39], s66 offen offset:128 nt lds\n"
    "buffer_load_dwordx4 v245, s[36:39], s66 offen offset:2176 nt lds\n"
    "s_waitcnt lgkmcnt(0)\n"
    "v_mfma_f32_32x32x16_bf16 v[0:15], v[192:195], a[72:75], v[0:15]\n"
    "ds_read_b64_tr_b16 v[208:209], v240 offset:12288\n"
    "v_mfma_f32_32x32x16_bf16 v[16:31], v[192:195], a[200:203], v[16:31]\n"
    "ds_read_b64_tr_b16 v[210:211], v240 offset:12544\n"
    "v_mfma_f32_32x32x16_bf16 v[32:47], v[192:195], v[224:227], v[32:47]\n"
    "ds_read_b64_tr_b16 v[212:213], v240 offset:12800\n"
    "v_mfma_f32_32x32x16_bf16 v[48:63], v[196:199], a[72:75], v[48:63]\n"
    "ds_read_b64_tr_b16 v[214:215], v240 offset:13056\n"
    "v_mfma_f32_32x32x16_bf16 v[64:79], v[196:199], a[200:203], v[64:79]\n"
    "ds_read_b64_tr_b16 v[216:217], v240 offset:13312\n"
    "v_mfma_f32_32x32x16_bf16 v[80:95], v[196:199], v[224:227], v[80:95]\n"
    "ds_read_b64_tr_b16 v[218:219], v240 offset:13568\n"
    "v_mfma_f32_32x32x16_bf16 v[96:111], v[200:203], a[72:75], v[96:111]\n"
    "ds_read_b64_tr_b16 v[220:221], v240 offset:13824\n"
    "v_mfma_f32_32x32x16_bf16 v[112:127], v[200:203], a[200:203], v[112:127]\n"
    "ds_read_b64_tr_b16 v[222:223], v240 offset:14080\n"
    "v_mfma_f32_32x32x16_bf16 v[128:143], v[200:203], v[224:227], v[128:143]\n"
    "ds_read_b128 v[228:231], v242 offset:512\n"
    "v_mfma_f32_32x32x16_bf16 v[144:159], v[204:207], a[72:75], v[144:159]\n"
    "v_mfma_f32_32x32x16_bf16 v[160:175], v[204:207], a[200:203], v[160:175]\n"
    "v_mfma_f32_32x32x16_bf16 v[176:191], v[204:207], v[224:227], v[176:191]\n"
    "s_waitcnt lgkmcnt(0)\n"
    "v_mfma_f32_32x32x16_bf16 v[0:15], v[208:211], a[76:79], v[0:15]\n"
    "s_waitcnt vmcnt(48)\n"
    "s_waitcnt vmcnt(30)\n"
    "ds_read_b64_tr_b16 v[192:193], v240 offset:16384\n"
    "v_mfma_f32_32x32x16_bf16 v[16:31], v[208:211], a[204:207], v[16:31]\n"
    "ds_read_b64_tr_b16 v[194:195], v240 offset:16640\n"
    "s_mov_b32 m0, 106496\n"
    "s_add_u32 s67, s75, 3407872\n"
    "buffer_load_dwordx4 v248, s[44:47], s67 offen lds\n"
    "v_mfma_f32_32x32x16_bf16 v[32:47], v[208:211], v[228:231], v[32:47]\n"
    "ds_read_b64_tr_b16 v[196:197], v240 offset:16896\n"
    "s_mov_b32 m0, 107392\n"
    "s_nop 0\n"
    "buffer_load_dwordx4 v248, s[44:47], s67 offen offset:128 lds\n"
    "v_mfma_f32_32x32x16_bf16 v[48:63], v[212:215], a[76:79], v[48:63]\n"
    "s_barrier\n"
    "ds_read_b64_tr_b16 v[198:199], v240 offset:17152\n"
    "s_mov_b32 m0, 108544\n"
    "s_add_u32 s67, s75, 3473408\n"
    "buffer_load_dwordx4 v248, s[44:47], s67 offen lds\n"
    "v_mfma_f32_32x32x16_bf16 v[64:79], v[212:215], a[204:207], v[64:79]\n"
    "ds_read_b64_tr_b16 v[200:201], v240 offset:17408\n"
    "s_mov_b32 m0, 109440\n"
    "s_nop 0\n"
    "buffer_load_dwordx4 v248, s[44:47], s67 offen offset:128 lds\n"
    "v_mfma_f32_32x32x16_bf16 v[80:95], v[212:215], v[228:231], v[80:95]\n"
    "ds_read_b64_tr_b16 v[202:203], v240 offset:17664\n"
    "s_mov_b32 m0, 110592\n"
    "s_add_u32 s67, s75, 3538944\n"
    "buffer_load_dwordx4 v248, s[44:47], s67 offen lds\n"
    "v_mfma_f32_32x32x16_bf16 v[96:111], v[216:219], a[76:79], v[96:111]\n"
    "ds_read_b64_tr_b16 v[204:205], v240 offset:17920\n"
    "s_mov_b32 m0, 111488\n"
    "s_nop 0\n"
    "buffer_load_dwordx4 v248, s[44:47], s67 offen offset:128 lds\n"
    "v_mfma_f32_32x32x16_bf16 v[112:127], v[216:219], a[204:207], v[112:127]\n"
    "ds_read_b64_tr_b16 v[206:207], v240 offset:18176\n"
    "s_mov_b32 m0, 112640\n"
    "s_add_u32 s67, s75, 3604480\n"
    "buffer_load_dwordx4 v248, s[44:47], s67 offen lds\n"
    "v_mfma_f32_32x32x16_bf16 v[128:143], v[216:219], v[228:231], v[128:143]\n"
    "ds_read_b128 v[224:227], v241 offset:4096\n"
    "s_mov_b32 m0, 113536\n"
    "s_nop 0\n"
    "buffer_load_dwordx4 v248, s[44:47], s67 offen offset:128 lds\n"
    "v_mfma_f32_32x32x16_bf16 v[144:159], v[220:223], a[76:79], v[144:159]\n"
    "s_mov_b32 m0, 155648\n"
    "s_mov_b32 s67, 66304\n"
    "buffer_load_dwordx4 v249, s[52:55], s67 offen lds\n"
    "v_mfma_f32_32x32x16_bf16 v[160:175], v[220:223], a[204:207], v[160:175]\n"
    "s_mov_b32 m0, 156672\n"
    "s_mov_b32 s67, 74496\n"
    "buffer_load_dwordx4 v250, s[52:55], s67 offen lds\n"
    "v_mfma_f32_32x32x16_bf16 v[176:191], v[220:223], v[228:231], v[176:191]\n"
    "s_mov_b32 m0, 157696\n"
    "s_mov_b32 s67, 82688\n"
    "buffer_load_dwordx4 v249, s[52:55], s67 offen lds\n"
    "s_mov_b32 m0, 158720\n"
    "s_mov_b32 s67, 90880\n"
    "buffer_load_dwordx4 v250, s[52:55], s67 offen lds\n"
    "s_add_u32 m0, s61, 32768\n"
    "s_add_u32 s66, s62, 65536\n"
    "buffer_load_dwordx4 v246, s[40:43], s66 offen nt lds\n"
    "buffer_load_dwordx4 v246, s[40:43], s66 offen offset:2048 nt lds\n"
    "s_waitcnt lgkmcnt(0)\n"
    "v_mfma_f32_32x32x16_bf16 v[0:15], v[192:195], a[80:83], v[0:15]\n"
    "ds_read_b64_tr_b16 v[208:209], v240 offset:20480\n"
    "s_add_u32 m0, s61, 33664\n"
    "s_nop 0\n"
    "buffer_load_dwordx4 v246, s[40:43], s66 offen offset:128 nt lds\n"
    "buffer_load_dwordx4 v246, s[40:43], s66 offen offset:2176 nt lds\n"
    "v_mfma_f32_32x32x16_bf16 v[16:31], v[192:195], a[208:211], v[16:31]\n"
    "ds_read_b64_tr_b16 v[210:211], v240 offset:20736\n"
    "v_mfma_f32_32x32x16_bf16 v[32:47], v[192:195], v[224:227], v[32:47]\n"
    "ds_read_b64_tr_b16 v[212:213], v240 offset:20992\n"
    "v_mfma_f32_32x32x16_bf16 v[48:63], v[196:199], a[80:83], v[48:63]\n"
    "ds_read_b64_tr_b16 v[214:215], v240 offset:21248\n"
    "v_mfma_f32_32x32x16_bf16 v[64:79], v[196:199], a[208:211], v[64:79]\n"
    "ds_read_b64_tr_b16 v[216:217], v240 offset:21504\n"
    "v_mfma_f32_32x32x16_bf16 v[80:95], v[196:199], v[224:227], v[80:95]\n"
    "ds_read_b64_tr_b16 v[218:219], v240 offset:21760\n"
    "v_mfma_f32_32x32x16_bf16 v[96:111], v[200:203], a[80:83], v[96:111]\n"
    "ds_read_b64_tr_b16 v[220:221], v240 offset:22016\n"
    "v_mfma_f32_32x32x16_bf16 v[112:127], v[200:203], a[208:211], v[112:127]\n"
    "ds_read_b64_tr_b16 v[222:223], v240 offset:22272\n"
    "v_mfma_f32_32x32x16_bf16 v[128:143], v[200:203], v[224:227], v[128:143]\n"
    "s_waitcnt vmcnt(44)\n"
    "s_barrier\n"
    "ds_read_b128 v[228:231], v242 offset:4096\n"
    "s_mov_b32 m0, 81920\n"
    "s_add_u32 s66, s64, 81920\n"
    "buffer_load_dwordx4 v247, s[48:51], s66 offen lds\n"
    "v_mfma_f32_32x32x16_bf16 v[144:159], v[204:207], a[80:83], v[144:159]\n"
    "s_mov_b32 m0, 82816\n"
    "s_nop 0\n"
    "buffer_load_dwordx4 v247, s[48:51], s66 offen offset:128 lds\n"
    "v_mfma_f32_32x32x16_bf16 v[160:175], v[204:207], a[208:211], v[160:175]\n"
    "s_mov_b32 m0, 83712\n"
    "s_nop 0\n"
    "buffer_load_dwordx4 v247, s[48:51], s66 offen offset:256 lds\n"
    "v_mfma_f32_32x32x16_bf16 v[176:191], v[204:207], v[224:227], v[176:191]\n"
    "s_mov_b32 m0, 84608\n"
    "s_nop 0\n"
    "buffer_load_dwordx4 v247, s[48:51], s66 offen offset:384 lds\n"
    "s_waitcnt lgkmcnt(0)\n"
    "v_mfma_f32_32x32x16_bf16 v[0:15], v[208:211], a[84:87], v[0:15]\n"
    "ds_read_b64_tr_b16 v[192:193], v240 offset:24576\n"
    "s_mov_b32 m0, 85504\n"
    "s_nop 0\n"
    "buffer_load_dwordx4 v247, s[48:51], s66 offen offset:512 lds\n"
    "v_mfma_f32_32x32x16_bf16 v[16:31], v[208:211], a[212:215], v[16:31]\n"
    "ds_read_b64_tr_b16 v[194:195], v240 offset:24832\n"
    "s_mov_b32 m0, 114688\n"
    "s_add_u32 s67, s75, 3670016\n"
    "buffer_load_dwordx4 v248, s[44:47], s67 offen lds\n"
    "v_mfma_f32_32x32x16_bf16 v[32:47], v[208:211], v[228:231], v[32:47]\n"
    "ds_read_b64_tr_b16 v[196:197], v240 offset:25088\n"
    "s_mov_b32 m0, 115584\n"
    "s_nop 0\n"
    "buffer_load_dwordx4 v248, s[44:47], s67 offen offset:128 lds\n"
    "s_mov_b32 m0, 86400\n"
    "s_nop 0\n"
    "buffer_load_dwordx4 v247, s[48:51], s66 offen offset:640 lds\n"
    "v_mfma_f32_32x32x16_bf16 v[48:63], v[212:215], a[84:87], v[48:63]\n"
    "ds_read_b64_tr_b16 v[198:199], v240 offset:25344\n"
    "s_mov_b32 m0, 116736\n"
    "s_add_u32 s67, s75, 3735552\n"
    "buffer_load_dwordx4 v248, s[44:47], s67 offen lds\n"
    "s_mov_b32 m0, 87296\n"
    "s_nop 0\n"
    "buffer_load_dwordx4 v247, s[48:51], s66 offen offset:768 lds\n"
    "v_mfma_f32_32x32x16_bf16 v[64:79], v[212:215], a[212:215], v[64:79]\n"
    "ds_read_b64_tr_b16 v[200:201], v240 offset:25600\n"
    "s_mov_b32 m0, 117632\n"
    "s_nop 0\n"
    "buffer_load_dwordx4 v248, s[44:47], s67 offen offset:128 lds\n"
    "s_mov_b32 m0, 88192\n"
    "s_nop 0\n"
    "buffer_load_dwordx4 v247, s[48:51], s66 offen offset:896 lds\n"
    "v_mfma_f32_32x32x16_bf16 v[80:95], v[212:215], v[228:231], v[80:95]\n"
    "ds_read_b64_tr_b16 v[202:203], v240 offset:25856\n"
    "s_mov_b32 m0, 118784\n"
    "s_add_u32 s67, s75, 3801088\n"
    "buffer_load_dwordx4 v248, s[44:47], s67 offen lds\n"
    "s_mov_b32 m0, 90112\n"
    "s_add_u32 s66, s64, 90112\n"
    "buffer_load_dwordx4 v247, s[48:51], s66 offen lds\n"
    "v_mfma_f32_32x32x16_bf16 v[96:111], v[216:219], a[84:87], v[96:111]\n"
    "ds_read_b64_tr_b16 v[204:205], v240 offset:26112\n"
    "s_mov_b32 m0, 119680\n"
    "s_nop 0\n"
    "buffer_load_dwordx4 v248, s[44:47], s67 offen offset:128 lds\n"
    "s_mov_b32 m0, 91008\n"
    "s_nop 0\n"
    "buffer_load_dwordx4 v247, s[48:51], s66 offen offset:128 lds\n"
    "v_mfma_f32_32x32x16_bf16 v[112:127], v[216:219], a[212:215], v[112:127]\n"
    "ds_read_b64_tr_b16 v[206:207], v240 offset:26368\n"
    "s_mov_b32 m0, 120832\n"
    "s_add_u32 s67, s75, 3866624\n"
    "buffer_load_dwordx4 v248, s[44:47], s67 offen lds\n"
    "s_mov_b32 m0, 91904\n"
    "s_nop 0\n"
    "buffer_load_dwordx4 v247, s[48:51], s66 offen offset:256 lds\n"
    "v_mfma_f32_32x32x16_bf16 v[128:143], v[216:219], v[228:231], v[128:143]\n"
    "ds_read_b128 v[224:227], v241 offset:4608\n"
    "s_mov_b32 m0, 121728\n"
    "s_nop 0\n"
    "buffer_load_dwordx4 v248, s[44:47], s67 offen offset:128 lds\n"
    "s_mov_b32 m0, 92800\n"
    "s_nop 0\n"
    "s_waitcnt vmcnt(62)\n"
    "buffer_load_dwordx4 v247, s[48:51], s66 offen offset:384 lds\n"
    "v_mfma_f32_32x32x16_bf16 v[144:159], v[220:223], a[84:87], v[144:159]\n"
    "s_mov_b32 m0, 93696\n"
    "s_nop 0\n"
    "s_waitcnt vmcnt(62)\n"
    "buffer_load_dwordx4 v247, s[48:51], s66 offen offset:512 lds\n"
    "v_mfma_f32_32x32x16_bf16 v[160:175], v[220:223], a[212:215], v[160:175]\n"
    "s_mov_b32 m0, 94592\n"
    "s_nop 0\n"
    "s_waitcnt vmcnt(62)\n"
    "buffer_load_dwordx4 v247, s[48:51], s66 offen offset:640 lds\n"
    "v_mfma_f32_32x32x16_bf16 v[176:191], v[220:223], v[228:231], v[176:191]\n"
    "s_mov_b32 m0, 95488\n"
    "s_nop 0\n"
    "s_waitcnt vmcnt(62)\n"
    "buffer_load_dwordx4 v247, s[48:51], s66 offen offset:768 lds\n"
    "s_waitcnt lgkmcnt(0)\n"
    "v_mfma_f32_32x32x16_bf16 v[0:15], v[192:195], a[88:91], v[0:15]\n"
    "ds_read_b64_tr_b16 v[208:209], v240 offset:28672\n"
    "s_mov_b32 m0, 96384\n"
    "s_nop 0\n"
    "s_waitcnt vmcnt(62)\n"
    "buffer_load_dwordx4 v247, s[48:51], s66 offen offset:896 lds\n"
    "v_mfma_f32_32x32x16_bf16 v[16:31], v[192:195], a[216:219], v[16:31]\n"
    "s_waitcnt vmcnt(44)\n"
    "s_barrier\n"
    "ds_read_b64_tr_b16 v[210:211], v240 offset:28928\n"
    "v_mfma_f32_32x32x16_bf16 v[32:47], v[192:195], v[224:227], v[32:47]\n"
    "ds_read_b64_tr_b16 v[212:213], v240 offset:29184\n"
    "s_add_u32 m0, s61, 0\n"
    "s_add_u32 s66, s62, 98304\n"
    "buffer_load_dwordx4 v244, s[36:39], s66 offen nt lds\n"
    "buffer_load_dwordx4 v245, s[36:39], s66 offen offset:2048 nt lds\n"
    "v_mfma_f32_32x32x16_bf16 v[48:63], v[196:199], a[88:91], v[48:63]\n"
    "ds_read_b64_tr_b16 v[214:215], v240 offset:29440\n"
    "s_add_u32 m0, s61, 896\n"
    "s_nop 0\n"
    "buffer_load_dwordx4 v244, s[36:39], s66 offen offset:128 nt lds\n"
    "buffer_load_dwordx4 v245, s[36:39], s66 offen offset:2176 nt lds\n"
    "v_mfma_f32_32x32x16_bf16 v[64:79], v[196:199], a[216:219], v[64:79]\n"
    "ds_read_b64_tr_b16 v[216:217], v240 offset:29696\n"
    "v_mfma_f32_32x32x16_bf16 v[80:95], v[196:199], v[224:227], v[80:95]\n"
    "ds_read_b64_tr_b16 v[218:219], v240 offset:29952\n"
    "v_mfma_f32_32x32x16_bf16 v[96:111], v[200:203], a[88:91], v[96:111]\n"
    "ds_read_b64_tr_b16 v[220:221], v240 offset:30208\n"
    "v_mfma_f32_32x32x16_bf16 v[112:127], v[200:203], a[216:219], v[112:127]\n"
    "ds_read_b64_tr_b16 v[222:223], v240 offset:30464\n"
    "v_mfma_f32_32x32x16_bf16 v[128:143], v[200:203], v[224:227], v[128:143]\n"
    "ds_read_b128 v[228:231], v242 offset:4608\n"
    "v_mfma_f32_32x32x16_bf16 v[144:159], v[204:207], a[88:91], v[144:159]\n"
    "v_mfma_f32_32x32x16_bf16 v[160:175], v[204:207], a[216:219], v[160:175]\n"
    "v_mfma_f32_32x32x16_bf16 v[176:191], v[204:207], v[224:227], v[176:191]\n"
    "s_waitcnt lgkmcnt(0)\n"
    "v_mfma_f32_32x32x16_bf16 v[0:15], v[208:211], a[92:95], v[0:15]\n"
    "s_waitcnt vmcnt(32)\n"
    "ds_read_b64_tr_b16 v[192:193], v240 offset:0\n"
    "v_mfma_f32_32x32x16_bf16 v[16:31], v[208:211], a[220:223], v[16:31]\n"
    "ds_read_b64_tr_b16 v[194:195], v240 offset:256\n"
    "s_mov_b32 m0, 122880\n"
    "s_add_u32 s67, s75, 3932160\n"
    "buffer_load_dwordx4 v248, s[44:47], s67 offen lds\n"
    "v_mfma_f32_32x32x16_bf16 v[32:47], v[208:211], v[228:231], v[32:47]\n"
    "ds_read_b64_tr_b16 v[196:197], v240 offset:512\n"
    "s_mov_b32 m0, 123776\n"
    "s_nop 0\n"
    "buffer_load_dwordx4 v248, s[44:47], s67 offen offset:128 lds\n"
    "v_mfma_f32_32x32x16_bf16 v[48:63], v[212:215], a[92:95], v[48:63]\n"
    "ds_read_b64_tr_b16 v[198:199], v240 offset:768\n"
    "s_mov_b32 m0, 124928\n"
    "s_add_u32 s67, s75, 3997696\n"
    "buffer_load_dwordx4 v248, s[44:47], s67 offen lds\n"
    "v_mfma_f32_32x32x16_bf16 v[64:79], v[212:215], a[220:223], v[64:79]\n"
    "ds_read_b64_tr_b16 v[200:201], v240 offset:1024\n"
    "s_mov_b32 m0, 125824\n"
    "s_nop 0\n"
    "buffer_load_dwordx4 v248, s[44:47], s67 offen offset:128 lds\n"
    "v_mfma_f32_32x32x16_bf16 v[80:95], v[212:215], v[228:231], v[80:95]\n"
    "ds_read_b64_tr_b16 v[202:203], v240 offset:1280\n"
    "s_mov_b32 m0, 126976\n"
    "s_add_u32 s67, s75, 4063232\n"
    "buffer_load_dwordx4 v248, s[44:47], s67 offen lds\n"
    "v_mfma_f32_32x32x16_bf16 v[96:111], v[216:219], a[92:95], v[96:111]\n"
    "s_barrier\n"
    "ds_read_b64_tr_b16 v[204:205], v240 offset:1536\n"
    "s_mov_b32 m0, 127872\n"
    "s_nop 0\n"
    "buffer_load_dwordx4 v248, s[44:47], s67 offen offset:128 lds\n"
    "v_mfma_f32_32x32x16_bf16 v[112:127], v[216:219], a[220:223], v[112:127]\n"
    "ds_read_b64_tr_b16 v[206:207], v240 offset:1792\n"
    "s_mov_b32 m0, 129024\n"
    "s_add_u32 s67, s75, 4128768\n"
    "buffer_load_dwordx4 v248, s[44:47], s67 offen lds\n"
    "v_mfma_f32_32x32x16_bf16 v[128:143], v[216:219], v[228:231], v[128:143]\n"
    "ds_read_b128 v[224:227], v241 offset:0\n"
    "s_mov_b32 m0, 129920\n"
    "s_nop 0\n"
    "buffer_load_dwordx4 v248, s[44:47], s67 offen offset:128 lds\n"
    "v_mfma_f32_32x32x16_bf16 v[144:159], v[220:223], a[92:95], v[144:159]\n"
    "s_mov_b32 m0, 159744\n"
    "s_mov_b32 s67, 66432\n"
    "buffer_load_dwordx4 v249, s[52:55], s67 offen lds\n"
    "v_mfma_f32_32x32x16_bf16 v[160:175], v[220:223], a[220:223], v[160:175]\n"
    "s_mov_b32 m0, 160768\n"
    "s_mov_b32 s67, 74624\n"
    "buffer_load_dwordx4 v250, s[52:55], s67 offen lds\n"
    "v_mfma_f32_32x32x16_bf16 v[176:191], v[220:223], v[228:231], v[176:191]\n"
    "s_mov_b32 m0, 161792\n"
    "s_mov_b32 s67, 82816\n"
    "buffer_load_dwordx4 v249, s[52:55], s67 offen lds\n"
    "s_mov_b32 m0, 162816\n"
    "s_mov_b32 s67, 91008\n"
    "buffer_load_dwordx4 v250, s[52:55], s67 offen lds\n"
    "s_waitcnt lgkmcnt(0)\n"
    "v_mfma_f32_32x32x16_bf16 v[0:15], v[192:195], a[96:99], v[0:15]\n"
    "ds_read_b64_tr_b16 v[208:209], v240 offset:4096\n"
    "s_add_u32 m0, s61, 49152\n"
    "s_add_u32 s66, s62, 81920\n"
    "buffer_load_dwordx4 v246, s[40:43], s66 offen nt lds\n"
    "buffer_load_dwordx4 v246, s[40:43], s66 offen offset:2048 nt lds\n"
    "v_mfma_f32_32x32x16_bf16 v[16:31], v[192:195], a[224:227], v[16:31]\n"
    "ds_read_b64_tr_b16 v[210:211], v240 offset:4352\n"
    "s_add_u32 m0, s61, 50048\n"
    "s_nop 0\n"
    "buffer_load_dwordx4 v246, s[40:43], s66 offen offset:128 nt lds\n"
    "buffer_load_dwordx4 v246, s[40:43], s66 offen offset:2176 nt lds\n"
    "v_mfma_f32_32x32x16_bf16 v[32:47], v[192:195], v[224:227], v[32:47]\n"
    "ds_read_b64_tr_b16 v[212:213], v240 offset:4608\n"
    "v_mfma_f32_32x32x16_bf16 v[48:63], v[196:199], a[96:99], v[48:63]\n"
    "ds_read_b64_tr_b16 v[214:215], v240 offset:4864\n"
    "v_mfma_f32_32x32x16_bf16 v[64:79], v[196:199], a[224:227], v[64:79]\n"
    "ds_read_b64_tr_b16 v[216:217], v240 offset:5120\n"
    "v_mfma_f32_32x32x16_bf16 v[80:95], v[196:199], v[224:227], v[80:95]\n"
    "ds_read_b64_tr_b16 v[218:219], v240 offset:5376\n"
    "v_mfma_f32_32x32x16_bf16 v[96:111], v[200:203], a[96:99], v[96:111]\n"
    "ds_read_b64_tr_b16 v[220:221], v240 offset:5632\n"
    "v_mfma_f32_32x32x16_bf16 v[112:127], v[200:203], a[224:227], v[112:127]\n"
    "ds_read_b64_tr_b16 v[222:223], v240 offset:5888\n"
    "v_mfma_f32_32x32x16_bf16 v[128:143], v[200:203], v[224:227], v[128:143]\n"
    "ds_read_b128 v[228:231], v242 offset:0\n"
    "v_mfma_f32_32x32x16_bf16 v[144:159], v[204:207], a[96:99], v[144:159]\n"
    "v_mfma_f32_32x32x16_bf16 v[160:175], v[204:207], a[224:227], v[160:175]\n"
    "s_waitcnt vmcnt(44)\n"
    "s_barrier\n"
    "s_mov_b32 m0, 65536\n"
    "s_add_u32 s66, s64, 98304\n"
    "buffer_load_dwordx4 v247, s[48:51], s66 offen lds\n"
    "v_mfma_f32_32x32x16_bf16 v[176:191], v[204:207], v[224:227], v[176:191]\n"
    "s_mov_b32 m0, 66432\n"
    "s_nop 0\n"
    "buffer_load_dwordx4 v247, s[48:51], s66 offen offset:128 lds\n"
    "s_waitcnt lgkmcnt(0)\n"
    "v_mfma_f32_32x32x16_bf16 v[0:15], v[208:211], a[100:103], v[0:15]\n"
    "ds_read_b64_tr_b16 v[192:193], v240 offset:8192\n"
    "s_mov_b32 m0, 67328\n"
    "s_nop 0\n"
    "buffer_load_dwordx4 v247, s[48:51], s66 offen offset:256 lds\n"
    "v_mfma_f32_32x32x16_bf16 v[16:31], v[208:211], a[228:231], v[16:31]\n"
    "ds_read_b64_tr_b16 v[194:195], v240 offset:8448\n"
    "s_mov_b32 m0, 98304\n"
    "s_add_u32 s67, s76, 0\n"
    "buffer_load_dwordx4 v248, s[44:47], s67 offen lds\n"
    "v_mfma_f32_32x32x16_bf16 v[32:47], v[208:211], v[228:231], v[32:47]\n"
    "ds_read_b64_tr_b16 v[196:197], v240 offset:8704\n"
    "s_mov_b32 m0, 99200\n"
    "s_nop 0\n"
    "buffer_load_dwordx4 v248, s[44:47], s67 offen offset:128 lds\n"
    "s_mov_b32 m0, 68224\n"
    "s_nop 0\n"
    "buffer_load_dwordx4 v247, s[48:51], s66 offen offset:384 lds\n"
    "v_mfma_f32_32x32x16_bf16 v[48:63], v[212:215], a[100:103], v[48:63]\n"
    "ds_read_b64_tr_b16 v[198:199], v240 offset:8960\n"
    "s_mov_b32 m0, 100352\n"
    "s_add_u32 s67, s76, 65536\n"
    "buffer_load_dwordx4 v248, s[44:47], s67 offen lds\n"
    "s_mov_b32 m0, 69120\n"
    "s_nop 0\n"
    "buffer_load_dwordx4 v247, s[48:51], s66 offen offset:512 lds\n"
    "v_mfma_f32_32x32x16_bf16 v[64:79], v[212:215], a[228:231], v[64:79]\n"
    "ds_read_b64_tr_b16 v[200:201], v240 offset:9216\n"
    "s_mov_b32 m0, 101248\n"
    "s_nop 0\n"
    "buffer_load_dwordx4 v248, s[44:47], s67 offen offset:128 lds\n"
    "s_mov_b32 m0, 70016\n"
    "s_nop 0\n"
    "buffer_load_dwordx4 v247, s[48:51], s66 offen offset:640 lds\n"
    "v_mfma_f32_32x32x16_bf16 v[80:95], v[212:215], v[228:231], v[80:95]\n"
    "ds_read_b64_tr_b16 v[202:203], v240 offset:9472\n"
    "s_mov_b32 m0, 102400\n"
    "s_add_u32 s67, s76, 131072\n"
    "buffer_load_dwordx4 v248, s[44:47], s67 offen lds\n"
    "s_mov_b32 m0, 70912\n"
    "s_nop 0\n"
    "buffer_load_dwordx4 v247, s[48:51], s66 offen offset:768 lds\n"
    "v_mfma_f32_32x32x16_bf16 v[96:111], v[216:219], a[100:103], v[96:111]\n"
    "ds_read_b64_tr_b16 v[204:205], v240 offset:9728\n"
    "s_mov_b32 m0, 103296\n"
    "s_nop 0\n"
    "buffer_load_dwordx4 v248, s[44:47], s67 offen offset:128 lds\n"
    "s_mov_b32 m0, 71808\n"
    "s_nop 0\n"
    "buffer_load_dwordx4 v247, s[48:51], s66 offen offset:896 lds\n"
    "v_mfma_f32_32x32x16_bf16 v[112:127], v[216:219], a[228:231], v[112:127]\n"
    "ds_read_b64_tr_b16 v[206:207], v240 offset:9984\n"
    "s_mov_b32 m0, 104448\n"
    "s_add_u32 s67, s76, 196608\n"
    "buffer_load_dwordx4 v248, s[44:47], s67 offen lds\n"
    "s_mov_b32 m0, 73728\n"
    "s_add_u32 s66, s64, 106496\n"
    "buffer_load_dwordx4 v247, s[48:51], s66 offen lds\n"
    "v_mfma_f32_32x32x16_bf16 v[128:143], v[216:219], v[228:231], v[128:143]\n"
    "ds_read_b128 v[224:227], v241 offset:512\n"
    "s_mov_b32 m0, 105344\n"
    "s_nop 0\n"
    "buffer_load_dwordx4 v248, s[44:47], s67 offen offset:128 lds\n"
    "s_mov_b32 m0, 74624\n"
    "s_nop 0\n"
    "buffer_load_dwordx4 v247, s[48:51], s66 offen offset:128 lds\n"
    "v_mfma_f32_32x32x16_bf16 v[144:159], v[220:223], a[100:103], v[144:159]\n"
    "s_mov_b32 m0, 75520\n"
    "s_nop 0\n"
    "buffer_load_dwordx4 v247, s[48:51], s66 offen offset:256 lds\n"
    "v_mfma_f32_32x32x16_bf16 v[160:175], v[220:223], a[228:231], v[160:175]\n"
    "s_mov_b32 m0, 76416\n"
    "s_nop 0\n"
    "s_waitcnt vmcnt(62)\n"
    "buffer_load_dwordx4 v247, s[48:51], s66 offen offset:384 lds\n"
    "v_mfma_f32_32x32x16_bf16 v[176:191], v[220:223], v[228:231], v[176:191]\n"
    "s_mov_b32 m0, 77312\n"
    "s_nop 0\n"
    "s_waitcnt vmcnt(62)\n"
    "buffer_load_dwordx4 v247, s[48:51], s66 offen offset:512 lds\n"
    "s_waitcnt lgkmcnt(0)\n"
    "v_mfma_f32_32x32x16_bf16 v[0:15], v[192:195], a[104:107], v[0:15]\n"
    "ds_read_b64_tr_b16 v[208:209], v240 offset:12288\n"
    "s_mov_b32 m0, 78208\n"
    "s_nop 0\n"
    "s_waitcnt vmcnt(62)\n"
    "buffer_load_dwordx4 v247, s[48:51], s66 offen offset:640 lds\n"
    "v_mfma_f32_32x32x16_bf16 v[16:31], v[192:195], a[232:235], v[16:31]\n"
    "ds_read_b64_tr_b16 v[210:211], v240 offset:12544\n"
    "s_mov_b32 m0, 79104\n"
    "s_nop 0\n"
    "s_waitcnt vmcnt(62)\n"
    "buffer_load_dwordx4 v247, s[48:51], s66 offen offset:768 lds\n"
    "v_mfma_f32_32x32x16_bf16 v[32:47], v[192:195], v[224:227], v[32:47]\n"
    "ds_read_b64_tr_b16 v[212:213], v240 offset:12800\n"
    "s_mov_b32 m0, 80000\n"
    "s_nop 0\n"
    "s_waitcnt vmcnt(62)\n"
    "buffer_load_dwordx4 v247, s[48:51], s66 offen offset:896 lds\n"
    "v_mfma_f32_32x32x16_bf16 v[48:63], v[196:199], a[104:107], v[48:63]\n"
    "s_waitcnt vmcnt(44)\n"
    "s_barrier\n"
    "ds_read_b64_tr_b16 v[214:215], v240 offset:13056\n"
    "v_mfma_f32_32x32x16_bf16 v[64:79], v[196:199], a[232:235], v[64:79]\n"
    "ds_read_b64_tr_b16 v[216:217], v240 offset:13312\n"
    "s_add_u32 m0, s61, 16384\n"
    "s_add_u32 s66, s62, 114688\n"
    "buffer_load_dwordx4 v244, s[36:39], s66 offen nt lds\n"
    "buffer_load_dwordx4 v245, s[36:39], s66 offen offset:2048 nt lds\n"
    "v_mfma_f32_32x32x16_bf16 v[80:95], v[196:199], v[224:227], v[80:95]\n"
    "ds_read_b64_tr_b16 v[218:219], v240 offset:13568\n"
    "s_add_u32 m0, s61, 17280\n"
    "s_nop 0\n"
    "buffer_load_dwordx4 v244, s[36:39], s66 offen offset:128 nt lds\n"
    "buffer_load_dwordx4 v245, s[36:39], s66 offen offset:2176 nt lds\n"
    "v_mfma_f32_32x32x16_bf16 v[96:111], v[200:203], a[104:107], v[96:111]\n"
    "ds_read_b64_tr_b16 v[220:221], v240 offset:13824\n"
    "v_mfma_f32_32x32x16_bf16 v[112:127], v[200:203], a[232:235], v[112:127]\n"
    "ds_read_b64_tr_b16 v[222:223], v240 offset:14080\n"
    "v_mfma_f32_32x32x16_bf16 v[128:143], v[200:203], v[224:227], v[128:143]\n"
    "ds_read_b128 v[228:231], v242 offset:512\n"
    "v_mfma_f32_32x32x16_bf16 v[144:159], v[204:207], a[104:107], v[144:159]\n"
    "v_mfma_f32_32x32x16_bf16 v[160:175], v[204:207], a[232:235], v[160:175]\n"
    "v_mfma_f32_32x32x16_bf16 v[176:191], v[204:207], v[224:227], v[176:191]\n"
    "s_waitcnt lgkmcnt(0)\n"
    "v_mfma_f32_32x32x16_bf16 v[0:15], v[208:211], a[108:111], v[0:15]\n"
    "s_waitcnt vmcnt(32)\n"
    "ds_read_b64_tr_b16 v[192:193], v240 offset:16384\n"
    "v_mfma_f32_32x32x16_bf16 v[16:31], v[208:211], a[236:239], v[16:31]\n"
    "ds_read_b64_tr_b16 v[194:195], v240 offset:16640\n"
    "s_mov_b32 m0, 106496\n"
    "s_add_u32 s67, s76, 262144\n"
    "buffer_load_dwordx4 v248, s[44:47], s67 offen lds\n"
    "v_mfma_f32_32x32x16_bf16 v[32:47], v[208:211], v[228:231], v[32:47]\n"
    "ds_read_b64_tr_b16 v[196:197], v240 offset:16896\n"
    "s_mov_b32 m0, 107392\n"
    "s_nop 0\n"
    "buffer_load_dwordx4 v248, s[44:47], s67 offen offset:128 lds\n"
    "v_mfma_f32_32x32x16_bf16 v[48:63], v[212:215], a[108:111], v[48:63]\n"
    "ds_read_b64_tr_b16 v[198:199], v240 offset:17152\n"
    "s_mov_b32 m0, 108544\n"
    "s_add_u32 s67, s76, 327680\n"
    "buffer_load_dwordx4 v248, s[44:47], s67 offen lds\n"
    "v_mfma_f32_32x32x16_bf16 v[64:79], v[212:215], a[236:239], v[64:79]\n"
    "ds_read_b64_tr_b16 v[200:201], v240 offset:17408\n"
    "s_mov_b32 m0, 109440\n"
    "s_nop 0\n"
    "buffer_load_dwordx4 v248, s[44:47], s67 offen offset:128 lds\n"
    "v_mfma_f32_32x32x16_bf16 v[80:95], v[212:215], v[228:231], v[80:95]\n"
    "ds_read_b64_tr_b16 v[202:203], v240 offset:17664\n"
    "s_mov_b32 m0, 110592\n"
    "s_add_u32 s67, s76, 393216\n"
    "buffer_load_dwordx4 v248, s[44:47], s67 offen lds\n"
    "v_mfma_f32_32x32x16_bf16 v[96:111], v[216:219], a[108:111], v[96:111]\n"
    "ds_read_b64_tr_b16 v[204:205], v240 offset:17920\n"
    "s_mov_b32 m0, 111488\n"
    "s_nop 0\n"
    "buffer_load_dwordx4 v248, s[44:47], s67 offen offset:128 lds\n"
    "v_mfma_f32_32x32x16_bf16 v[112:127], v[216:219], a[236:239], v[112:127]\n"
    "ds_read_b64_tr_b16 v[206:207], v240 offset:18176\n"
    "s_mov_b32 m0, 112640\n"
    "s_add_u32 s67, s76, 458752\n"
    "buffer_load_dwordx4 v248, s[44:47], s67 offen lds\n"
    "v_mfma_f32_32x32x16_bf16 v[128:143], v[216:219], v[228:231], v[128:143]\n"
    "s_barrier\n"
    "ds_read_b128 v[224:227], v241 offset:4096\n"
    "s_mov_b32 m0, 113536\n"
    "s_nop 0\n"
    "buffer_load_dwordx4 v248, s[44:47], s67 offen offset:128 lds\n"
    "v_mfma_f32_32x32x16_bf16 v[144:159], v[220:223], a[108:111], v[144:159]\n"
    "s_mov_b32 m0, 155648\n"
    "s_mov_b32 s67, 65536\n"
    "buffer_load_dwordx4 v249, s[52:55], s67 offen lds\n"
    "v_mfma_f32_32x32x16_bf16 v[160:175], v[220:223], a[236:239], v[160:175]\n"
    "s_mov_b32 m0, 156672\n"
    "s_mov_b32 s67, 73728\n"
    "buffer_load_dwordx4 v250, s[52:55], s67 offen lds\n"
    "v_mfma_f32_32x32x16_bf16 v[176:191], v[220:223], v[228:231], v[176:191]\n"
    "s_mov_b32 m0, 157696\n"
    "s_mov_b32 s67, 81920\n"
    "buffer_load_dwordx4 v249, s[52:55], s67 offen lds\n"
    "s_mov_b32 m0, 158720\n"
    "s_mov_b32 s67, 90112\n"
    "buffer_load_dwordx4 v250, s[52:55], s67 offen lds\n"
    "s_waitcnt lgkmcnt(0)\n"
    "v_mfma_f32_32x32x16_bf16 v[0:15], v[192:195], a[112:115], v[0:15]\n"
    "ds_read_b64_tr_b16 v[208:209], v240 offset:20480\n"
    "s_add_u32 m0, s61, 32768\n"
    "s_add_u32 s66, s62, 98304\n"
    "buffer_load_dwordx4 v246, s[40:43], s66 offen nt lds\n"
    "buffer_load_dwordx4 v246, s[40:43], s66 offen offset:2048 nt lds\n"
    "v_mfma_f32_32x32x16_bf16 v[16:31], v[192:195], a[240:243], v[16:31]\n"
    "ds_read_b64_tr_b16 v[210:211], v240 offset:20736\n"
    "s_add_u32 m0, s61, 33664\n"
    "s_nop 0\n"
    "buffer_load_dwordx4 v246, s[40:43], s66 offen offset:128 nt lds\n"
    "buffer_load_dwordx4 v246, s[40:43], s66 offen offset:2176 nt lds\n"
    "v_mfma_f32_32x32x16_bf16 v[32:47], v[192:195], v[224:227], v[32:47]\n"
    "ds_read_b64_tr_b16 v[212:213], v240 offset:20992\n"
    "v_mfma_f32_32x32x16_bf16 v[48:63], v[196:199], a[112:115], v[48:63]\n"
    "ds_read_b64_tr_b16 v[214:215], v240 offset:21248\n"
    "v_mfma_f32_32x32x16_bf16 v[64:79], v[196:199], a[240:243], v[64:79]\n"
    "ds_read_b64_tr_b16 v[216:217], v240 offset:21504\n"
    "v_mfma_f32_32x32x16_bf16 v[80:95], v[196:199], v[224:227], v[80:95]\n"
    "ds_read_b64_tr_b16 v[218:219], v240 offset:21760\n"
    "v_mfma_f32_32x32x16_bf16 v[96:111], v[200:203], a[112:115], v[96:111]\n"
    "ds_read_b64_tr_b16 v[220:221], v240 offset:22016\n"
    "v_mfma_f32_32x32x16_bf16 v[112:127], v[200:203], a[240:243], v[112:127]\n"
    "ds_read_b64_tr_b16 v[222:223], v240 offset:22272\n"
    "v_mfma_f32_32x32x16_bf16 v[128:143], v[200:203], v[224:227], v[128:143]\n"
    "ds_read_b128 v[228:231], v242 offset:4096\n"
    "v_mfma_f32_32x32x16_bf16 v[144:159], v[204:207], a[112:115], v[144:159]\n"
    "v_mfma_f32_32x32x16_bf16 v[160:175], v[204:207], a[240:243], v[160:175]\n"
    "v_mfma_f32_32x32x16_bf16 v[176:191], v[204:207], v[224:227], v[176:191]\n"
    "s_waitcnt lgkmcnt(0)\n"
    "v_mfma_f32_32x32x16_bf16 v[0:15], v[208:211], a[116:119], v[0:15]\n"
    "ds_read_b64_tr_b16 v[192:193], v240 offset:24576\n"
    "v_mfma_f32_32x32x16_bf16 v[16:31], v[208:211], a[244:247], v[16:31]\n"
    "s_waitcnt vmcnt(44)\n"
    "s_barrier\n"
    "ds_read_b64_tr_b16 v[194:195], v240 offset:24832\n"
    "s_mov_b32 m0, 114688\n"
    "s_add_u32 s67, s76, 524288\n"
    "buffer_load_dwordx4 v248, s[44:47], s67 offen lds\n"
    "v_mfma_f32_32x32x16_bf16 v[32:47], v[208:211], v[228:231], v[32:47]\n"
    "ds_read_b64_tr_b16 v[196:197], v240 offset:25088\n"
    "s_mov_b32 m0, 115584\n"
    "s_nop 0\n"
    "buffer_load_dwordx4 v248, s[44:47], s67 offen offset:128 lds\n"
    "s_mov_b32 m0, 81920\n"
    "s_add_u32 s66, s64, 114688\n"
    "buffer_load_dwordx4 v247, s[48:51], s66 offen lds\n"
    "v_mfma_f32_32x32x16_bf16 v[48:63], v[212:215], a[116:119], v[48:63]\n"
    "ds_read_b64_tr_b16 v[198:199], v240 offset:25344\n"
    "s_mov_b32 m0, 116736\n"
    "s_add_u32 s67, s76, 589824\n"
    "buffer_load_dwordx4 v248, s[44:47], s67 offen lds\n"
    "s_mov_b32 m0, 82816\n"
    "s_nop 0\n"
    "buffer_load_dwordx4 v247, s[48:51], s66 offen offset:128 lds\n"
    "v_mfma_f32_32x32x16_bf16 v[64:79], v[212:215], a[244:247], v[64:79]\n"
    "ds_read_b64_tr_b16 v[200:201], v240 offset:25600\n"
    "s_mov_b32 m0, 117632\n"
    "s_nop 0\n"
    "buffer_load_dwordx4 v248, s[44:47], s67 offen offset:128 lds\n"
    "s_mov_b32 m0, 83712\n"
    "s_nop 0\n"
    "buffer_load_dwordx4 v247, s[48:51], s66 offen offset:256 lds\n"
    "v_mfma_f32_32x32x16_bf16 v[80:95], v[212:215], v[228:231], v[80:95]\n"
    "ds_read_b64_tr_b16 v[202:203], v240 offset:25856\n"
    "s_mov_b32 m0, 118784\n"
    "s_add_u32 s67, s76, 655360\n"
    "buffer_load_dwordx4 v248, s[44:47], s67 offen lds\n"
    "s_mov_b32 m0, 84608\n"
    "s_nop 0\n"
    "buffer_load_dwordx4 v247, s[48:51], s66 offen offset:384 lds\n"
    "v_mfma_f32_32x32x16_bf16 v[96:111], v[216:219], a[116:119], v[96:111]\n"
    "ds_read_b64_tr_b16 v[204:205], v240 offset:26112\n"
    "s_mov_b32 m0, 119680\n"
    "s_nop 0\n"
    "buffer_load_dwordx4 v248, s[44:47], s67 offen offset:128 lds\n"
    "s_mov_b32 m0, 85504\n"
    "s_nop 0\n"
    "buffer_load_dwordx4 v247, s[48:51], s66 offen offset:512 lds\n"
    "v_mfma_f32_32x32x16_bf16 v[112:127], v[216:219], a[244:247], v[112:127]\n"
    "ds_read_b64_tr_b16 v[206:207], v240 offset:26368\n"
    "s_mov_b32 m0, 120832\n"
    "s_add_u32 s67, s76, 720896\n"
    "buffer_load_dwordx4 v248, s[44:47], s67 offen lds\n"
    "s_mov_b32 m0, 86400\n"
    "s_nop 0\n"
    "buffer_load_dwordx4 v247, s[48:51], s66 offen offset:640 lds\n"
    "v_mfma_f32_32x32x16_bf16 v[128:143], v[216:219], v[228:231], v[128:143]\n"
    "ds_read_b128 v[224:227], v241 offset:4608\n"
    "s_mov_b32 m0, 121728\n"
    "s_nop 0\n"
    "buffer_load_dwordx4 v248, s[44:47], s67 offen offset:128 lds\n"
    "s_mov_b32 m0, 87296\n"
    "s_nop 0\n"
    "buffer_load_dwordx4 v247, s[48:51], s66 offen offset:768 lds\n"
    "v_mfma_f32_32x32x16_bf16 v[144:159], v[220:223], a[116:119], v[144:159]\n"
    "s_mov_b32 m0, 88192\n"
    "s_nop 0\n"
    "buffer_load_dwordx4 v247, s[48:51], s66 offen offset:896 lds\n"
    "v_mfma_f32_32x32x16_bf16 v[160:175], v[220:223], a[244:247], v[160:175]\n"
    "s_mov_b32 m0, 90112\n"
    "s_add_u32 s66, s64, 122880\n"
    "buffer_load_dwordx4 v247, s[48:51], s66 offen lds\n"
    "v_mfma_f32_32x32x16_bf16 v[176:191], v[220:223], v[228:231], v[176:191]\n"
    "s_mov_b32 m0, 91008\n"
    "s_nop 0\n"
    "buffer_load_dwordx4 v247, s[48:51], s66 offen offset:128 lds\n"
    "ds_read_b64_tr_b16 v[208:209], v240 offset:28672\n"
    "ds_read_b64_tr_b16 v[210:211], v240 offset:28928\n"
    "ds_read_b64_tr_b16 v[212:213], v240 offset:29184\n"
    "ds_read_b64_tr_b16 v[214:215], v240 offset:29440\n"
    "ds_read_b64_tr_b16 v[216:217], v240 offset:29696\n"
    "ds_read_b64_tr_b16 v[218:219], v240 offset:29952\n"
    "s_waitcnt lgkmcnt(14)\n"
    "ds_read_b64_tr_b16 v[220:221], v240 offset:30208\n"
    "s_waitcnt lgkmcnt(14)\n"
    "ds_read_b64_tr_b16 v[222:223], v240 offset:30464\n"
    "s_waitcnt lgkmcnt(14)\n"
    "ds_read_b128 v[228:231], v242 offset:4608\n"
    "s_mov_b32 m0, 91904\n"
    "s_nop 0\n"
    "buffer_load_dwordx4 v247, s[48:51], s66 offen offset:256 lds\n"
    "v_mfma_f32_32x32x16_bf16 v[0:15], v[192:195], a[120:123], v[0:15]\n"
    "s_mov_b32 m0, 92800\n"
    "s_nop 0\n"
    "s_waitcnt vmcnt(62)\n"
    "buffer_load_dwordx4 v247, s[48:51], s66 offen offset:384 lds\n"
    "v_mfma_f32_32x32x16_bf16 v[16:31], v[192:195], a[248:251], v[16:31]\n"
    "s_mov_b32 m0, 93696\n"
    "s_nop 0\n"
    "s_waitcnt vmcnt(62)\n"
    "buffer_load_dwordx4 v247, s[48:51], s66 offen offset:512 lds\n"
    "s_waitcnt lgkmcnt(9)\n"
    "v_mfma_f32_32x32x16_bf16 v[32:47], v[192:195], v[224:227], v[32:47]\n"
    "s_mov_b32 m0, 94592\n"
    "s_nop 0\n"
    "s_waitcnt vmcnt(62)\n"
    "buffer_load_dwordx4 v247, s[48:51], s66 offen offset:640 lds\n"
    "s_waitcnt lgkmcnt(7)\n"
    "v_mfma_f32_32x32x16_bf16 v[0:15], v[208:211], a[124:127], v[0:15]\n"
    "s_waitcnt vmcnt(49)\n"
    "s_waitcnt vmcnt(26)\n"
    "ds_read_b64_tr_b16 v[192:193], v240 offset:0\n"
    "ds_read_b64_tr_b16 v[194:195], v240 offset:256\n"
    "s_mov_b32 m0, 95488\n"
    "s_nop 0\n"
    "buffer_load_dwordx4 v247, s[48:51], s66 offen offset:768 lds\n"
    "v_mfma_f32_32x32x16_bf16 v[16:31], v[208:211], a[252:255], v[16:31]\n"
    "s_mov_b32 m0, 122880\n"
    "s_add_u32 s67, s76, 786432\n"
    "buffer_load_dwordx4 v248, s[44:47], s67 offen lds\n"
    "s_mov_b32 m0, 123776\n"
    "s_nop 0\n"
    "buffer_load_dwordx4 v248, s[44:47], s67 offen offset:128 lds\n"
    "s_mov_b32 m0, 96384\n"
    "s_nop 0\n"
    "buffer_load_dwordx4 v247, s[48:51], s66 offen offset:896 lds\n"
    "s_waitcnt lgkmcnt(2)\n"
    "v_mfma_f32_32x32x16_bf16 v[32:47], v[208:211], v[228:231], v[32:47]\n"
    "s_barrier\n"
    "s_mov_b32 m0, 124928\n"
    "s_add_u32 s67, s76, 851968\n"
    "buffer_load_dwordx4 v248, s[44:47], s67 offen lds\n"
    "s_mov_b32 m0, 125824\n"
    "s_nop 0\n"
    "buffer_load_dwordx4 v248, s[44:47], s67 offen offset:128 lds\n"
    "v_mfma_f32_32x32x16_bf16 v[48:63], v[196:199], a[120:123], v[48:63]\n"
    "s_mov_b32 m0, 126976\n"
    "s_add_u32 s67, s76, 917504\n"
    "buffer_load_dwordx4 v248, s[44:47], s67 offen lds\n"
    "s_mov_b32 m0, 127872\n"
    "s_nop 0\n"
    "buffer_load_dwordx4 v248, s[44:47], s67 offen offset:128 lds\n"
    "v_mfma_f32_32x32x16_bf16 v[64:79], v[196:199], a[248:251], v[64:79]\n"
    "s_mov_b32 m0, 129024\n"
    "s_add_u32 s67, s76, 983040\n"
    "buffer_load_dwordx4 v248, s[44:47], s67 offen lds\n"
    "s_mov_b32 m0, 129920\n"
    "s_nop 0\n"
    "buffer_load_dwordx4 v248, s[44:47], s67 offen offset:128 lds\n"
    "v_mfma_f32_32x32x16_bf16 v[80:95], v[196:199], v[224:227], v[80:95]\n"
    "s_mov_b32 m0, 159744\n"
    "s_mov_b32 s67, 65664\n"
    "buffer_load_dwordx4 v249, s[52:55], s67 offen lds\n"
    "s_mov_b32 m0, 160768\n"
    "s_mov_b32 s67, 73856\n"
    "buffer_load_dwordx4 v250, s[52:55], s67 offen lds\n"
    "v_mfma_f32_32x32x16_bf16 v[48:63], v[212:215], a[124:127], v[48:63]\n"
    "ds_read_b64_tr_b16 v[196:197], v240 offset:512\n"
    "ds_read_b64_tr_b16 v[198:199], v240 offset:768\n"
    "s_mov_b32 m0, 161792\n"
    "s_mov_b32 s67, 82048\n"
    "buffer_load_dwordx4 v249, s[52:55], s67 offen lds\n"
    "v_mfma_f32_32x32x16_bf16 v[64:79], v[212:215], a[252:255], v[64:79]\n"
    "s_mov_b32 m0, 162816\n"
    "s_mov_b32 s67, 90240\n"
    "buffer_load_dwordx4 v250, s[52:55], s67 offen lds\n"
    "v_pk_mul_f32 v[0:1], v[0:1], s[72:73]\n"
    "v_mfma_f32_32x32x16_bf16 v[80:95], v[212:215], v[228:231], v[80:95]\n"
    "v_pk_mul_f32 v[2:3], v[2:3], s[72:73]\n"
    "v_pk_mul_f32 v[4:5], v[4:5], s[72:73]\n"
    "v_pk_mul_f32 v[6:7], v[6:7], s[72:73]\n"
    "s_add_u32 m0, s61, 0\n"
    "s_add_u32 s66, s63, 0\n"
    "buffer_load_dwordx4 v244, s[36:39], s66 offen nt lds\n"
    "buffer_load_dwordx4 v245, s[36:39], s66 offen offset:2048 nt lds\n"
    "v_mfma_f32_32x32x16_bf16 v[96:111], v[200:203], a[120:123], v[96:111]\n"
    "v_pk_mul_f32 v[8:9], v[8:9], s[72:73]\n"
    "v_pk_mul_f32 v[10:11], v[10:11], s[72:73]\n"
    "v_pk_mul_f32 v[12:13], v[12:13], s[72:73]\n"
    "v_mfma_f32_32x32x16_bf16 v[112:127], v[200:203], a[248:251], v[112:127]\n"
    "v_pk_mul_f32 v[14:15], v[14:15], s[72:73]\n"
    "v_cvt_pk_bf16_f32 v232, v0, v1\n"
    "v_cvt_pk_bf16_f32 v233, v2, v3\n"
    "v_cvt_pk_bf16_f32 v234, v4, v5\n"
    "v_cvt_pk_bf16_f32 v235, v6, v7\n"
    "v_mfma_f32_32x32x16_bf16 v[128:143], v[200:203], v[224:227], v[128:143]\n"
    "v_cvt_pk_bf16_f32 v236, v8, v9\n"
    "v_cvt_pk_bf16_f32 v237, v10, v11\n"
    "v_cvt_pk_bf16_f32 v238, v12, v13\n"
    "v_cvt_pk_bf16_f32 v239, v14, v15\n"
    "v_mfma_f32_32x32x16_bf16 v[96:111], v[216:219], a[124:127], v[96:111]\n"
    "ds_read_b64_tr_b16 v[200:201], v240 offset:1024\n"
    "ds_read_b64_tr_b16 v[202:203], v240 offset:1280\n"
    "ds_write_b128 v243, v[232:235] offset:0\n"
    "v_mfma_f32_32x32x16_bf16 v[112:127], v[216:219], a[252:255], v[112:127]\n"
    "ds_write_b128 v243, v[236:239] offset:1024\n"
    "v_pk_mul_f32 v[16:17], v[16:17], s[72:73]\n"
    "v_mfma_f32_32x32x16_bf16 v[128:143], v[216:219], v[228:231], v[128:143]\n"
    "v_pk_mul_f32 v[18:19], v[18:19], s[72:73]\n"
    "v_pk_mul_f32 v[20:21], v[20:21], s[72:73]\n"
    "v_pk_mul_f32 v[22:23], v[22:23], s[72:73]\n"
    "v_mfma_f32_32x32x16_bf16 v[144:159], v[204:207], a[120:123], v[144:159]\n"
    "v_pk_mul_f32 v[24:25], v[24:25], s[72:73]\n"
    "v_pk_mul_f32 v[26:27], v[26:27], s[72:73]\n"
    "v_pk_mul_f32 v[28:29], v[28:29], s[72:73]\n"
    "s_add_u32 m0, s61, 896\n"
    "s_nop 0\n"
    "buffer_load_dwordx4 v244, s[36:39], s66 offen offset:128 nt lds\n"
    "buffer_load_dwordx4 v245, s[36:39], s66 offen offset:2176 nt lds\n"
    "v_mfma_f32_32x32x16_bf16 v[160:175], v[204:207], a[248:251], v[160:175]\n"
    "s_barrier\n"
    "v_pk_mul_f32 v[30:31], v[30:31], s[72:73]\n"
    "v_cvt_pk_bf16_f32 v232, v16, v17\n"
    "v_cvt_pk_bf16_f32 v233, v18, v19\n"
    "v_cvt_pk_bf16_f32 v234, v20, v21\n"
    "v_cvt_pk_bf16_f32 v235, v22, v23\n"
    "v_mfma_f32_32x32x16_bf16 v[176:191], v[204:207], v[224:227], v[176:191]\n"
    "v_cvt_pk_bf16_f32 v236, v24, v25\n"
    "v_cvt_pk_bf16_f32 v237, v26, v27\n"
    "v_cvt_pk_bf16_f32 v238, v28, v29\n"
    "v_cvt_pk_bf16_f32 v239, v30, v31\n"
    "v_mfma_f32_32x32x16_bf16 v[144:159], v[220:223], a[124:127], v[144:159]\n"
    "ds_read_b64_tr_b16 v[204:205], v240 offset:1536\n"
    "ds_read_b64_tr_b16 v[206:207], v240 offset:1792\n"
    "ds_read_b128 v[224:227], v241 offset:0\n"
    "ds_write_b128 v243, v[232:235] offset:8192\n"
    "v_mfma_f32_32x32x16_bf16 v[160:175], v[220:223], a[252:255], v[160:175]\n"
    "ds_write_b128 v243, v[236:239] offset:9216\n"
    "v_pk_mul_f32 v[32:33], v[32:33], s[72:73]\n"
    "v_mfma_f32_32x32x16_bf16 v[176:191], v[220:223], v[228:231], v[176:191]\n"
    "v_pk_mul_f32 v[34:35], v[34:35], s[72:73]\n"
    "v_pk_mul_f32 v[36:37], v[36:37], s[72:73]\n"
    "v_pk_mul_f32 v[38:39], v[38:39], s[72:73]\n"
    "s_add_u32 m0, s61, 49152\n"
    "s_add_u32 s66, s62, 114688\n"
    "buffer_load_dwordx4 v246, s[40:43], s66 offen nt lds\n"
    "buffer_load_dwordx4 v246, s[40:43], s66 offen offset:2048 nt lds\n"
    "s_waitcnt lgkmcnt(11)\n"
    "v_mfma_f32_32x32x16_bf16 v[0:15], v[192:195], a[0:3], 0\n"
    "ds_read_b64_tr_b16 v[208:209], v240 offset:4096\n"
    "v_pk_mul_f32 v[40:41], v[40:41], s[72:73]\n"
    "v_pk_mul_f32 v[42:43], v[42:43], s[72:73]\n"
    "v_pk_mul_f32 v[44:45], v[44:45], s[72:73]\n"
    "v_mfma_f32_32x32x16_bf16 v[16:31], v[192:195], a[128:131], 0\n"
    "ds_read_b64_tr_b16 v[210:211], v240 offset:4352\n"
    "v_pk_mul_f32 v[46:47], v[46:47], s[72:73]\n"
    "v_cvt_pk_bf16_f32 v232, v32, v33\n"
    "v_cvt_pk_bf16_f32 v233, v34, v35\n"
    "v_cvt_pk_bf16_f32 v234, v36, v37\n"
    "v_cvt_pk_bf16_f32 v235, v38, v39\n"
    "v_cvt_pk_bf16_f32 v236, v40, v41\n"
    "v_cvt_pk_bf16_f32 v237, v42, v43\n"
    "v_cvt_pk_bf16_f32 v238, v44, v45\n"
    "v_cvt_pk_bf16_f32 v239, v46, v47\n"
    "ds_write_b128 v243, v[232:235] offset:16384\n"
    "ds_write_b128 v243, v[236:239] offset:17408\n"
    "s_waitcnt lgkmcnt(6)\n"
    "v_mfma_f32_32x32x16_bf16 v[32:47], v[192:195], v[224:227], 0\n"
    "ds_read_b64_tr_b16 v[212:213], v240 offset:4608\n"
    "v_pk_mul_f32 v[48:49], v[48:49], s[72:73]\n"
    "v_pk_mul_f32 v[50:51], v[50:51], s[72:73]\n"
    "v_pk_mul_f32 v[52:53], v[52:53], s[72:73]\n"
    "v_pk_mul_f32 v[54:55], v[54:55], s[72:73]\n"
    "v_pk_mul_f32 v[56:57], v[56:57], s[72:73]\n"
    "v_pk_mul_f32 v[58:59], v[58:59], s[72:73]\n"
    "v_pk_mul_f32 v[60:61], v[60:61], s[72:73]\n"
    "v_pk_mul_f32 v[62:63], v[62:63], s[72:73]\n"
    "v_cvt_pk_bf16_f32 v232, v48, v49\n"
    "v_cvt_pk_bf16_f32 v233, v50, v51\n"
    "v_cvt_pk_bf16_f32 v234, v52, v53\n"
    "v_cvt_pk_bf16_f32 v235, v54, v55\n"
    "v_cvt_pk_bf16_f32 v236, v56, v57\n"
    "v_cvt_pk_bf16_f32 v237, v58, v59\n"
    "v_cvt_pk_bf16_f32 v238, v60, v61\n"
    "v_cvt_pk_bf16_f32 v239, v62, v63\n"
    "ds_write_b128 v243, v[232:235] offset:2048\n"
    "ds_write_b128 v243, v[236:239] offset:3072\n"
    "v_mfma_f32_32x32x16_bf16 v[48:63], v[196:199], a[0:3], 0\n"
    "ds_read_b64_tr_b16 v[214:215], v240 offset:4864\n"
    "v_pk_mul_f32 v[64:65], v[64:65], s[72:73]\n"
    "v_pk_mul_f32 v[66:67], v[66:67], s[72:73]\n"
    "v_pk_mul_f32 v[68:69], v[68:69], s[72:73]\n"
    "v_pk_mul_f32 v[70:71], v[70:71], s[72:73]\n"
    "v_pk_mul_f32 v[72:73], v[72:73], s[72:73]\n"
    "v_pk_mul_f32 v[74:75], v[74:75], s[72:73]\n"
    "v_pk_mul_f32 v[76:77], v[76:77], s[72:73]\n"
    "v_pk_mul_f32 v[78:79], v[78:79], s[72:73]\n"
    "v_cvt_pk_bf16_f32 v232, v64, v65\n"
    "v_cvt_pk_bf16_f32 v233, v66, v67\n"
    "v_cvt_pk_bf16_f32 v234, v68, v69\n"
    "v_cvt_pk_bf16_f32 v235, v70, v71\n"
    "v_cvt_pk_bf16_f32 v236, v72, v73\n"
    "v_cvt_pk_bf16_f32 v237, v74, v75\n"
    "v_cvt_pk_bf16_f32 v238, v76, v77\n"
    "v_cvt_pk_bf16_f32 v239, v78, v79\n"
    "ds_write_b128 v243, v[232:235] offset:10240\n"
    "ds_write_b128 v243, v[236:239] offset:11264\n"
    "s_add_u32 m0, s61, 50048\n"
    "s_nop 0\n"
    "buffer_load_dwordx4 v246, s[40:43], s66 offen offset:128 nt lds\n"
    "buffer_load_dwordx4 v246, s[40:43], s66 offen offset:2176 nt lds\n"
    "v_mfma_f32_32x32x16_bf16 v[64:79], v[196:199], a[128:131], 0\n"
    "s_waitcnt vmcnt(44)\n"
    "s_barrier\n"
    "ds_read_b64_tr_b16 v[216:217], v240 offset:5120\n"
    "v_pk_mul_f32 v[80:81], v[80:81], s[72:73]\n"
    "v_pk_mul_f32 v[82:83], v[82:83], s[72:73]\n"
    "v_pk_mul_f32 v[84:85], v[84:85], s[72:73]\n"
    "v_pk_mul_f32 v[86:87], v[86:87], s[72:73]\n"
    "v_pk_mul_f32 v[88:89], v[88:89], s[72:73]\n"
    "v_pk_mul_f32 v[90:91], v[90:91], s[72:73]\n"
    "v_pk_mul_f32 v[92:93], v[92:93], s[72:73]\n"
    "v_pk_mul_f32 v[94:95], v[94:95], s[72:73]\n"
    "v_cvt_pk_bf16_f32 v232, v80, v81\n"
    "v_cvt_pk_bf16_f32 v233, v82, v83\n"
    "v_cvt_pk_bf16_f32 v234, v84, v85\n"
    "v_cvt_pk_bf16_f32 v235, v86, v87\n"
    "v_cvt_pk_bf16_f32 v236, v88, v89\n"
    "v_cvt_pk_bf16_f32 v237, v90, v91\n"
    "v_cvt_pk_bf16_f32 v238, v92, v93\n"
    "v_cvt_pk_bf16_f32 v239, v94, v95\n"
    "ds_write_b128 v243, v[232:235] offset:18432\n"
    "ds_write_b128 v243, v[236:239] offset:19456\n"
    "s_mov_b32 m0, 65536\n"
    "s_add_u32 s66, s65, 0\n"
    "buffer_load_dwordx4 v247, s[48:51], s66 offen lds\n"
    "s_mov_b32 m0, 66432\n"
    "s_nop 0\n"
    "buffer_load_dwordx4 v247, s[48:51], s66 offen offset:128 lds\n"
    "s_mov_b32 m0, 67328\n"
    "s_nop 0\n"
    "buffer_load_dwordx4 v247, s[48:51], s66 offen offset:256 lds\n"
    "s_mov_b32 m0, 68224\n"
    "s_nop 0\n"
    "buffer_load_dwordx4 v247, s[48:51], s66 offen offset:384 lds\n"
    "s_mov_b32 m0, 69120\n"
    "s_nop 0\n"
    "buffer_load_dwordx4 v247, s[48:51], s66 offen offset:512 lds\n"
    "v_mfma_f32_32x32x16_bf16 v[80:95], v[196:199], v[224:227], 0\n"
    "s_waitcnt lgkmcnt(14)\n"
    "ds_read_b64_tr_b16 v[218:219], v240 offset:5376\n"
    "v_pk_mul_f32 v[96:97], v[96:97], s[72:73]\n"
    "v_pk_mul_f32 v[98:99], v[98:99], s[72:73]\n"
    "v_pk_mul_f32 v[100:101], v[100:101], s[72:73]\n"
    "v_pk_mul_f32 v[102:103], v[102:103], s[72:73]\n"
    "v_pk_mul_f32 v[104:105], v[104:105], s[72:73]\n"
    "v_pk_mul_f32 v[106:107], v[106:107], s[72:73]\n"
    "v_pk_mul_f32 v[108:109], v[108:109], s[72:73]\n"
    "v_pk_mul_f32 v[110:111], v[110:111], s[72:73]\n"
    "v_cvt_pk_bf16_f32 v232, v96, v97\n"
    "v_cvt_pk_bf16_f32 v233, v98, v99\n"
    "v_cvt_pk_bf16_f32 v234, v100, v101\n"
    "v_cvt_pk_bf16_f32 v235, v102, v103\n"
    "v_cvt_pk_bf16_f32 v236, v104, v105\n"
    "v_cvt_pk_bf16_f32 v237, v106, v107\n"
    "v_cvt_pk_bf16_f32 v238, v108, v109\n"
    "v_cvt_pk_bf16_f32 v239, v110, v111\n"
    "s_waitcnt lgkmcnt(14)\n"
    "ds_write_b128 v243, v[232:235] offset:4096\n"
    "s_waitcnt lgkmcnt(14)\n"
    "ds_write_b128 v243, v[236:239] offset:5120\n"
    "s_mov_b32 m0, 70016\n"
    "s_nop 0\n"
    "buffer_load_dwordx4 v247, s[48:51], s66 offen offset:640 lds\n"
    "s_mov_b32 m0, 70912\n"
    "s_nop 0\n"
    "buffer_load_dwordx4 v247, s[48:51], s66 offen offset:768 lds\n"
    "s_mov_b32 m0, 71808\n"
    "s_nop 0\n"
    "buffer_load_dwordx4 v247, s[48:51], s66 offen offset:896 lds\n"
    "s_mov_b32 m0, 73728\n"
    "s_add_u32 s66, s65, 8192\n"
    "buffer_load_dwordx4 v247, s[48:51], s66 offen lds\n"
    "s_mov_b32 m0, 74624\n"
    "s_nop 0\n"
    "buffer_load_dwordx4 v247, s[48:51], s66 offen offset:128 lds\n"
    "v_mfma_f32_32x32x16_bf16 v[96:111], v[200:203], a[0:3], 0\n"
    "s_waitcnt lgkmcnt(14)\n"
    "ds_read_b64_tr_b16 v[220:221], v240 offset:5632\n"
    "v_pk_mul_f32 v[112:113], v[112:113], s[72:73]\n"
    "v_pk_mul_f32 v[114:115], v[114:115], s[72:73]\n"
    "v_pk_mul_f32 v[116:117], v[116:117], s[72:73]\n"
    "v_pk_mul_f32 v[118:119], v[118:119], s[72:73]\n"
    "v_pk_mul_f32 v[120:121], v[120:121], s[72:73]\n"
    "v_pk_mul_f32 v[122:123], v[122:123], s[72:73]\n"
    "v_pk_mul_f32 v[124:125], v[124:125], s[72:73]\n"
    "v_pk_mul_f32 v[126:127], v[126:127], s[72:73]\n"
    "v_cvt_pk_bf16_f32 v232, v112, v113\n"
    "v_cvt_pk_bf16_f32 v233, v114, v115\n"
    "v_cvt_pk_bf16_f32 v234, v116, v117\n"
    "v_cvt_pk_bf16_f32 v235, v118, v119\n"
    "v_cvt_pk_bf16_f32 v236, v120, v121\n"
    "v_cvt_pk_bf16_f32 v237, v122, v123\n"
    "v_cvt_pk_bf16_f32 v238, v124, v125\n"
    "v_cvt_pk_bf16_f32 v239, v126, v127\n"
    "s_waitcnt lgkmcnt(14)\n"
    "ds_write_b128 v243, v[232:235] offset:12288\n"
    "s_waitcnt lgkmcnt(14)\n"
    "ds_write_b128 v243, v[236:239] offset:13312\n"
    "s_mov_b32 m0, 75520\n"
    "s_nop 0\n"
    "buffer_load_dwordx4 v247, s[48:51], s66 offen offset:256 lds\n"
    "s_mov_b32 m0, 76416\n"
    "s_nop 0\n"
    "buffer_load_dwordx4 v247, s[48:51], s66 offen offset:384 lds\n"
    "s_mov_b32 m0, 77312\n"
    "s_nop 0\n"
    "buffer_load_dwordx4 v247, s[48:51], s66 offen offset:512 lds\n"
    "s_mov_b32 m0, 78208\n"
    "s_nop 0\n"
    "buffer_load_dwordx4 v247, s[48:51], s66 offen offset:640 lds\n"
    "s_mov_b32 m0, 79104\n"
    "s_nop 0\n"
    "buffer_load_dwordx4 v247, s[48:51], s66 offen offset:768 lds\n"
    "v_mfma_f32_32x32x16_bf16 v[112:127], v[200:203], a[128:131], 0\n"
    "s_waitcnt lgkmcnt(14)\n"
    "ds_read_b64_tr_b16 v[222:223], v240 offset:5888\n"
    "v_pk_mul_f32 v[128:129], v[128:129], s[72:73]\n"
    "v_pk_mul_f32 v[130:131], v[130:131], s[72:73]\n"
    "v_pk_mul_f32 v[132:133], v[132:133], s[72:73]\n"
    "v_pk_mul_f32 v[134:135], v[134:135], s[72:73]\n"
    "v_pk_mul_f32 v[136:137], v[136:137], s[72:73]\n"
    "v_pk_mul_f32 v[138:139], v[138:139], s[72:73]\n"
    "v_pk_mul_f32 v[140:141], v[140:141], s[72:73]\n"
    "v_pk_mul_f32 v[142:143], v[142:143], s[72:73]\n"
    "v_cvt_pk_bf16_f32 v232, v128, v129\n"
    "v_cvt_pk_bf16_f32 v233, v130, v131\n"
    "v_cvt_pk_bf16_f32 v234, v132, v133\n"
    "v_cvt_pk_bf16_f32 v235, v134, v135\n"
    "v_cvt_pk_bf16_f32 v236, v136, v137\n"
    "v_cvt_pk_bf16_f32 v237, v138, v139\n"
    "v_cvt_pk_bf16_f32 v238, v140, v141\n"
    "v_cvt_pk_bf16_f32 v239, v142, v143\n"
    "s_waitcnt lgkmcnt(14)\n"
    "ds_write_b128 v243, v[232:235] offset:20480\n"
    "s_waitcnt lgkmcnt(14)\n"
    "ds_write_b128 v243, v[236:239] offset:21504\n"
    "s_mov_b32 m0, 80000\n"
    "s_nop 0\n"
    "buffer_load_dwordx4 v247, s[48:51], s66 offen offset:896 lds\n"
    "v_mfma_f32_32x32x16_bf16 v[128:143], v[200:203], v[224:227], 0\n"
    "s_waitcnt vmcnt(34)\n"
    "s_barrier\n"
    "s_waitcnt lgkmcnt(14)\n"
    "ds_read_b128 v[228:231], v242 offset:0\n"
    "v_pk_mul_f32 v[144:145], v[144:145], s[72:73]\n"
    "v_pk_mul_f32 v[146:147], v[146:147], s[72:73]\n"
    "v_pk_mul_f32 v[148:149], v[148:149], s[72:73]\n"
    "v_pk_mul_f32 v[150:151], v[150:151], s[72:73]\n"
    "v_pk_mul_f32 v[152:153], v[152:153], s[72:73]\n"
    "v_pk_mul_f32 v[154:155], v[154:155], s[72:73]\n"
    "v_pk_mul_f32 v[156:157], v[156:157], s[72:73]\n"
    "v_pk_mul_f32 v[158:159], v[158:159], s[72:73]\n"
    "v_cvt_pk_bf16_f32 v232, v144, v145\n"
    "v_cvt_pk_bf16_f32 v233, v146, v147\n"
    "v_cvt_pk_bf16_f32 v234, v148, v149\n"
    "v_cvt_pk_bf16_f32 v235, v150, v151\n"
    "v_cvt_pk_bf16_f32 v236, v152, v153\n"
    "v_cvt_pk_bf16_f32 v237, v154, v155\n"
    "v_cvt_pk_bf16_f32 v238, v156, v157\n"
    "v_cvt_pk_bf16_f32 v239, v158, v159\n"
    "s_waitcnt lgkmcnt(14)\n"
    "ds_write_b128 v243, v[232:235] offset:6144\n"
    "s_waitcnt lgkmcnt(14)\n"
    "ds_write_b128 v243, v[236:239] offset:7168\n"
    "v_mfma_f32_32x32x16_bf16 v[144:159], v[204:207], a[0:3], 0\n"
    "v_pk_mul_f32 v[160:161], v[160:161], s[72:73]\n"
    "v_pk_mul_f32 v[162:163], v[162:163], s[72:73]\n"
    "v_pk_mul_f32 v[164:165], v[164:165], s[72:73]\n"
    "v_pk_mul_f32 v[166:167], v[166:167], s[72:73]\n"
    "v_pk_mul_f32 v[168:169], v[168:169], s[72:73]\n"
    "v_pk_mul_f32 v[170:171], v[170:171], s[72:73]\n"
    "v_pk_mul_f32 v[172:173], v[172:173], s[72:73]\n"
    "v_pk_mul_f32 v[174:175], v[174:175], s[72:73]\n"
    "v_cvt_pk_bf16_f32 v232, v160, v161\n"
    "v_cvt_pk_bf16_f32 v233, v162, v163\n"
    "v_cvt_pk_bf16_f32 v234, v164, v165\n"
    "v_cvt_pk_bf16_f32 v235, v166, v167\n"
    "v_cvt_pk_bf16_f32 v236, v168, v169\n"
    "v_cvt_pk_bf16_f32 v237, v170, v171\n"
    "v_cvt_pk_bf16_f32 v238, v172, v173\n"
    "v_cvt_pk_bf16_f32 v239, v174, v175\n"
    "s_waitcnt lgkmcnt(14)\n"
    "ds_write_b128 v243, v[232:235] offset:14336\n"
    "s_waitcnt lgkmcnt(14)\n"
    "ds_write_b128 v243, v[236:239] offset:15360\n"
    "s_add_u32 m0, s61, 16384\n"
    "s_add_u32 s66, s63, 16384\n"
    "buffer_load_dwordx4 v244, s[36:39], s66 offen nt lds\n"
    "buffer_load_dwordx4 v245, s[36:39], s66 offen offset:2048 nt lds\n"
    "v_mfma_f32_32x32x16_bf16 v[160:175], v[204:207], a[128:131], 0\n"
    "v_pk_mul_f32 v[176:177], v[176:177], s[72:73]\n"
    "v_pk_mul_f32 v[178:179], v[178:179], s[72:73]\n"
    "v_pk_mul_f32 v[180:181], v[180:181], s[72:73]\n"
    "v_pk_mul_f32 v[182:183], v[182:183], s[72:73]\n"
    "v_pk_mul_f32 v[184:185], v[184:185], s[72:73]\n"
    "v_pk_mul_f32 v[186:187], v[186:187], s[72:73]\n"
    "v_pk_mul_f32 v[188:189], v[188:189], s[72:73]\n"
    "v_pk_mul_f32 v[190:191], v[190:191], s[72:73]\n"
    "v_cvt_pk_bf16_f32 v232, v176, v177\n"
    "v_cvt_pk_bf16_f32 v233, v178, v179\n"
    "v_cvt_pk_bf16_f32 v234, v180, v181\n"
    "v_cvt_pk_bf16_f32 v235, v182, v183\n"
    "v_cvt_pk_bf16_f32 v236, v184, v185\n"
    "v_cvt_pk_bf16_f32 v237, v186, v187\n"
    "v_cvt_pk_bf16_f32 v238, v188, v189\n"
    "v_cvt_pk_bf16_f32 v239, v190, v191\n"
    "s_waitcnt lgkmcnt(14)\n"
    "ds_write_b128 v243, v[232:235] offset:22528\n"
    "s_waitcnt lgkmcnt(14)\n"
    "ds_write_b128 v243, v[236:239] offset:23552\n"
    "v_mfma_f32_32x32x16_bf16 v[176:191], v[204:207], v[224:227], 0\n"
    "s_add_u32 m0, s61, 17280\n"
    "s_nop 0\n"
    "buffer_load_dwordx4 v244, s[36:39], s66 offen offset:128 nt lds\n"
    "buffer_load_dwordx4 v245, s[36:39], s66 offen offset:2176 nt lds\n"
    "s_waitcnt lgkmcnt(0)\n"
    "s_waitcnt vmcnt(24)\n"
    "s_barrier\n"
    "s_add_u32 m0, s61, 32768\n"
    "s_add_u32 s66, s63, 0\n"
    "buffer_load_dwordx4 v246, s[40:43], s66 offen nt lds\n"
    "buffer_load_dwordx4 v246, s[40:43], s66 offen offset:2048 nt lds\n"
    "s_add_u32 m0, s61, 33664\n"
    "s_nop 0\n"
    "buffer_load_dwordx4 v246, s[40:43], s66 offen offset:128 nt lds\n"
    "buffer_load_dwordx4 v246, s[40:43], s66 offen offset:2176 nt lds\n"
    "s_mov_b32 s64, s65\n"
    "s_mov_b32 s62, s63\n"
    "s_add_u32 s63, s63, 131072\n"
    "s_add_u32 s65, s65, 131072\n"
    "s_sub_u32 s68, s68, 1\n"
    "s_cmp_eq_u32 s68, 1\n"
    "s_cselect_b32 s63, 268435456, s63\n"
    "s_cselect_b32 s65, 268435456, s65\n"
    "s_mov_b32 s75, s76\n"
    "s_add_u32 s76, s76, 256\n"
    "s_cmp_eq_u32 s68, 2\n"
    "s_cselect_b32 s76, 268435456, s76\n"
    "s_cmp_lg_u32 s68, 1\n"
    "s_cbranch_scc1 L_w3_loop\n"
    "s_waitcnt vmcnt(24)\n"
    "s_barrier\n"
    "s_mov_b32 m0, 81920\n"
    "s_add_u32 s66, s64, 16384\n"
    "buffer_load_dwordx4 v247, s[48:51], s66 offen lds\n"
    "s_mov_b32 m0, 82816\n"
    "s_nop 0\n"
    "buffer_load_dwordx4 v247, s[48:51], s66 offen offset:128 lds\n"
    "s_mov_b32 m0, 83712\n"
    "s_nop 0\n"
    "buffer_load_dwordx4 v247, s[48:51], s66 offen offset:256 lds\n"
    "s_mov_b32 m0, 84608\n"
    "s_nop 0\n"
    "buffer_load_dwordx4 v247, s[48:51], s66 offen offset:384 lds\n"
    "s_mov_b32 m0, 85504\n"
    "s_nop 0\n"
    "buffer_load_dwordx4 v247, s[48:51], s66 offen offset:512 lds\n"
    "s_mov_b32 m0, 86400\n"
    "s_nop 0\n"
    "buffer_load_dwordx4 v247, s[48:51], s66 offen offset:640 lds\n"
    "s_mov_b32 m0, 87296\n"
    "s_nop 0\n"
    "buffer_load_dwordx4 v247, s[48:51], s66 offen offset:768 lds\n"
    "s_mov_b32 m0, 88192\n"
    "s_nop 0\n"
    "buffer_load_dwordx4 v247, s[48:51], s66 offen offset:896 lds\n"
    "s_mov_b32 m0, 90112\n"
    "s_add_u32 s66, s64, 24576\n"
    "buffer_load_dwordx4 v247, s[48:51], s66 offen lds\n"
    "s_mov_b32 m0, 91008\n"
    "s_nop 0\n"
    "buffer_load_dwordx4 v247, s[48:51], s66 offen offset:128 lds\n"
    "s_mov_b32 m0, 91904\n"
    "s_nop 0\n"
    "buffer_load_dwordx4 v247, s[48:51], s66 offen offset:256 lds\n"
    "s_mov_b32 m0, 92800\n"
    "s_nop 0\n"
    "buffer_load_dwordx4 v247, s[48:51], s66 offen offset:384 lds\n"
    "s_mov_b32 m0, 93696\n"
    "s_nop 0\n"
    "buffer_load_dwordx4 v247, s[48:51], s66 offen offset:512 lds\n"
    "s_mov_b32 m0, 94592\n"
    "s_nop 0\n"
    "buffer_load_dwordx4 v247, s[48:51], s66 offen offset:640 lds\n"
    "s_mov_b32 m0, 95488\n"
    "s_nop 0\n"
    "buffer_load_dwordx4 v247, s[48:51], s66 offen offset:768 lds\n"
    "s_mov_b32 m0, 96384\n"
    "s_nop 0\n"
    "buffer_load_dwordx4 v247, s[48:51], s66 offen offset:896 lds\n"
    "s_waitcnt vmcnt(24)\n"
    "s_barrier\n"
    "s_add_u32 s85, s76, s83\n"
    "buffer_load_dword v253, v251, s[44:47], s85 offen\n"
    "s_add_u32 s85, s81, s84\n"
    "buffer_load_dword v253, v252, s[48:51], s85 offen\n"
    "s_add_u32 m0, s61, 0\n"
    "s_add_u32 s66, s62, 32768\n"
    "buffer_load_dwordx4 v244, s[36:39], s66 offen nt lds\n"
    "buffer_load_dwordx4 v245, s[36:39], s66 offen offset:2048 nt lds\n"
    "s_add_u32 m0, s61, 896\n"
    "s_nop 0\n"
    "buffer_load_dwordx4 v244, s[36:39], s66 offen offset:128 nt lds\n"
    "buffer_load_dwordx4 v245, s[36:39], s66 offen offset:2176 nt lds\n"
    "s_waitcnt vmcnt(26)\n"
    "s_barrier\n"
    "s_add_u32 m0, s61, 49152\n"
    "s_add_u32 s66, s62, 16384\n"
    "buffer_load_dwordx4 v246, s[40:43], s66 offen nt lds\n"
    "buffer_load_dwordx4 v246, s[40:43], s66 offen offset:2048 nt lds\n"
    "s_add_u32 m0, s61, 50048\n"
    "s_nop 0\n"
    "buffer_load_dwordx4 v246, s[40:43], s66 offen offset:128 nt lds\n"
    "buffer_load_dwordx4 v246, s[40:43], s66 offen offset:2176 nt lds\n"
    "s_waitcnt vmcnt(26)\n"
    "s_barrier\n"
    "s_mov_b32 m0, 65536\n"
    "s_add_u32 s66, s64, 32768\n"
    "buffer_load_dwordx4 v247, s[48:51], s66 offen lds\n"
    "s_mov_b32 m0, 66432\n"
    "s_nop 0\n"
    "buffer_load_dwordx4 v247, s[48:51], s66 offen offset:128 lds\n"
    "s_mov_b32 m0, 67328\n"
    "s_nop 0\n"
    "buffer_load_dwordx4 v247, s[48:51], s66 offen offset:256 lds\n"
    "s_mov_b32 m0, 68224\n"
    "s_nop 0\n"
    "buffer_load_dwordx4 v247, s[48:51], s66 offen offset:384 lds\n"
    "s_mov_b32 m0, 69120\n"
    "s_nop 0\n"
    "buffer_load_dwordx4 v247, s[48:51], s66 offen offset:512 lds\n"
    "s_mov_b32 m0, 70016\n"
    "s_nop 0\n"
    "buffer_load_dwordx4 v247, s[48:51], s66 offen offset:640 lds\n"
    "s_mov_b32 m0, 70912\n"
    "s_nop 0\n"
    "buffer_load_dwordx4 v247, s[48:51], s66 offen offset:768 lds\n"
    "s_mov_b32 m0, 71808\n"
    "s_nop 0\n"
    "buffer_load_dwordx4 v247, s[48:51], s66 offen offset:896 lds\n"
    "s_mov_b32 m0, 73728\n"
    "s_add_u32 s66, s64, 40960\n"
    "buffer_load_dwordx4 v247, s[48:51], s66 offen lds\n"
    "s_mov_b32 m0, 74624\n"
    "s_nop 0\n"
    "buffer_load_dwordx4 v247, s[48:51], s66 offen offset:128 lds\n"
    "s_mov_b32 m0, 75520\n"
    "s_nop 0\n"
    "buffer_load_dwordx4 v247, s[48:51], s66 offen offset:256 lds\n"
    "s_mov_b32 m0, 76416\n"
    "s_nop 0\n"
    "buffer_load_dwordx4 v247, s[48:51], s66 offen offset:384 lds\n"
    "s_mov_b32 m0, 77312\n"
    "s_nop 0\n"
    "buffer_load_dwordx4 v247, s[48:51], s66 offen offset:512 lds\n"
    "s_mov_b32 m0, 78208\n"
    "s_nop 0\n"
    "buffer_load_dwordx4 v247, s[48:51], s66 offen offset:640 lds\n"
    "s_mov_b32 m0, 79104\n"
    "s_nop 0\n"
    "buffer_load_dwordx4 v247, s[48:51], s66 offen offset:768 lds\n"
    "s_mov_b32 m0, 80000\n"
    "s_nop 0\n"
    "buffer_load_dwordx4 v247, s[48:51], s66 offen offset:896 lds\n"
    "s_waitcnt vmcnt(26)\n"
    "s_barrier\n"
    "s_add_u32 m0, s61, 16384\n"
    "s_add_u32 s66, s62, 49152\n"
    "buffer_load_dwordx4 v244, s[36:39], s66 offen nt lds\n"
    "buffer_load_dwordx4 v245, s[36:39], s66 offen offset:2048 nt lds\n"
    "s_add_u32 m0, s61, 17280\n"
    "s_nop 0\n"
    "buffer_load_dwordx4 v244, s[36:39], s66 offen offset:128 nt lds\n"
    "buffer_load_dwordx4 v245, s[36:39], s66 offen offset:2176 nt lds\n"
    "s_waitcnt vmcnt(24)\n"
    "s_barrier\n"
    "s_add_u32 m0, s61, 32768\n"
    "s_add_u32 s66, s62, 32768\n"
    "buffer_load_dwordx4 v246, s[40:43], s66 offen nt lds\n"
    "buffer_load_dwordx4 v246, s[40:43], s66 offen offset:2048 nt lds\n"
    "s_add_u32 m0, s61, 33664\n"
    "s_nop 0\n"
    "buffer_load_dwordx4 v246, s[40:43], s66 offen offset:128 nt lds\n"
    "buffer_load_dwordx4 v246, s[40:43], s66 offen offset:2176 nt lds\n"
    "s_waitcnt vmcnt(24)\n"
    "s_barrier\n"
    "s_mov_b32 m0, 81920\n"
    "s_add_u32 s66, s64, 49152\n"
    "buffer_load_dwordx4 v247, s[48:51], s66 offen lds\n"
    "s_mov_b32 m0, 82816\n"
    "s_nop 0\n"
    "buffer_load_dwordx4 v247, s[48:51], s66 offen offset:128 lds\n"
    "s_mov_b32 m0, 83712\n"
    "s_nop 0\n"
    "buffer_load_dwordx4 v247, s[48:51], s66 offen offset:256 lds\n"
    "s_mov_b32 m0, 84608\n"
    "s_nop 0\n"
    "buffer_load_dwordx4 v247, s[48:51], s66 offen offset:384 lds\n"
    "s_mov_b32 m0, 85504\n"
    "s_nop 0\n"
    "buffer_load_dwordx4 v247, s[48:51], s66 offen offset:512 lds\n"
    "s_mov_b32 m0, 86400\n"
    "s_nop 0\n"
    "buffer_load_dwordx4 v247, s[48:51], s66 offen offset:640 lds\n"
    "s_mov_b32 m0, 87296\n"
    "s_nop 0\n"
    "buffer_load_dwordx4 v247, s[48:51], s66 offen offset:768 lds\n"
    "s_mov_b32 m0, 88192\n"
    "s_nop 0\n"
    "buffer_load_dwordx4 v247, s[48:51], s66 offen offset:896 lds\n"
    "s_mov_b32 m0, 90112\n"
    "s_add_u32 s66, s64, 57344\n"
    "buffer_load_dwordx4 v247, s[48:51], s66 offen lds\n"
    "s_mov_b32 m0, 91008\n"
    "s_nop 0\n"
    "buffer_load_dwordx4 v247, s[48:51], s66 offen offset:128 lds\n"
    "s_mov_b32 m0, 91904\n"
    "s_nop 0\n"
    "buffer_load_dwordx4 v247, s[48:51], s66 offen offset:256 lds\n"
    "s_mov_b32 m0, 92800\n"
    "s_nop 0\n"
    "buffer_load_dwordx4 v247, s[48:51], s66 offen offset:384 lds\n"
    "s_mov_b32 m0, 93696\n"
    "s_nop 0\n"
    "buffer_load_dwordx4 v247, s[48:51], s66 offen offset:512 lds\n"
    "s_mov_b32 m0, 94592\n"
    "s_nop 0\n"
    "buffer_load_dwordx4 v247, s[48:51], s66 offen offset:640 lds\n"
    "s_mov_b32 m0, 95488\n"
    "s_nop 0\n"
    "buffer_load_dwordx4 v247, s[48:51], s66 offen offset:768 lds\n"
    "s_mov_b32 m0, 96384\n"
    "s_nop 0\n"
    "buffer_load_dwordx4 v247, s[48:51], s66 offen offset:896 lds\n"
    "s_waitcnt vmcnt(24)\n"
    "s_barrier\n"
    "s_add_u32 m0, s61, 0\n"
    "s_add_u32 s66, s62, 65536\n"
    "buffer_load_dwordx4 v244, s[36:39], s66 offen nt lds\n"
    "buffer_load_dwordx4 v245, s[36:39], s66 offen offset:2048 nt lds\n"
    "s_add_u32 m0, s61, 896\n"
    "s_nop 0\n"
    "buffer_load_dwordx4 v244, s[36:39], s66 offen offset:128 nt lds\n"
    "buffer_load_dwordx4 v245, s[36:39], s66 offen offset:2176 nt lds\n"
    "s_waitcnt vmcnt(24)\n"
    "s_barrier\n"
    "s_add_u32 m0, s61, 49152\n"
    "s_add_u32 s66, s62, 49152\n"
    "buffer_load_dwordx4 v246, s[40:43], s66 offen nt lds\n"
    "buffer_load_dwordx4 v246, s[40:43], s66 offen offset:2048 nt lds\n"
    "s_add_u32 m0, s61, 50048\n"
    "s_nop 0\n"
    "buffer_load_dwordx4 v246, s[40:43], s66 offen offset:128 nt lds\n"
    "buffer_load_dwordx4 v246, s[40:43], s66 offen offset:2176 nt lds\n"
    "s_waitcnt vmcnt(24)\n"
    "s_barrier\n"
    "s_mov_b32 m0, 65536\n"
    "s_add_u32 s66, s64, 65536\n"
    "buffer_load_dwordx4 v247, s[48:51], s66 offen lds\n"
    "s_mov_b32 m0, 66432\n"
    "s_nop 0\n"
    "buffer_load_dwordx4 v247, s[48:51], s66 offen offset:128 lds\n"
    "s_mov_b32 m0, 67328\n"
    "s_nop 0\n"
    "buffer_load_dwordx4 v247, s[48:51], s66 offen offset:256 lds\n"
    "s_mov_b32 m0, 68224\n"
    "s_nop 0\n"
    "buffer_load_dwordx4 v247, s[48:51], s66 offen offset:384 lds\n"
    "s_mov_b32 m0, 69120\n"
    "s_nop 0\n"
    "buffer_load_dwordx4 v247, s[48:51], s66 offen offset:512 lds\n"
    "s_mov_b32 m0, 70016\n"
    "s_nop 0\n"
    "buffer_load_dwordx4 v247, s[48:51], s66 offen offset:640 lds\n"
    "s_mov_b32 m0, 70912\n"
    "s_nop 0\n"
    "buffer_load_dwordx4 v247, s[48:51], s66 offen offset:768 lds\n"
    "s_mov_b32 m0, 71808\n"
    "s_nop 0\n"
    "buffer_load_dwordx4 v247, s[48:51], s66 offen offset:896 lds\n"
    "s_mov_b32 m0, 73728\n"
    "s_add_u32 s66, s64, 73728\n"
    "buffer_load_dwordx4 v247, s[48:51], s66 offen lds\n"
    "s_mov_b32 m0, 74624\n"
    "s_nop 0\n"
    "buffer_load_dwordx4 v247, s[48:51], s66 offen offset:128 lds\n"
    "s_mov_b32 m0, 75520\n"
    "s_nop 0\n"
    "buffer_load_dwordx4 v247, s[48:51], s66 offen offset:256 lds\n"
    "s_mov_b32 m0, 76416\n"
    "s_nop 0\n"
    "buffer_load_dwordx4 v247, s[48:51], s66 offen offset:384 lds\n"
    "s_mov_b32 m0, 77312\n"
    "s_nop 0\n"
    "buffer_load_dwordx4 v247, s[48:51], s66 offen offset:512 lds\n"
    "s_mov_b32 m0, 78208\n"
    "s_nop 0\n"
    "buffer_load_dwordx4 v247, s[48:51], s66 offen offset:640 lds\n"
    "s_mov_b32 m0, 79104\n"
    "s_nop 0\n"
    "buffer_load_dwordx4 v247, s[48:51], s66 offen offset:768 lds\n"
    "s_mov_b32 m0, 80000\n"
    "s_nop 0\n"
    "buffer_load_dwordx4 v247, s[48:51], s66 offen offset:896 lds\n"
    "s_mov_b32 m0, 98304\n"
    "s_add_u32 s67, s81, 0\n"
    "buffer_load_dwordx4 v247, s[48:51], s67 offen lds\n"
    "s_mov_b32 m0, 99328\n"
    "s_add_u32 s67, s81, 8192\n"
    "buffer_load_dwordx4 v247, s[48:51], s67 offen lds\n"
    "s_mov_b32 m0, 100352\n"
    "s_add_u32 s67, s81, 16384\n"
    "buffer_load_dwordx4 v247, s[48:51], s67 offen lds\n"
    "s_mov_b32 m0, 101376\n"
    "s_add_u32 s67, s81, 24576\n"
    "buffer_load_dwordx4 v247, s[48:51], s67 offen lds\n"
    "s_mov_b32 m0, 102400\n"
    "s_add_u32 s67, s81, 32768\n"
    "buffer_load_dwordx4 v247, s[48:51], s67 offen lds\n"
    "s_mov_b32 m0, 103424\n"
    "s_add_u32 s67, s81, 40960\n"
    "buffer_load_dwordx4 v247, s[48:51], s67 offen lds\n"
    "s_mov_b32 m0, 104448\n"
    "s_add_u32 s67, s81, 49152\n"
    "buffer_load_dwordx4 v247, s[48:51], s67 offen lds\n"
    "s_mov_b32 m0, 105472\n"
    "s_add_u32 s67, s81, 57344\n"
    "buffer_load_dwordx4 v247, s[48:51], s67 offen lds\n"
    "s_mov_b32 m0, 106496\n"
    "s_add_u32 s67, s81, 65536\n"
    "buffer_load_dwordx4 v247, s[48:51], s67 offen lds\n"
    "s_mov_b32 m0, 107520\n"
    "s_add_u32 s67, s81, 73728\n"
    "buffer_load_dwordx4 v247, s[48:51], s67 offen lds\n"
    "s_mov_b32 m0, 108544\n"
    "s_add_u32 s67, s81, 81920\n"
    "buffer_load_dwordx4 v247, s[48:51], s67 offen lds\n"
    "s_mov_b32 m0, 109568\n"
    "s_add_u32 s67, s81, 90112\n"
    "buffer_load_dwordx4 v247, s[48:51], s67 offen lds\n"
    "s_mov_b32 m0, 110592\n"
    "s_add_u32 s67, s81, 98304\n"
    "buffer_load_dwordx4 v247, s[48:51], s67 offen lds\n"
    "s_mov_b32 m0, 111616\n"
    "s_add_u32 s67, s81, 106496\n"
    "buffer_load_dwordx4 v247, s[48:51], s67 offen lds\n"
    "s_mov_b32 m0, 112640\n"
    "s_add_u32 s67, s81, 114688\n"
    "buffer_load_dwordx4 v247, s[48:51], s67 offen lds\n"
    "s_mov_b32 m0, 113664\n"
    "s_add_u32 s67, s81, 122880\n"
    "buffer_load_dwordx4 v247, s[48:51], s67 offen lds\n"
    "s_waitcnt vmcnt(40)\n"
    "s_barrier\n"
    "s_add_u32 m0, s61, 16384\n"
    "s_add_u32 s66, s62, 81920\n"
    "buffer_load_dwordx4 v244, s[36:39], s66 offen nt lds\n"
    "buffer_load_dwordx4 v245, s[36:39], s66 offen offset:2048 nt lds\n"
    "s_add_u32 m0, s61, 17280\n"
    "s_nop 0\n"
    "buffer_load_dwordx4 v244, s[36:39], s66 offen offset:128 nt lds\n"
    "buffer_load_dwordx4 v245, s[36:39], s66 offen offset:2176 nt lds\n"
    "s_waitcnt vmcnt(40)\n"
    "s_barrier\n"
    "s_add_u32 m0, s61, 32768\n"
    "s_add_u32 s66, s62, 65536\n"
    "buffer_load_dwordx4 v246, s[40:43], s66 offen nt lds\n"
    "buffer_load_dwordx4 v246, s[40:43], s66 offen offset:2048 nt lds\n"
    "s_add_u32 m0, s61, 33664\n"
    "s_nop 0\n"
    "buffer_load_dwordx4 v246, s[40:43], s66 offen offset:128 nt lds\n"
    "buffer_load_dwordx4 v246, s[40:43], s66 offen offset:2176 nt lds\n"
    "s_waitcnt vmcnt(40)\n"
    "s_barrier\n"
    "s_mov_b32 m0, 81920\n"
    "s_add_u32 s66, s64, 81920\n"
    "buffer_load_dwordx4 v247, s[48:51], s66 offen lds\n"
    "s_mov_b32 m0, 82816\n"
    "s_nop 0\n"
    "buffer_load_dwordx4 v247, s[48:51], s66 offen offset:128 lds\n"
    "s_mov_b32 m0, 83712\n"
    "s_nop 0\n"
    "buffer_load_dwordx4 v247, s[48:51], s66 offen offset:256 lds\n"
    "s_mov_b32 m0, 84608\n"
    "s_nop 0\n"
    "buffer_load_dwordx4 v247, s[48:51], s66 offen offset:384 lds\n"
    "s_mov_b32 m0, 85504\n"
    "s_nop 0\n"
    "buffer_load_dwordx4 v247, s[48:51], s66 offen offset:512 lds\n"
    "s_mov_b32 m0, 86400\n"
    "s_nop 0\n"
    "buffer_load_dwordx4 v247, s[48:51], s66 offen offset:640 lds\n"
    "s_mov_b32 m0, 87296\n"
    "s_nop 0\n"
    "buffer_load_dwordx4 v247, s[48:51], s66 offen offset:768 lds\n"
    "s_mov_b32 m0, 88192\n"
    "s_nop 0\n"
    "buffer_load_dwordx4 v247, s[48:51], s66 offen offset:896 lds\n"
    "s_mov_b32 m0, 90112\n"
    "s_add_u32 s66, s64, 90112\n"
    "buffer_load_dwordx4 v247, s[48:51], s66 offen lds\n"
    "s_mov_b32 m0, 91008\n"
    "s_nop 0\n"
    "buffer_load_dwordx4 v247, s[48:51], s66 offen offset:128 lds\n"
    "s_mov_b32 m0, 91904\n"
    "s_nop 0\n"
    "buffer_load_dwordx4 v247, s[48:51], s66 offen offset:256 lds\n"
    "s_mov_b32 m0, 92800\n"
    "s_nop 0\n"
    "buffer_load_dwordx4 v247, s[48:51], s66 offen offset:384 lds\n"
    "s_mov_b32 m0, 93696\n"
    "s_nop 0\n"
    "buffer_load_dwordx4 v247, s[48:51], s66 offen offset:512 lds\n"
    "s_mov_b32 m0, 94592\n"
    "s_nop 0\n"
    "buffer_load_dwordx4 v247, s[48:51], s66 offen offset:640 lds\n"
    "s_mov_b32 m0, 95488\n"
    "s_nop 0\n"
    "buffer_load_dwordx4 v247, s[48:51], s66 offen offset:768 lds\n"
    "s_mov_b32 m0, 96384\n"
    "s_nop 0\n"
    "buffer_load_dwordx4 v247, s[48:51], s66 offen offset:896 lds\n"
    "s_mov_b32 m0, 114688\n"
    "s_add_u32 s67, s81, 128\n"
    "buffer_load_dwordx4 v247, s[48:51], s67 offen lds\n"
    "s_mov_b32 m0, 115712\n"
    "s_add_u32 s67, s81, 8320\n"
    "buffer_load_dwordx4 v247, s[48:51], s67 offen lds\n"
    "s_mov_b32 m0, 116736\n"
    "s_add_u32 s67, s81, 16512\n"
    "buffer_load_dwordx4 v247, s[48:51], s67 offen lds\n"
    "s_mov_b32 m0, 117760\n"
    "s_add_u32 s67, s81, 24704\n"
    "buffer_load_dwordx4 v247, s[48:51], s67 offen lds\n"
    "s_mov_b32 m0, 118784\n"
    "s_add_u32 s67, s81, 32896\n"
    "buffer_load_dwordx4 v247, s[48:51], s67 offen lds\n"
    "s_mov_b32 m0, 119808\n"
    "s_add_u32 s67, s81, 41088\n"
    "buffer_load_dwordx4 v247, s[48:51], s67 offen lds\n"
    "s_mov_b32 m0, 120832\n"
    "s_add_u32 s67, s81, 49280\n"
    "buffer_load_dwordx4 v247, s[48:51], s67 offen lds\n"
    "s_mov_b32 m0, 121856\n"
    "s_add_u32 s67, s81, 57472\n"
    "s_waitcnt vmcnt(62)\n"
    "buffer_load_dwordx4 v247, s[48:51], s67 offen lds\n"
    "s_mov_b32 m0, 122880\n"
    "s_add_u32 s67, s81, 65664\n"
    "s_waitcnt vmcnt(62)\n"
    "buffer_load_dwordx4 v247, s[48:51], s67 offen lds\n"
    "s_mov_b32 m0, 123904\n"
    "s_add_u32 s67, s81, 73856\n"
    "s_waitcnt vmcnt(62)\n"
    "buffer_load_dwordx4 v247, s[48:51], s67 offen lds\n"
    "s_mov_b32 m0, 124928\n"
    "s_add_u32 s67, s81, 82048\n"
    "s_waitcnt vmcnt(62)\n"
    "buffer_load_dwordx4 v247, s[48:51], s67 offen lds\n"
    "s_mov_b32 m0, 125952\n"
    "s_add_u32 s67, s81, 90240\n"
    "s_waitcnt vmcnt(62)\n"
    "buffer_load_dwordx4 v247, s[48:51], s67 offen lds\n"
    "s_mov_b32 m0, 126976\n"
    "s_add_u32 s67, s81, 98432\n"
    "s_waitcnt vmcnt(62)\n"
    "buffer_load_dwordx4 v247, s[48:51], s67 offen lds\n"
    "s_mov_b32 m0, 128000\n"
    "s_add_u32 s67, s81, 106624\n"
    "s_waitcnt vmcnt(62)\n"
    "buffer_load_dwordx4 v247, s[48:51], s67 offen lds\n"
    "s_mov_b32 m0, 129024\n"
    "s_add_u32 s67, s81, 114816\n"
    "s_waitcnt vmcnt(62)\n"
    "buffer_load_dwordx4 v247, s[48:51], s67 offen lds\n"
    "s_mov_b32 m0, 130048\n"
    "s_add_u32 s67, s81, 123008\n"
    "s_waitcnt vmcnt(62)\n"
    "buffer_load_dwordx4 v247, s[48:51], s67 offen lds\n"
    "s_waitcnt vmcnt(56)\n"
    "s_barrier\n"
    "s_add_u32 m0, s61, 0\n"
    "s_add_u32 s66, s62, 98304\n"
    "buffer_load_dwordx4 v244, s[36:39], s66 offen nt lds\n"
    "buffer_load_dwordx4 v245, s[36:39], s66 offen offset:2048 nt lds\n"
    "s_add_u32 m0, s61, 896\n"
    "s_nop 0\n"
    "buffer_load_dwordx4 v244, s[36:39], s66 offen offset:128 nt lds\n"
    "buffer_load_dwordx4 v245, s[36:39], s66 offen offset:2176 nt lds\n"
    "s_waitcnt vmcnt(40)\n"
    "s_barrier\n"
    "s_add_u32 m0, s61, 49152\n"
    "s_add_u32 s66, s62, 81920\n"
    "buffer_load_dwordx4 v246, s[40:43], s66 offen nt lds\n"
    "buffer_load_dwordx4 v246, s[40:43], s66 offen offset:2048 nt lds\n"
    "s_add_u32 m0, s61, 50048\n"
    "s_nop 0\n"
    "buffer_load_dwordx4 v246, s[40:43], s66 offen offset:128 nt lds\n"
    "buffer_load_dwordx4 v246, s[40:43], s66 offen offset:2176 nt lds\n"
    "s_waitcnt vmcnt(40)\n"
    "s_barrier\n"
    "s_mov_b32 m0, 65536\n"
    "s_add_u32 s66, s64, 98304\n"
    "buffer_load_dwordx4 v247, s[48:51], s66 offen lds\n"
    "s_mov_b32 m0, 66432\n"
    "s_nop 0\n"
    "buffer_load_dwordx4 v247, s[48:51], s66 offen offset:128 lds\n"
    "s_mov_b32 m0, 67328\n"
    "s_nop 0\n"
    "buffer_load_dwordx4 v247, s[48:51], s66 offen offset:256 lds\n"
    "s_mov_b32 m0, 68224\n"
    "s_nop 0\n"
    "buffer_load_dwordx4 v247, s[48:51], s66 offen offset:384 lds\n"
    "s_mov_b32 m0, 69120\n"
    "s_nop 0\n"
    "buffer_load_dwordx4 v247, s[48:51], s66 offen offset:512 lds\n"
    "s_mov_b32 m0, 70016\n"
    "s_nop 0\n"
    "buffer_load_dwordx4 v247, s[48:51], s66 offen offset:640 lds\n"
    "s_mov_b32 m0, 70912\n"
    "s_nop 0\n"
    "buffer_load_dwordx4 v247, s[48:51], s66 offen offset:768 lds\n"
    "s_mov_b32 m0, 71808\n"
    "s_nop 0\n"
    "buffer_load_dwordx4 v247, s[48:51], s66 offen offset:896 lds\n"
    "s_mov_b32 m0, 73728\n"
    "s_add_u32 s66, s64, 106496\n"
    "buffer_load_dwordx4 v247, s[48:51], s66 offen lds\n"
    "s_mov_b32 m0, 74624\n"
    "s_nop 0\n"
    "buffer_load_dwordx4 v247, s[48:51], s66 offen offset:128 lds\n"
    "s_mov_b32 m0, 75520\n"
    "s_nop 0\n"
    "buffer_load_dwordx4 v247, s[48:51], s66 offen offset:256 lds\n"
    "s_mov_b32 m0, 76416\n"
    "s_nop 0\n"
    "buffer_load_dwordx4 v247, s[48:51], s66 offen offset:384 lds\n"
    "s_mov_b32 m0, 77312\n"
    "s_nop 0\n"
    "buffer_load_dwordx4 v247, s[48:51], s66 offen offset:512 lds\n"
    "s_mov_b32 m0, 78208\n"
    "s_nop 0\n"
    "buffer_load_dwordx4 v247, s[48:51], s66 offen offset:640 lds\n"
    "s_mov_b32 m0, 79104\n"
    "s_nop 0\n"
    "buffer_load_dwordx4 v247, s[48:51], s66 offen offset:768 lds\n"
    "s_mov_b32 m0, 80000\n"
    "s_nop 0\n"
    "buffer_load_dwordx4 v247, s[48:51], s66 offen offset:896 lds\n"
    "s_mov_b32 m0, 131072\n"
    "s_add_u32 s67, s81, 256\n"
    "buffer_load_dwordx4 v247, s[48:51], s67 offen lds\n"
    "s_mov_b32 m0, 132096\n"
    "s_add_u32 s67, s81, 8448\n"
    "buffer_load_dwordx4 v247, s[48:51], s67 offen lds\n"
    "s_mov_b32 m0, 133120\n"
    "s_add_u32 s67, s81, 16640\n"
    "buffer_load_dwordx4 v247, s[48:51], s67 offen lds\n"
    "s_mov_b32 m0, 134144\n"
    "s_add_u32 s67, s81, 24832\n"
    "buffer_load_dwordx4 v247, s[48:51], s67 offen lds\n"
    "s_mov_b32 m0, 135168\n"
    "s_add_u32 s67, s81, 33024\n"
    "buffer_load_dwordx4 v247, s[48:51], s67 offen lds\n"
    "s_mov_b32 m0, 136192\n"
    "s_add_u32 s67, s81, 41216\n"
    "buffer_load_dwordx4 v247, s[48:51], s67 offen lds\n"
    "s_mov_b32 m0, 137216\n"
    "s_add_u32 s67, s81, 49408\n"
    "buffer_load_dwordx4 v247, s[48:51], s67 offen lds\n"
    "s_mov_b32 m0, 138240\n"
    "s_add_u32 s67, s81, 57600\n"
    "s_waitcnt vmcnt(62)\n"
    "buffer_load_dwordx4 v247, s[48:51], s67 offen lds\n"
    "s_mov_b32 m0, 139264\n"
    "s_add_u32 s67, s81, 65792\n"
    "s_waitcnt vmcnt(62)\n"
    "buffer_load_dwordx4 v247, s[48:51], s67 offen lds\n"
    "s_mov_b32 m0, 140288\n"
    "s_add_u32 s67, s81, 73984\n"
    "s_waitcnt vmcnt(62)\n"
    "buffer_load_dwordx4 v247, s[48:51], s67 offen lds\n"
    "s_mov_b32 m0, 141312\n"
    "s_add_u32 s67, s81, 82176\n"
    "s_waitcnt vmcnt(62)\n"
    "buffer_load_dwordx4 v247, s[48:51], s67 offen lds\n"
    "s_mov_b32 m0, 142336\n"
    "s_add_u32 s67, s81, 90368\n"
    "s_waitcnt vmcnt(62)\n"
    "buffer_load_dwordx4 v247, s[48:51], s67 offen lds\n"
    "s_mov_b32 m0, 143360\n"
    "s_add_u32 s67, s81, 98560\n"
    "s_waitcnt vmcnt(62)\n"
    "buffer_load_dwordx4 v247, s[48:51], s67 offen lds\n"
    "s_mov_b32 m0, 144384\n"
    "s_add_u32 s67, s81, 106752\n"
    "s_waitcnt vmcnt(62)\n"
    "buffer_load_dwordx4 v247, s[48:51], s67 offen lds\n"
    "s_mov_b32 m0, 145408\n"
    "s_add_u32 s67, s81, 114944\n"
    "s_waitcnt vmcnt(62)\n"
    "buffer_load_dwordx4 v247, s[48:51], s67 offen lds\n"
    "s_mov_b32 m0, 146432\n"
    "s_add_u32 s67, s81, 123136\n"
    "s_waitcnt vmcnt(62)\n"
    "buffer_load_dwordx4 v247, s[48:51], s67 offen lds\n"
    "s_waitcnt vmcnt(56)\n"
    "s_barrier\n"
    "s_add_u32 m0, s61, 16384\n"
    "s_add_u32 s66, s62, 114688\n"
    "buffer_load_dwordx4 v244, s[36:39], s66 offen nt lds\n"
    "buffer_load_dwordx4 v245, s[36:39], s66 offen offset:2048 nt lds\n"
    "s_add_u32 m0, s61, 17280\n"
    "s_nop 0\n"
    "buffer_load_dwordx4 v244, s[36:39], s66 offen offset:128 nt lds\n"
    "buffer_load_dwordx4 v245, s[36:39], s66 offen offset:2176 nt lds\n"
    "s_waitcnt vmcnt(40)\n"
    "s_barrier\n"
    "s_add_u32 m0, s61, 32768\n"
    "s_add_u32 s66, s62, 98304\n"
    "buffer_load_dwordx4 v246, s[40:43], s66 offen nt lds\n"
    "buffer_load_dwordx4 v246, s[40:43], s66 offen offset:2048 nt lds\n"
    "s_add_u32 m0, s61, 33664\n"
    "s_nop 0\n"
    "buffer_load_dwordx4 v246, s[40:43], s66 offen offset:128 nt lds\n"
    "buffer_load_dwordx4 v246, s[40:43], s66 offen offset:2176 nt lds\n"
    "s_waitcnt vmcnt(40)\n"
    "s_barrier\n"
    "s_mov_b32 m0, 81920\n"
    "s_add_u32 s66, s64, 114688\n"
    "buffer_load_dwordx4 v247, s[48:51], s66 offen lds\n"
    "s_mov_b32 m0, 82816\n"
    "s_nop 0\n"
    "buffer_load_dwordx4 v247, s[48:51], s66 offen offset:128 lds\n"
    "s_mov_b32 m0, 83712\n"
    "s_nop 0\n"
    "buffer_load_dwordx4 v247, s[48:51], s66 offen offset:256 lds\n"
    "s_mov_b32 m0, 84608\n"
    "s_nop 0\n"
    "buffer_load_dwordx4 v247, s[48:51], s66 offen offset:384 lds\n"
    "s_mov_b32 m0, 85504\n"
    "s_nop 0\n"
    "buffer_load_dwordx4 v247, s[48:51], s66 offen offset:512 lds\n"
    "s_mov_b32 m0, 86400\n"
    "s_nop 0\n"
    "buffer_load_dwordx4 v247, s[48:51], s66 offen offset:640 lds\n"
    "s_mov_b32 m0, 87296\n"
    "s_nop 0\n"
    "buffer_load_dwordx4 v247, s[48:51], s66 offen offset:768 lds\n"
    "s_mov_b32 m0, 88192\n"
    "s_nop 0\n"
    "buffer_load_dwordx4 v247, s[48:51], s66 offen offset:896 lds\n"
    "s_mov_b32 m0, 90112\n"
    "s_add_u32 s66, s64, 122880\n"
    "buffer_load_dwordx4 v247, s[48:51], s66 offen lds\n"
    "s_mov_b32 m0, 91008\n"
    "s_nop 0\n"
    "buffer_load_dwordx4 v247, s[48:51], s66 offen offset:128 lds\n"
    "s_mov_b32 m0, 91904\n"
    "s_nop 0\n"
    "buffer_load_dwordx4 v247, s[48:51], s66 offen offset:256 lds\n"
    "s_mov_b32 m0, 92800\n"
    "s_nop 0\n"
    "buffer_load_dwordx4 v247, s[48:51], s66 offen offset:384 lds\n"
    "s_mov_b32 m0, 93696\n"
    "s_nop 0\n"
    "buffer_load_dwordx4 v247, s[48:51], s66 offen offset:512 lds\n"
    "s_mov_b32 m0, 94592\n"
    "s_nop 0\n"
    "buffer_load_dwordx4 v247, s[48:51], s66 offen offset:640 lds\n"
    "s_mov_b32 m0, 95488\n"
    "s_nop 0\n"
    "buffer_load_dwordx4 v247, s[48:51], s66 offen offset:768 lds\n"
    "s_mov_b32 m0, 96384\n"
    "s_nop 0\n"
    "buffer_load_dwordx4 v247, s[48:51], s66 offen offset:896 lds\n"
    "s_mov_b32 m0, 147456\n"
    "s_add_u32 s67, s81, 384\n"
    "buffer_load_dwordx4 v247, s[48:51], s67 offen lds\n"
    "s_mov_b32 m0, 148480\n"
    "s_add_u32 s67, s81, 8576\n"
    "buffer_load_dwordx4 v247, s[48:51], s67 offen lds\n"
    "s_mov_b32 m0, 149504\n"
    "s_add_u32 s67, s81, 16768\n"
    "buffer_load_dwordx4 v247, s[48:51], s67 offen lds\n"
    "s_mov_b32 m0, 150528\n"
    "s_add_u32 s67, s81, 24960\n"
    "buffer_load_dwordx4 v247, s[48:51], s67 offen lds\n"
    "s_mov_b32 m0, 151552\n"
    "s_add_u32 s67, s81, 33152\n"
    "buffer_load_dwordx4 v247, s[48:51], s67 offen lds\n"
    "s_mov_b32 m0, 152576\n"
    "s_add_u32 s67, s81, 41344\n"
    "buffer_load_dwordx4 v247, s[48:51], s67 offen lds\n"
    "s_mov_b32 m0, 153600\n"
    "s_add_u32 s67, s81, 49536\n"
    "buffer_load_dwordx4 v247, s[48:51], s67 offen lds\n"
    "s_mov_b32 m0, 154624\n"
    "s_add_u32 s67, s81, 57728\n"
    "s_waitcnt vmcnt(62)\n"
    "buffer_load_dwordx4 v247, s[48:51], s67 offen lds\n"
    "s_mov_b32 m0, 155648\n"
    "s_add_u32 s67, s81, 65920\n"
    "s_waitcnt vmcnt(62)\n"
    "buffer_load_dwordx4 v247, s[48:51], s67 offen lds\n"
    "s_mov_b32 m0, 156672\n"
    "s_add_u32 s67, s81, 74112\n"
    "s_waitcnt vmcnt(62)\n"
    "buffer_load_dwordx4 v247, s[48:51], s67 offen lds\n"
    "s_mov_b32 m0, 157696\n"
    "s_add_u32 s67, s81, 82304\n"
    "s_waitcnt vmcnt(62)\n"
    "buffer_load_dwordx4 v247, s[48:51], s67 offen lds\n"
    "s_mov_b32 m0, 158720\n"
    "s_add_u32 s67, s81, 90496\n"
    "s_waitcnt vmcnt(62)\n"
    "buffer_load_dwordx4 v247, s[48:51], s67 offen lds\n"
    "s_mov_b32 m0, 159744\n"
    "s_add_u32 s67, s81, 98688\n"
    "s_waitcnt vmcnt(62)\n"
    "buffer_load_dwordx4 v247, s[48:51], s67 offen lds\n"
    "s_mov_b32 m0, 160768\n"
    "s_add_u32 s67, s81, 106880\n"
    "s_waitcnt vmcnt(62)\n"
    "buffer_load_dwordx4 v247, s[48:51], s67 offen lds\n"
    "s_mov_b32 m0, 161792\n"
    "s_add_u32 s67, s81, 115072\n"
    "s_waitcnt vmcnt(62)\n"
    "buffer_load_dwordx4 v247, s[48:51], s67 offen lds\n"
    "s_mov_b32 m0, 162816\n"
    "s_add_u32 s67, s81, 123264\n"
    "s_waitcnt vmcnt(62)\n"
    "buffer_load_dwordx4 v247, s[48:51], s67 offen lds\n"
    "s_waitcnt vmcnt(56)\n"
    "s_barrier\n"
    "s_add_u32 m0, s61, 0\n"
    "s_add_u32 s66, s63, 0\n"
    "buffer_load_dwordx4 v244, s[36:39], s66 offen nt lds\n"
    "buffer_load_dwordx4 v245, s[36:39], s66 offen offset:2048 nt lds\n"
    "s_add_u32 m0, s61, 896\n"
    "s_nop 0\n"
    "buffer_load_dwordx4 v244, s[36:39], s66 offen offset:128 nt lds\n"
    "buffer_load_dwordx4 v245, s[36:39], s66 offen offset:2176 nt lds\n"
    "s_waitcnt vmcnt(40)\n"
    "s_barrier\n"
    "s_add_u32 m0, s61, 49152\n"
    "s_add_u32 s66, s62, 114688\n"
    "buffer_load_dwordx4 v246, s[40:43], s66 offen nt lds\n"
    "buffer_load_dwordx4 v246, s[40:43], s66 offen offset:2048 nt lds\n"
    "s_add_u32 m0, s61, 50048\n"
    "s_nop 0\n"
    "buffer_load_dwordx4 v246, s[40:43], s66 offen offset:128 nt lds\n"
    "buffer_load_dwordx4 v246, s[40:43], s66 offen offset:2176 nt lds\n"
    "s_waitcnt vmcnt(40)\n"
    "s_barrier\n"
    "s_mov_b32 m0, 65536\n"
    "s_add_u32 s66, s65, 0\n"
    "buffer_load_dwordx4 v247, s[48:51], s66 offen lds\n"
    "s_mov_b32 m0, 66432\n"
    "s_nop 0\n"
    "buffer_load_dwordx4 v247, s[48:51], s66 offen offset:128 lds\n"
    "s_mov_b32 m0, 67328\n"
    "s_nop 0\n"
    "buffer_load_dwordx4 v247, s[48:51], s66 offen offset:256 lds\n"
    "s_mov_b32 m0, 68224\n"
    "s_nop 0\n"
    "buffer_load_dwordx4 v247, s[48:51], s66 offen offset:384 lds\n"
    "s_mov_b32 m0, 69120\n"
    "s_nop 0\n"
    "buffer_load_dwordx4 v247, s[48:51], s66 offen offset:512 lds\n"
    "s_mov_b32 m0, 70016\n"
    "s_nop 0\n"
    "buffer_load_dwordx4 v247, s[48:51], s66 offen offset:640 lds\n"
    "s_mov_b32 m0, 70912\n"
    "s_nop 0\n"
    "buffer_load_dwordx4 v247, s[48:51], s66 offen offset:768 lds\n"
    "s_mov_b32 m0, 71808\n"
    "s_nop 0\n"
    "buffer_load_dwordx4 v247, s[48:51], s66 offen offset:896 lds\n"
    "s_mov_b32 m0, 73728\n"
    "s_add_u32 s66, s65, 8192\n"
    "buffer_load_dwordx4 v247, s[48:51], s66 offen lds\n"
    "s_mov_b32 m0, 74624\n"
    "s_nop 0\n"
    "buffer_load_dwordx4 v247, s[48:51], s66 offen offset:128 lds\n"
    "s_mov_b32 m0, 75520\n"
    "s_nop 0\n"
    "buffer_load_dwordx4 v247, s[48:51], s66 offen offset:256 lds\n"
    "s_mov_b32 m0, 76416\n"
    "s_nop 0\n"
    "buffer_load_dwordx4 v247, s[48:51], s66 offen offset:384 lds\n"
    "s_mov_b32 m0, 77312\n"
    "s_nop 0\n"
    "buffer_load_dwordx4 v247, s[48:51], s66 offen offset:512 lds\n"
    "s_mov_b32 m0, 78208\n"
    "s_nop 0\n"
    "buffer_load_dwordx4 v247, s[48:51], s66 offen offset:640 lds\n"
    "s_mov_b32 m0, 79104\n"
    "s_nop 0\n"
    "buffer_load_dwordx4 v247, s[48:51], s66 offen offset:768 lds\n"
    "s_mov_b32 m0, 80000\n"
    "s_nop 0\n"
    "buffer_load_dwordx4 v247, s[48:51], s66 offen offset:896 lds\n"
    "s_waitcnt vmcnt(40)\n"
    "s_barrier\n"
    "s_add_u32 m0, s61, 16384\n"
    "s_add_u32 s66, s63, 16384\n"
    "buffer_load_dwordx4 v244, s[36:39], s66 offen nt lds\n"
    "buffer_load_dwordx4 v245, s[36:39], s66 offen offset:2048 nt lds\n"
    "s_add_u32 m0, s61, 17280\n"
    "s_nop 0\n"
    "buffer_load_dwordx4 v244, s[36:39], s66 offen offset:128 nt lds\n"
    "buffer_load_dwordx4 v245, s[36:39], s66 offen offset:2176 nt lds\n"
    "s_waitcnt vmcnt(24)\n"
    "s_barrier\n"
    "s_add_u32 m0, s61, 32768\n"
    "s_add_u32 s66, s63, 0\n"
    "buffer_load_dwordx4 v246, s[40:43], s66 offen nt lds\n"
    "buffer_load_dwordx4 v246, s[40:43], s66 offen offset:2048 nt lds\n"
    "s_add_u32 m0, s61, 33664\n"
    "s_nop 0\n"
    "buffer_load_dwordx4 v246, s[40:43], s66 offen offset:128 nt lds\n"
    "buffer_load_dwordx4 v246, s[40:43], s66 offen offset:2176 nt lds\n"
    "s_mov_b32 s64, s65\n"
    "s_mov_b32 s62, s63\n"
    "s_add_u32 s63, s63, 131072\n"
    "s_add_u32 s65, s65, 131072\n"
    "s_sub_u32 s68, s68, 1\n"
    "s_cmp_eq_u32 s68, 1\n"
    "s_cselect_b32 s63, 268435456, s63\n"
    "s_cselect_b32 s65, 268435456, s65\n"
    "s_add_u32 s79, s78, 3\n"
    "s_lshl_b32 s79, s79, 17\n"
    "s_lshl_b32 s67, s60, 15\n"
    "s_add_u32 s79, s79, s67\n"
    "s_waitcnt vmcnt(24)\n"
    "s_waitcnt vmcnt(0)\n"
    "s_barrier\n"
    "s_add_u32 m0, s61, 32768\n"
    "s_add_u32 s66, s79, 512\n"
    "buffer_load_dwordx4 v247, s[48:51], s66 offen lds\n"
    "s_add_u32 m0, s61, 33792\n"
    "s_add_u32 s66, s79, 8704\n"
    "buffer_load_dwordx4 v247, s[48:51], s66 offen lds\n"
    "s_add_u32 m0, s61, 34816\n"
    "s_add_u32 s66, s79, 16896\n"
    "buffer_load_dwordx4 v247, s[48:51], s66 offen lds\n"
    "s_add_u32 m0, s61, 35840\n"
    "s_add_u32 s66, s79, 25088\n"
    "buffer_load_dwordx4 v247, s[48:51], s66 offen lds\n"
    "s_add_u32 m0, s61, 65536\n"
    "s_add_u32 s66, s79, 640\n"
    "buffer_load_dwordx4 v247, s[48:51], s66 offen lds\n"
    "s_add_u32 m0, s61, 66560\n"
    "s_add_u32 s66, s79, 8832\n"
    "buffer_load_dwordx4 v247, s[48:51], s66 offen lds\n"
    "s_add_u32 m0, s61, 67584\n"
    "s_add_u32 s66, s79, 17024\n"
    "buffer_load_dwordx4 v247, s[48:51], s66 offen lds\n"
    "s_add_u32 m0, s61, 68608\n"
    "s_add_u32 s66, s79, 25216\n"
    "buffer_load_dwordx4 v247, s[48:51], s66 offen lds\n"
    "s_add_u32 m0, s61, 81920\n"
    "s_add_u32 s66, s79, 768\n"
    "buffer_load_dwordx4 v247, s[48:51], s66 offen lds\n"
    "s_add_u32 m0, s61, 82944\n"
    "s_add_u32 s66, s79, 8960\n"
    "buffer_load_dwordx4 v247, s[48:51], s66 offen lds\n"
    "s_add_u32 m0, s61, 83968\n"
    "s_add_u32 s66, s79, 17152\n"
    "buffer_load_dwordx4 v247, s[48:51], s66 offen lds\n"
    "s_add_u32 m0, s61, 84992\n"
    "s_add_u32 s66, s79, 25344\n"
    "buffer_load_dwordx4 v247, s[48:51], s66 offen lds\n"
    "s_barrier\n"
    "s_barrier\n"
    "s_add_u32 m0, s61, 49152\n"
    "s_add_u32 s66, s79, 896\n"
    "buffer_load_dwordx4 v247, s[48:51], s66 offen lds\n"
    "s_add_u32 m0, s61, 50176\n"
    "s_add_u32 s66, s79, 9088\n"
    "buffer_load_dwordx4 v247, s[48:51], s66 offen lds\n"
    "s_add_u32 m0, s61, 51200\n"
    "s_add_u32 s66, s79, 17280\n"
    "buffer_load_dwordx4 v247, s[48:51], s66 offen lds\n"
    "s_add_u32 m0, s61, 52224\n"
    "s_add_u32 s66, s79, 25472\n"
    "buffer_load_dwordx4 v247, s[48:51], s66 offen lds\n"
    "s_barrier\n"
    "s_barrier\n"
    "s_waitcnt vmcnt(12)\n"
    "s_barrier\n"
    "s_waitcnt vmcnt(8)\n"
    "s_barrier\n"
    "s_waitcnt vmcnt(4)\n"
    "s_barrier\n"
    "s_waitcnt vmcnt(0)\n"
    "s_barrier\n"
    "s_barrier\n"
    "s_endpgm\n"
      :
      : "s"(kp), "s"(vp), "s"(wq), "s"(wo), "s"(xp), "s"(pp), "s"(wid), "s"(h0), "s"(b)
      : "v0", "v1", "v2", "v3", "v4", "v5", "v6", "v7", "v8", "v9", "v10", "v11", "v12", "v13", "v14", "v15", "v16", "v17", "v18", "v19", "v20", "v21", "v22", "v23", "v24", "v25", "v26", "v27", "v28", "v29", "v30", "v31", "v32", "v33", "v34", "v35", "v36", "v37", "v38", "v39", "v40", "v41", "v42", "v43", "v44", "v45", "v46", "v47", "v48", "v49", "v50", "v51", "v52", "v53", "v54", "v55", "v56", "v57", "v58", "v59", "v60", "v61", "v62", "v63", "v64", "v65", "v66", "v67", "v68", "v69", "v70", "v71", "v72", "v73", "v74", "v75", "v76", "v77", "v78", "v79", "v80", "v81", "v82", "v83", "v84", "v85", "v86", "v87", "v88", "v89", "v90", "v91", "v92", "v93", "v94", "v95", "v96", "v97", "v98", "v99", "v100", "v101", "v102", "v103", "v104", "v105", "v106", "v107", "v108", "v109", "v110", "v111", "v112", "v113", "v114", "v115", "v116", "v117", "v118", "v119", "v120", "v121", "v122", "v123", "v124", "v125", "v126", "v127", "v128", "v129", "v130", "v131", "v132", "v133", "v134", "v135", "v136", "v137", "v138", "v139", "v140", "v141", "v142", "v143", "v144", "v145", "v146", "v147", "v148", "v149", "v150", "v151", "v152", "v153", "v154", "v155", "v156", "v157", "v158", "v159", "v160", "v161", "v162", "v163", "v164", "v165", "v166", "v167", "v168", "v169", "v170", "v171", "v172", "v173", "v174", "v175", "v176", "v177", "v178", "v179", "v180", "v181", "v182", "v183", "v184", "v185", "v186", "v187", "v188", "v189", "v190", "v191", "v192", "v193", "v194", "v195", "v196", "v197", "v198", "v199", "v200", "v201", "v202", "v203", "v204", "v205", "v206", "v207", "v208", "v209", "v210", "v211", "v212", "v213", "v214", "v215", "v216", "v217", "v218", "v219", "v220", "v221", "v222", "v223", "v224", "v225", "v226", "v227", "v228", "v229", "v230", "v231", "v232", "v233", "v234", "v235", "v236", "v237", "v238", "v239", "v240", "v241", "v242", "v243", "v244", "v245", "v246", "v247", "v248", "v249", "v250", "v251", "v252", "v253", "v254", "v255", "a0", "a1", "a2", "a3", "a4", "a5", "a6", "a7", "a8", "a9", "a10", "a11", "a12", "a13", "a14", "a15", "a16", "a17", "a18", "a19", "a20", "a21", "a22", "a23", "a24", "a25", "a26", "a27", "a28", "a29", "a30", "a31", "a32", "a33", "a34", "a35", "a36", "a37", "a38", "a39", "a40", "a41", "a42", "a43", "a44", "a45", "a46", "a47", "a48", "a49", "a50", "a51", "a52", "a53", "a54", "a55", "a56", "a57", "a58", "a59", "a60", "a61", "a62", "a63", "a64", "a65", "a66", "a67", "a68", "a69", "a70", "a71", "a72", "a73", "a74", "a75", "a76", "a77", "a78", "a79", "a80", "a81", "a82", "a83", "a84", "a85", "a86", "a87", "a88", "a89", "a90", "a91", "a92", "a93", "a94", "a95", "a96", "a97", "a98", "a99", "a100", "a101", "a102", "a103", "a104", "a105", "a106", "a107", "a108", "a109", "a110", "a111", "a112", "a113", "a114", "a115", "a116", "a117", "a118", "a119", "a120", "a121", "a122", "a123", "a124", "a125", "a126", "a127", "a128", "a129", "a130", "a131", "a132", "a133", "a134", "a135", "a136", "a137", "a138", "a139", "a140", "a141", "a142", "a143", "a144", "a145", "a146", "a147", "a148", "a149", "a150", "a151", "a152", "a153", "a154", "a155", "a156", "a157", "a158", "a159", "a160", "a161", "a162", "a163", "a164", "a165", "a166", "a167", "a168", "a169", "a170", "a171", "a172", "a173", "a174", "a175", "a176", "a177", "a178", "a179", "a180", "a181", "a182", "a183", "a184", "a185", "a186", "a187", "a188", "a189", "a190", "a191", "a192", "a193", "a194", "a195", "a196", "a197", "a198", "a199", "a200", "a201", "a202", "a203", "a204", "a205", "a206", "a207", "a208", "a209", "a210", "a211", "a212", "a213", "a214", "a215", "a216", "a217", "a218", "a219", "a220", "a221", "a222", "a223", "a224", "a225", "a226", "a227", "a228", "a229", "a230", "a231", "a232", "a233", "a234", "a235", "a236", "a237", "a238", "a239", "a240", "a241", "a242", "a243", "a244", "a245", "a246", "a247", "a248", "a249", "a250", "a251", "a252", "a253", "a254", "a255", "s36", "s37", "s38", "s39", "s40", "s41", "s42", "s43", "s44", "s45", "s46", "s47", "s48", "s49", "s50", "s51", "s52", "s53", "s54", "s55", "s56", "s57", "s58", "s59", "s60", "s61", "s62", "s63", "s64", "s65", "s66", "s67", "s68", "s69", "s70", "s71", "s72", "s73", "s74", "s75", "s76", "s77", "s78", "s79", "s80", "s81", "s82", "s83", "s84", "s85", "s86", "s87", "s88", "s89", "s90", "s91", "s92", "s93", "s94", "s95", "s96", "s97", "s98", "s99", "vcc", "scc", "memory");
}

typedef unsigned u32x4 __attribute__((ext_vector_type(4)));
typedef unsigned u32x2 __attribute__((ext_vector_type(2)));
typedef float f32x2 __attribute__((ext_vector_type(2)));
typedef __bf16 bf16x2 __attribute__((ext_vector_type(2)));

__device__ __forceinline__ unsigned pack_bf16(float lo, float hi) {
  f32x2 v = {lo, hi};
  bf16x2 r = __builtin_convertvector(v, bf16x2);
  return __builtin_bit_cast(unsigned, r);
}

__global__ __launch_bounds__(64) void reduce_kernel(const char* __restrict__ part, unsigned short* __restrict__ out) {
  __shared__ __attribute__((aligned(16))) unsigned tile_lds[32 * 36];
  const unsigned lane = threadIdx.x;
  const unsigned w = blockIdx.x;
  const unsigned np = w & 7u, j = (w >> 3) % 3u, grp = w / 24u;
  const char* p = part + (size_t)grp * 8u * kPartBytes + j * 32768u + np * 4096u + lane * 16u;
  u32x4 d[8][4];
#pragma unroll
  for (int g = 0; g < 8; ++g)
#pragma unroll
    for (int k = 0; k < 4; ++k) d[g][k] = *(const u32x4*)(p + (size_t)g * kPartBytes + k * 1024);
  const unsigned q = lane & 31u, h = lane >> 5;
#pragma unroll
  for (int k = 0; k < 4; ++k) {
    float acc[8];
#pragma unroll
    for (int i = 0; i < 8; ++i) acc[i] = 0.f;
#pragma unroll
    for (int g = 0; g < 8; ++g)
#pragma unroll
      for (int i = 0; i < 4; ++i) {
        const unsigned x = d[g][k][i];
        acc[2 * i] += __uint_as_float(x << 16);
        acc[2 * i + 1] += __uint_as_float(x & 0xffff0000u);
      }
#pragma unroll
    for (int c = 0; c < 2; ++c) {
      u32x2 v = {pack_bf16(acc[4 * c], acc[4 * c + 1]), pack_bf16(acc[4 * c + 2], acc[4 * c + 3])};
      *(u32x2*)&tile_lds[q * 36u + 16u * (k >> 1) + 8u * (k & 1) + 4u * c + 2u * h] = v;
    }
  }
  __syncthreads();
  const unsigned br = grp >> 4, b = grp & 15u;
#pragma unroll
  for (int r = 0; r < 4; ++r) {
    const unsigned row = r * 8u + (lane >> 3), c16 = lane & 7u;
    const u32x4 v = *(const u32x4*)&tile_lds[row * 36u + c16 * 4u];
    *(u32x4*)(out + (size_t)(b * 96u + j * 32u + row) * 1024u + br * 512u + np * 64u + c16 * 8u) = v;
  }
}

}

extern "C" void kernel_launch(void* const* d_in, const int* in_sizes, int n_in, void* d_out, int out_size, void* d_ws,
                              size_t ws_size, hipStream_t stream) {
  (void)in_sizes; (void)n_in; (void)out_size; (void)ws_size;
  (void)hipFuncSetAttribute((const void*)fused_attn_kernel, hipFuncAttributeMaxDynamicSharedMemorySize, kLdsBytes);
  char* part = (char*)d_ws;
  fused_attn_kernel<<<256, 256, kLdsBytes, stream>>>((const char*)d_in[0], (const char*)d_in[1], (const char*)d_in[2],
                                                      (const char*)d_in[3], (const char*)d_in[4], (const char*)d_in[5],
                                                      (const char*)d_in[6], (const char*)d_in[7], (const char*)d_in[8],
                                                      (const char*)d_in[9], part);
  reduce_kernel<<<768, 64, 0, stream>>>(part, (unsigned short*)d_out);
}
```
